```python
import torch

def _softmax(x, axis=-1):
    return torch.softmax(x, dim=axis)

def _gelu(x):
    return torch.nn.functional.gelu(x, approximate='tanh')

def _silu(x):
    return torch.nn.functional.silu(x)

def _sigmoid(x):
    return torch.sigmoid(x)

def _layernorm(x, axis=-1, eps=1e-05):
    m = x.mean(dim=axis, keepdim=True)
    v = x.var(dim=axis, keepdim=True, unbiased=False)
    return (x - m) / torch.sqrt(v + eps)

def _scatter_add(tgt, idx, src, axis=0):
    return tgt.clone().index_add_(axis, idx, src)

def _segment_sum(src, seg, n):
    out = torch.zeros((n,) + src.shape[1:], dtype=src.dtype)
    return out.index_add_(0, seg, src)

def _pad(x, pw):
    flat = tuple((v for pair in reversed(pw) for v in pair))
    return torch.nn.functional.pad(x, flat)
kernel_name = 'interleave_layers__memory_aug_recurrent_mem_tr__mixer_token_mlp__prompt'

def setup_inputs():

    _w = lambda a: (lambda t: t.to(torch.bfloat16) if t.is_floating_point() else t)(torch.as_tensor(a))

    def _A_setup():
        B, S, Dm = (1, 2048, 640)
        H, Dh = (32, 128)
        M, seg = (8, 32)
        seg = min(seg, S)
        S = S // seg * seg
        return {'x': _w((lambda *s: torch.randn(*s, dtype=torch.bfloat16) * 0.05)(B, S, Dm)), 'mem0': _w((lambda *s: torch.randn(*s, dtype=torch.bfloat16) * 0.2)(B, M, Dm)), 'Wq': _w((lambda *s: torch.randn(*s, dtype=torch.bfloat16) * 0.8)(Dm, H, Dh) * Dm ** (-0.5)), 'Wk': _w((lambda *s: torch.randn(*s, dtype=torch.bfloat16) * 0.8)(Dm, H, Dh) * Dm ** (-0.5)), 'Wv': _w((lambda *s: torch.randn(*s, dtype=torch.bfloat16) * 0.2)(Dm, H, Dh) * Dm ** (-0.5)), 'Wo': _w((lambda *s: torch.randn(*s, dtype=torch.bfloat16) * 0.2)(H, Dh, Dm) * (H * Dh) ** (-0.5)), 'seg_len': seg}
    _A_in = _A_setup()

    def _B_setup():
        B, S, D, F = (1, 2048, 640, 1728)
        return {'x': _w((lambda *s: torch.randn(*s, dtype=torch.bfloat16) * 0.05)(B, S, D)), 'w1': _w((lambda *s: torch.randn(*s, dtype=torch.bfloat16) * 0.2)(S, F) * S ** (-0.5)), 'b1': _w((lambda *s: torch.randn(*s, dtype=torch.bfloat16) * 3.2)(F) * 0.02), 'w2': _w((lambda *s: torch.randn(*s, dtype=torch.bfloat16) * 0.1)(F, S) * F ** (-0.5)), 'b2': _w((lambda *s: torch.randn(*s, dtype=torch.bfloat16) * 0.2)(S) * 0.02)}
    _B_in = _B_setup()
    return {'A_x': _A_in['x'], 'A_mem0': _A_in['mem0'], 'A_Wq': _A_in['Wq'], 'A_Wk': _A_in['Wk'], 'A_Wv': _A_in['Wv'], 'A_Wo': _A_in['Wo'], 'A_seg_len': _A_in['seg_len'], 'B_x': _B_in['x'], 'B_w1': _B_in['w1'], 'B_b1': _B_in['b1'], 'B_w2': _B_in['w2'], 'B_b2': _B_in['b2']}

def reference(A_x, A_mem0, A_Wq, A_Wk, A_Wv, A_Wo, A_seg_len, B_x, B_w1, B_b1, B_w2, B_b2):

    def _A_ref(x, mem0, Wq, Wk, Wv, Wo, seg_len):
        B, S, Dm = x.shape
        H, Dh = (Wq.shape[1], Wq.shape[2])
        M = mem0.shape[1]
        scale = Dh ** (-0.5)
        n_seg = S // seg_len
        L = M + seg_len
        idx = torch.arange(L)
        mask = (idx[:, None] < M) | (idx[:, None] >= idx[None, :])
        mem = mem0
        outs = []
        for s in range(n_seg):
            xs = x[:, s * seg_len:(s + 1) * seg_len, :].clone()
            h = torch.concatenate([mem, xs], axis=1)
            q = torch.einsum('btd,dhe->bhte', h, Wq)
            k = torch.einsum('btd,dhe->bhte', h, Wk)
            v = torch.einsum('btd,dhe->bhte', h, Wv)
            scores = torch.einsum('bhqe,bhke->bhqk', q, k) * scale
            scores = torch.where(mask[None, None], scores, -1000000000.0)
            p = _softmax(scores, axis=-1)
            o = torch.einsum('bhqk,bhke->bhqe', p, v)
            o = torch.einsum('bhte,hed->btd', o, Wo)
            mem = o[:, :M, :].clone()
            outs.append(o[:, M:, :])
        return torch.concatenate(outs, axis=1)

    def _B_ref(x, w1, b1, w2, b2):
        B, S, D = x.shape
        h = _layernorm(x, axis=-1)
        u = torch.einsum('bsd,sf->bdf', h, w1) + b1
        u = _gelu(u)
        y = torch.einsum('bdf,fs->bds', u, w2) + b2
        return (lambda x, *d: x.permute(*d))(y, 0, 2, 1)
    x = A_x
    x = x + _A_ref(_layernorm(x, axis=-1), A_mem0, A_Wq, A_Wk, A_Wv, A_Wo, A_seg_len)
    x = x + _B_ref(_layernorm(x, axis=-1), B_w1, B_b1, B_w2, B_b2)
    x = x + _A_ref(_layernorm(x, axis=-1), A_mem0, A_Wq, A_Wk, A_Wv, A_Wo, A_seg_len)
    x = x + _B_ref(_layernorm(x, axis=-1), B_w1, B_b1, B_w2, B_b2)
    return x
```

```cpp
#include <hip/hip_runtime.h>
#include <hip/hip_bf16.h>
#include <stdint.h>
namespace nv {
typedef unsigned short u16;
constexpr int T = 2048, D = 640, H = 32, E = 128, HE = 4096, M = 8, SEG = 32, NS = 64, F = 1728, L = 40;
__device__ __forceinline__ float b2f(u16 v) { return __uint_as_float((unsigned)v << 16); }
__device__ __forceinline__ u16 f2b(float f) { unsigned u = __float_as_uint(f); return (u16)((u + 0x7fffu + ((u >> 16) & 1u)) >> 16); }
__device__ __forceinline__ float rb(float f) { return b2f(f2b(f)); }
__global__ void ln(const u16* x, u16* h) {
    int row = blockIdx.x, l = threadIdx.x; float v[10], s = 0;
    for (int i = 0; i < 10; ++i) { v[i] = b2f(x[row * D + l + 64 * i]); s += v[i]; }
    for (int m = 32; m; m >>= 1) s += __shfl_xor(s, m);
    float mean = s / D, q = 0;
    for (int i = 0; i < 10; ++i) q += (v[i] - mean) * (v[i] - mean);
    for (int m = 32; m; m >>= 1) q += __shfl_xor(q, m);
    float mb = rb(mean), vb = rb(q / D), den = rb(sqrtf(rb(vb + 1e-5f)));
    for (int i = 0; i < 10; ++i) h[row * D + l + 64 * i] = f2b(rb(v[i] - mb) / den);
}
__global__ void gemm(const u16* A, long sam, long sak, const u16* B, long sbk, long sbn, u16* C, int Mm, int N, int K) {
    __shared__ float As[16][17], Bs[16][17];
    int tx = threadIdx.x & 15, ty = threadIdx.x >> 4, m0 = blockIdx.y * 16, n0 = blockIdx.x * 16; float acc = 0;
    for (int k0 = 0; k0 < K; k0 += 16) {
        As[ty][tx] = (m0 + ty < Mm) ? b2f(A[(m0 + ty) * sam + (k0 + tx) * sak]) : 0.f;
        Bs[ty][tx] = b2f(B[(k0 + ty) * sbk + (n0 + tx) * sbn]);
        __syncthreads();
        for (int k = 0; k < 16; ++k) acc += As[ty][k] * Bs[k][tx];
        __syncthreads();
    }
    if (m0 + ty < Mm) C[(long)(m0 + ty) * N + n0 + tx] = f2b(acc);
}
__global__ void attn(const u16* qm, const u16* km, const u16* vm, const u16* qx, const u16* kx, const u16* vx, u16* o, int seg0, int memq) {
    int h = blockIdx.x, s = memq ? seg0 : blockIdx.y, t = threadIdx.x;
    __shared__ float p[32][L];
    const u16* kmm = memq ? km : km + (long)s * M * HE; const u16* vmm = memq ? vm : vm + (long)s * M * HE;
    int nq = memq ? M : SEG;
    for (int idx = t; idx < nq * L; idx += blockDim.x) {
        int i = idx / L, j = idx % L;
        const u16* q = memq ? qm + i * HE + h * E : qx + (long)(s * SEG + i) * HE + h * E;
        const u16* k = j < M ? kmm + j * HE + h * E : kx + (long)(s * SEG + j - M) * HE + h * E;
        float a = 0; for (int e = 0; e < E; ++e) a += b2f(q[e]) * b2f(k[e]);
        a = rb(rb(a) * 0.08838834764831845f);
        if (!memq && j > i + M) a = rb(-1e9f);
        p[i][j] = a;
    }
    __syncthreads();
    if (t < nq) { float m = -3e38f; for (int j = 0; j < L; ++j) m = fmaxf(m, p[t][j]); float sum = 0; for (int j = 0; j < L; ++j) { p[t][j] = expf(p[t][j] - m); sum += p[t][j]; } for (int j = 0; j < L; ++j) p[t][j] = rb(p[t][j] / sum); }
    __syncthreads();
    for (int idx = t; idx < nq * E; idx += blockDim.x) {
        int i = idx / E, e = idx % E; float a = 0;
        for (int j = 0; j < L; ++j) a += p[i][j] * b2f(j < M ? vmm[j * HE + h * E + e] : vx[(long)(s * SEG + j - M) * HE + h * E + e]);
        (memq ? o + i * HE : o + (long)(s * SEG + i) * HE)[h * E + e] = f2b(a);
    }
}
__global__ void add(const u16* a, const u16* b, u16* c, int n) { int i = blockIdx.x * 256 + threadIdx.x; if (i < n) c[i] = f2b(b2f(a[i]) + b2f(b[i])); }
__global__ void gelu(u16* u, const u16* b1) { int i = blockIdx.x * 256 + threadIdx.x; if (i < D * F) { float x = rb(b2f(u[i]) + b2f(b1[i % F])); float y = 0.5f * x * (1.f + tanhf(0.7978845608028654f * (x + 0.044715f * x * x * x))); u[i] = f2b(y); } }
__global__ void addy(const u16* x, const u16* y, const u16* b2, u16* out) { int i = blockIdx.x * 256 + threadIdx.x; if (i < T * D) { int s = i / D, d = i % D; out[i] = f2b(b2f(x[i]) + rb(b2f(y[d * T + s]) + b2f(b2[s]))); } }
struct Bufs { u16 *h, *q, *k, *v, *o, *t, *x1, *x2, *qm, *km, *vm, *om, *mem, *u, *y, *h2; };
inline Bufs carve(void* ws) { u16* p = (u16*)ws; Bufs b; auto g = [&](size_t n) { u16* r = p; p += (n + 127) / 128 * 128; return r; };
    b.h = g(T * D); b.q = g((size_t)T * HE); b.k = g((size_t)T * HE); b.v = g((size_t)T * HE); b.o = g((size_t)T * HE); b.t = g(T * D); b.x1 = g(T * D); b.x2 = g(T * D);
    b.qm = g(M * HE); b.km = g(NS * M * HE); b.vm = g(NS * M * HE); b.om = g(M * HE); b.mem = g((NS + 1) * M * D); b.u = g(D * F); b.y = g(D * T); b.h2 = g(T * D); return b; }
inline void layerA(const u16* x, u16* xo, void* const* in, Bufs& b, hipStream_t st) {
    const u16 *mem0 = (const u16*)in[1], *Wq = (const u16*)in[2], *Wk = (const u16*)in[3], *Wv = (const u16*)in[4], *Wo = (const u16*)in[5];
    ln<<<T, 64, 0, st>>>(x, b.h);
    gemm<<<dim3(HE / 16, T / 16), 256, 0, st>>>(b.h, D, 1, Wq, HE, 1, b.q, T, HE, D);
    gemm<<<dim3(HE / 16, T / 16), 256, 0, st>>>(b.h, D, 1, Wk, HE, 1, b.k, T, HE, D);
    gemm<<<dim3(HE / 16, T / 16), 256, 0, st>>>(b.h, D, 1, Wv, HE, 1, b.v, T, HE, D);
    (void)hipMemcpyAsync(b.mem, mem0, M * D * 2, hipMemcpyDeviceToDevice, st);
    for (int s = 0; s < NS; ++s) {
        const u16* mem = b.mem + s * M * D;
        gemm<<<dim3(HE / 16, 1), 256, 0, st>>>(mem, D, 1, Wq, HE, 1, b.qm, M, HE, D);
        gemm<<<dim3(HE / 16, 1), 256, 0, st>>>(mem, D, 1, Wk, HE, 1, b.km + s * M * HE, M, HE, D);
        gemm<<<dim3(HE / 16, 1), 256, 0, st>>>(mem, D, 1, Wv, HE, 1, b.vm + s * M * HE, M, HE, D);
        attn<<<dim3(H, 1), 256, 0, st>>>(b.qm, b.km + s * M * HE, b.vm + s * M * HE, b.q, b.k, b.v, b.om, s, 1);
        gemm<<<dim3(D / 16, 1), 256, 0, st>>>(b.om, HE, 1, Wo, D, 1, b.mem + (s + 1) * M * D, M, D, HE);
    }
    attn<<<dim3(H, NS), 256, 0, st>>>(nullptr, b.km, b.vm, b.q, b.k, b.v, b.o, 0, 0);
    gemm<<<dim3(D / 16, T / 16), 256, 0, st>>>(b.o, HE, 1, Wo, D, 1, b.t, T, D, HE);
    add<<<T * D / 256, 256, 0, st>>>(x, b.t, xo, T * D);
}
inline void layerB(const u16* x, u16* xo, void* const* in, Bufs& b, hipStream_t st) {
    const u16 *w1 = (const u16*)in[8], *b1 = (const u16*)in[9], *w2 = (const u16*)in[10], *b2 = (const u16*)in[11];
    ln<<<T, 64, 0, st>>>(x, b.h); ln<<<T, 64, 0, st>>>(b.h, b.h2);
    gemm<<<dim3(F / 16, D / 16), 256, 0, st>>>(b.h2, 1, D, w1, F, 1, b.u, D, F, T);
    gelu<<<(D * F + 255) / 256, 256, 0, st>>>(b.u, b1);
    gemm<<<dim3(T / 16, D / 16), 256, 0, st>>>(b.u, F, 1, w2, T, 1, b.y, D, T, F);
    addy<<<T * D / 256, 256, 0, st>>>(x, b.y, b2, xo);
}
inline void run(void* const* in, void* out, void* ws, hipStream_t st) {
    Bufs b = carve(ws);
    layerA((const u16*)in[0], b.x1, in, b, st); layerB(b.x1, b.x2, in, b, st);
    layerA(b.x2, b.x1, in, b, st); layerB(b.x1, (u16*)out, in, b, st);
}
}
extern "C" void kernel_launch(void* const* d_in, const int* in_sizes, int n_in, void* d_out, int out_size, void* d_ws, size_t ws_size, hipStream_t stream) {
    nv::run(d_in, d_out, d_ws, stream);
}
```

```cpp
#include <hip/hip_runtime.h>
#include <stdint.h>
#define P_D1 352
#define P_D12 2464
#define P_LINIT 3600
#define HEAD_ASM \
    "s_mov_b64 s[36:37], %2\n" \
    "s_mov_b64 s[38:39], %3\n" \
    "s_mov_b32 s40, %4\n" \
    "s_mov_b32 s41, %5\n" \
    "s_mov_b32 s42, %6\n" \
    "s_mov_b32 s45, %7\n" \
    "s_mov_b32 s46, %8\n" \
    "s_mov_b64 s[84:85], %9\n" \
    "s_mov_b32 s79, -1\n" \
    "s_movk_i32 s44, 1200\n" \
    "s_mov_b32 s99, %10\n" \
    "s_mov_b32 s94, %11\n" \
    "s_mov_b64 exec, -1\n" \
    "s_mov_b32 s78, 0xffbfffbf\n" \
    "s_mov_b32 s68, 0x3e0293ee\n" \
    "s_mov_b32 s69, 0x3e0293ee\n" \
    "s_mov_b32 s74, 0x33333333\n" \
    "s_mov_b32 s75, 0x33333333\n" \
    "s_mov_b32 s76, 0xcccccccc\n" \
    "s_mov_b32 s77, 0xcccccccc\n" \
    "s_mov_b32 s71, 0\n" \
    "s_mov_b32 s82, 0\n" \
    "s_mov_b32 s83, 0\n" \
    "s_movk_i32 s98, 1400\n" \
    "s_mov_b32 s97, 0\n" \
    "s_mov_b32 s96, 0\n" \
    "v_mbcnt_lo_u32_b32 v100, -1, 0\n" \
    "v_mbcnt_hi_u32_b32 v100, -1, v100\n" \
    "v_lshlrev_b32 v0, 4, v100\n" \
    "v_lshrrev_b32 v101, 4, v100\n" \
    "v_and_b32 v102, 15, v100\n" \
    "v_bfe_u32 v103, v100, 2, 2\n" \
    "v_and_b32 v104, 3, v100\n" \
    "v_and_b32 v8, 2, v100\n" \
    "v_cmp_ne_u32 vcc, 0, v8\n" \
    "v_mov_b32 v8, 0x4444\n" \
    "v_mov_b32 v9, 0xeeee\n" \
    "v_cndmask_b32 v1, v8, v9, vcc\n" \
    "v_mov_b32 v13, 0\n" \
    "v_mov_b32 v14, 0\n" \
    "v_mov_b32 v196, 0x3f803f80\n" \
    "v_mov_b32 v197, 0x3f803f80\n" \
    "v_mov_b32 v198, 0x3f803f80\n" \
    "v_mov_b32 v199, 0x3f803f80\n" \
    "s_lshl_b32 s66, s41, 7\n" \
    "v_mov_b32 v10, s66\n" \
    "s_lshr_b32 s66, s41, 3\n" \
    "s_lshl_b32 s66, s66, 14\n" \
    "s_and_b32 s67, s41, 7\n" \
    "s_lshl_b32 s67, s67, 10\n" \
    "s_add_u32 s66, s66, s67\n" \
    "s_lshl_b32 s67, s42, 8\n" \
    "s_add_u32 s66, s66, s67\n" \
    "v_lshrrev_b32 v8, 1, v103\n" \
    "v_lshlrev_b32 v8, 16, v8\n" \
    "v_and_b32 v9, 1, v103\n" \
    "v_lshl_add_u32 v8, v9, 7, v8\n" \
    "v_and_b32 v9, 1, v104\n" \
    "v_lshl_add_u32 v8, v9, 6, v8\n" \
    "v_lshl_add_u32 v8, v101, 4, v8\n" \
    "v_add_u32 v2, s66, v8\n" \
    "s_lshr_b32 s66, s41, 2\n" \
    "s_lshl_b32 s66, s66, 15\n" \
    "s_and_b32 s67, s41, 3\n" \
    "s_lshl_b32 s67, s67, 11\n" \
    "s_add_u32 s66, s66, s67\n" \
    "s_lshl_b32 s67, s42, 9\n" \
    "s_add_u32 s66, s66, s67\n" \
    "v_lshl_add_u32 v3, v100, 3, s66\n" \
    "s_lshl_b32 s66, s42, 10\n" \
    "s_add_u32 s66, s66, 131072\n" \
    "v_lshlrev_b32 v8, 5, v102\n" \
    "v_lshl_add_u32 v8, v101, 3, v8\n" \
    "v_add_u32 v4, s66, v8\n" \
    "v_lshrrev_b32 v8, 1, v101\n" \
    "v_lshlrev_b32 v8, 4, v8\n" \
    "v_and_b32 v9, 1, v101\n" \
    "v_lshl_add_u32 v8, v9, 2, v8\n" \
    "v_add_u32 v8, v8, v103\n" \
    "v_lshlrev_b32 v8, 5, v8\n" \
    "v_lshl_add_u32 v8, v104, 3, v8\n" \
    "v_add_u32 v5, 131072, v8\n" \
    "v_and_b32 v8, 31, v100\n" \
    "v_lshlrev_b32 v6, 7, v8\n" \
    "v_and_b32 v8, 1, v100\n" \
    "v_lshl_add_u32 v8, v103, 1, v8\n" \
    "v_lshl_add_u32 v8, v101, 3, v8\n" \
    "s_lshl_b32 s66, s42, 5\n" \
    "v_add_lshl_u32 v7, v8, s66, 4\n" \
    "s_lshl_b32 s66, s42, 4\n" \
    "s_add_u32 s66, s66, 0\n" \
    "v_add_lshl_u32 v8, v101, s66, 13\n" \
    "v_and_b32 v9, 1, v101\n" \
    "v_or_b32 v9, 0, v9\n" \
    "v_lshrrev_b32 v240, 1, v102\n" \
    "v_xor_b32 v240, v240, v9\n" \
    "v_lshl_add_u32 v8, v240, 5, v8\n" \
    "v_and_b32 v9, 1, v100\n" \
    "v_lshl_add_u32 v240, v9, 4, v8\n" \
    "s_lshl_b32 s66, s42, 4\n" \
    "s_add_u32 s66, s66, 4\n" \
    "v_add_lshl_u32 v8, v101, s66, 13\n" \
    "v_and_b32 v9, 1, v101\n" \
    "v_or_b32 v9, 2, v9\n" \
    "v_lshrrev_b32 v241, 1, v102\n" \
    "v_xor_b32 v241, v241, v9\n" \
    "v_lshl_add_u32 v8, v241, 5, v8\n" \
    "v_and_b32 v9, 1, v100\n" \
    "v_lshl_add_u32 v241, v9, 4, v8\n" \
    "s_lshl_b32 s66, s42, 4\n" \
    "s_add_u32 s66, s66, 8\n" \
    "v_add_lshl_u32 v8, v101, s66, 13\n" \
    "v_and_b32 v9, 1, v101\n" \
    "v_or_b32 v9, 4, v9\n" \
    "v_lshrrev_b32 v242, 1, v102\n" \
    "v_xor_b32 v242, v242, v9\n" \
    "v_lshl_add_u32 v8, v242, 5, v8\n" \
    "v_and_b32 v9, 1, v100\n" \
    "v_lshl_add_u32 v242, v9, 4, v8\n" \
    "s_lshl_b32 s66, s42, 4\n" \
    "s_add_u32 s66, s66, 12\n" \
    "v_add_lshl_u32 v8, v101, s66, 13\n" \
    "v_and_b32 v9, 1, v101\n" \
    "v_or_b32 v9, 6, v9\n" \
    "v_lshrrev_b32 v243, 1, v102\n" \
    "v_xor_b32 v243, v243, v9\n" \
    "v_lshl_add_u32 v8, v243, 5, v8\n" \
    "v_and_b32 v9, 1, v100\n" \
    "v_lshl_add_u32 v243, v9, 4, v8\n" \
    "v_lshrrev_b32 v8, 1, v101\n" \
    "v_lshlrev_b32 v8, 4, v8\n" \
    "v_lshrrev_b32 v105, 1, v103\n" \
    "v_lshl_add_u32 v8, v105, 3, v8\n" \
    "v_and_b32 v106, 1, v101\n" \
    "v_lshl_add_u32 v8, v106, 2, v8\n" \
    "v_and_b32 v106, 1, v103\n" \
    "v_add_u32 v8, v8, v106\n" \
    "v_lshlrev_b32 v8, 8, v8\n" \
    "v_lshl_add_u32 v8, v104, 3, v8\n" \
    "v_and_b32 v107, 1, v101\n" \
    "v_lshl_add_u32 v107, v107, 1, v106\n" \
    "v_lshl_add_u32 v107, v105, 2, v107\n" \
    "s_lshl_b32 s66, s42, 1\n" \
    "s_or_b32 s66, s66, 0\n" \
    "v_xor_b32 v105, s66, v107\n" \
    "v_lshl_add_u32 v244, v105, 5, v8\n" \
    "v_add_u32 v246, 0x10000, v244\n" \
    "s_lshl_b32 s66, s42, 1\n" \
    "s_or_b32 s66, s66, 1\n" \
    "v_xor_b32 v105, s66, v107\n" \
    "v_lshl_add_u32 v245, v105, 5, v8\n" \
    "v_add_u32 v247, 0x10000, v245\n" \
    "s_lshl_b32 s66, s41, 8\n" \
    "s_add_u32 s66, s66, 0\n" \
    "s_mov_b64 s[80:81], %0\n" \
    "s_add_u32 s80, s80, s66\n" \
    "s_addc_u32 s81, s81, 0\n" \
    "s_lshl_b32 s66, s42, 12\n" \
    "s_add_u32 m0, s66, 0\n" \
    "s_nop 0\n" \
    "global_load_lds_dwordx4 v240, s[80:81]\n" \
    "s_add_u32 m0, s66, 1024\n" \
    "s_nop 0\n" \
    "global_load_lds_dwordx4 v241, s[80:81]\n" \
    "s_add_u32 m0, s66, 2048\n" \
    "s_nop 0\n" \
    "global_load_lds_dwordx4 v242, s[80:81]\n" \
    "s_add_u32 m0, s66, 3072\n" \
    "s_nop 0\n" \
    "global_load_lds_dwordx4 v243, s[80:81]\n" \
    "s_lshl_b32 s66, s41, 8\n" \
    "s_add_u32 s66, s66, 524288\n" \
    "s_mov_b64 s[80:81], %0\n" \
    "s_add_u32 s80, s80, s66\n" \
    "s_addc_u32 s81, s81, 0\n" \
    "s_lshl_b32 s66, s42, 12\n" \
    "s_add_u32 m0, s66, 16384\n" \
    "s_nop 0\n" \
    "global_load_lds_dwordx4 v240, s[80:81]\n" \
    "s_add_u32 m0, s66, 17408\n" \
    "s_nop 0\n" \
    "global_load_lds_dwordx4 v241, s[80:81]\n" \
    "s_add_u32 m0, s66, 18432\n" \
    "s_nop 0\n" \
    "global_load_lds_dwordx4 v242, s[80:81]\n" \
    "s_add_u32 m0, s66, 19456\n" \
    "s_nop 0\n" \
    "global_load_lds_dwordx4 v243, s[80:81]\n" \
    "s_lshl_b32 s66, s41, 8\n" \
    "s_add_u32 s66, s66, 1048576\n" \
    "s_mov_b64 s[80:81], %0\n" \
    "s_add_u32 s80, s80, s66\n" \
    "s_addc_u32 s81, s81, 0\n" \
    "s_lshl_b32 s66, s42, 12\n" \
    "s_add_u32 m0, s66, 32768\n" \
    "s_nop 0\n" \
    "global_load_lds_dwordx4 v240, s[80:81]\n" \
    "s_add_u32 m0, s66, 33792\n" \
    "s_nop 0\n" \
    "global_load_lds_dwordx4 v241, s[80:81]\n" \
    "s_add_u32 m0, s66, 34816\n" \
    "s_nop 0\n" \
    "global_load_lds_dwordx4 v242, s[80:81]\n" \
    "s_add_u32 m0, s66, 35840\n" \
    "s_nop 0\n" \
    "global_load_lds_dwordx4 v243, s[80:81]\n" \
    "s_lshl_b32 s66, s41, 8\n" \
    "s_add_u32 s66, s66, 1572864\n" \
    "s_mov_b64 s[80:81], %0\n" \
    "s_add_u32 s80, s80, s66\n" \
    "s_addc_u32 s81, s81, 0\n" \
    "s_lshl_b32 s66, s42, 12\n" \
    "s_add_u32 m0, s66, 49152\n" \
    "s_nop 0\n" \
    "global_load_lds_dwordx4 v240, s[80:81]\n" \
    "s_add_u32 m0, s66, 50176\n" \
    "s_nop 0\n" \
    "global_load_lds_dwordx4 v241, s[80:81]\n" \
    "s_add_u32 m0, s66, 51200\n" \
    "s_nop 0\n" \
    "global_load_lds_dwordx4 v242, s[80:81]\n" \
    "s_add_u32 m0, s66, 52224\n" \
    "s_nop 0\n" \
    "global_load_lds_dwordx4 v243, s[80:81]\n" \
    "s_lshl_b32 s66, s41, 8\n" \
    "s_add_u32 s66, s66, 2097152\n" \
    "s_mov_b64 s[80:81], %0\n" \
    "s_add_u32 s80, s80, s66\n" \
    "s_addc_u32 s81, s81, 0\n" \
    "s_lshl_b32 s66, s42, 12\n" \
    "s_add_u32 m0, s66, 65536\n" \
    "s_nop 0\n" \
    "global_load_lds_dwordx4 v240, s[80:81]\n" \
    "s_add_u32 m0, s66, 66560\n" \
    "s_nop 0\n" \
    "global_load_lds_dwordx4 v241, s[80:81]\n" \
    "s_add_u32 m0, s66, 67584\n" \
    "s_nop 0\n" \
    "global_load_lds_dwordx4 v242, s[80:81]\n" \
    "s_add_u32 m0, s66, 68608\n" \
    "s_nop 0\n" \
    "global_load_lds_dwordx4 v243, s[80:81]\n" \
    "s_lshl_b32 s66, s41, 8\n" \
    "s_add_u32 s66, s66, 2621440\n" \
    "s_mov_b64 s[80:81], %0\n" \
    "s_add_u32 s80, s80, s66\n" \
    "s_addc_u32 s81, s81, 0\n" \
    "s_lshl_b32 s66, s42, 12\n" \
    "s_add_u32 m0, s66, 81920\n" \
    "s_nop 0\n" \
    "global_load_lds_dwordx4 v240, s[80:81]\n" \
    "s_add_u32 m0, s66, 82944\n" \
    "s_nop 0\n" \
    "global_load_lds_dwordx4 v241, s[80:81]\n" \
    "s_add_u32 m0, s66, 83968\n" \
    "s_nop 0\n" \
    "global_load_lds_dwordx4 v242, s[80:81]\n" \
    "s_add_u32 m0, s66, 84992\n" \
    "s_nop 0\n" \
    "global_load_lds_dwordx4 v243, s[80:81]\n" \
    "s_lshl_b32 s66, s41, 8\n" \
    "s_add_u32 s66, s66, 3145728\n" \
    "s_mov_b64 s[80:81], %0\n" \
    "s_add_u32 s80, s80, s66\n" \
    "s_addc_u32 s81, s81, 0\n" \
    "s_lshl_b32 s66, s42, 12\n" \
    "s_add_u32 m0, s66, 98304\n" \
    "s_nop 0\n" \
    "global_load_lds_dwordx4 v240, s[80:81]\n" \
    "s_add_u32 m0, s66, 99328\n" \
    "s_nop 0\n" \
    "global_load_lds_dwordx4 v241, s[80:81]\n" \
    "s_add_u32 m0, s66, 100352\n" \
    "s_nop 0\n" \
    "global_load_lds_dwordx4 v242, s[80:81]\n" \
    "s_add_u32 m0, s66, 101376\n" \
    "s_nop 0\n" \
    "global_load_lds_dwordx4 v243, s[80:81]\n" \
    "s_lshl_b32 s66, s41, 8\n" \
    "s_add_u32 s66, s66, 3670016\n" \
    "s_mov_b64 s[80:81], %0\n" \
    "s_add_u32 s80, s80, s66\n" \
    "s_addc_u32 s81, s81, 0\n" \
    "s_lshl_b32 s66, s42, 12\n" \
    "s_add_u32 m0, s66, 114688\n" \
    "s_nop 0\n" \
    "global_load_lds_dwordx4 v240, s[80:81]\n" \
    "s_add_u32 m0, s66, 115712\n" \
    "s_nop 0\n" \
    "global_load_lds_dwordx4 v241, s[80:81]\n" \
    "s_add_u32 m0, s66, 116736\n" \
    "s_nop 0\n" \
    "global_load_lds_dwordx4 v242, s[80:81]\n" \
    "s_add_u32 m0, s66, 117760\n" \
    "s_nop 0\n" \
    "global_load_lds_dwordx4 v243, s[80:81]\n" \
    "s_waitcnt vmcnt(16)\n" \
    "s_barrier\n" \
    "ds_read_b64_tr_b16 a[0:1], v244 offset:0\n" \
    "ds_read_b64_tr_b16 a[2:3], v244 offset:512\n" \
    "ds_read_b64_tr_b16 a[4:5], v244 offset:8192\n" \
    "ds_read_b64_tr_b16 a[6:7], v244 offset:8704\n" \
    "ds_read_b64_tr_b16 a[8:9], v245 offset:0\n" \
    "ds_read_b64_tr_b16 a[10:11], v245 offset:512\n" \
    "ds_read_b64_tr_b16 a[12:13], v245 offset:8192\n" \
    "ds_read_b64_tr_b16 a[14:15], v245 offset:8704\n" \
    "s_waitcnt lgkmcnt(0)\n" \
    "ds_read_b64_tr_b16 a[32:33], v244 offset:16384\n" \
    "ds_read_b64_tr_b16 a[34:35], v244 offset:16896\n" \
    "ds_read_b64_tr_b16 a[36:37], v244 offset:24576\n" \
    "ds_read_b64_tr_b16 a[38:39], v244 offset:25088\n" \
    "ds_read_b64_tr_b16 a[40:41], v245 offset:16384\n" \
    "ds_read_b64_tr_b16 a[42:43], v245 offset:16896\n" \
    "ds_read_b64_tr_b16 a[44:45], v245 offset:24576\n" \
    "ds_read_b64_tr_b16 a[46:47], v245 offset:25088\n" \
    "s_waitcnt lgkmcnt(0)\n" \
    "ds_read_b64_tr_b16 a[64:65], v244 offset:32768\n" \
    "ds_read_b64_tr_b16 a[66:67], v244 offset:33280\n" \
    "ds_read_b64_tr_b16 a[68:69], v244 offset:40960\n" \
    "ds_read_b64_tr_b16 a[70:71], v244 offset:41472\n" \
    "ds_read_b64_tr_b16 a[72:73], v245 offset:32768\n" \
    "ds_read_b64_tr_b16 a[74:75], v245 offset:33280\n" \
    "ds_read_b64_tr_b16 a[76:77], v245 offset:40960\n" \
    "ds_read_b64_tr_b16 a[78:79], v245 offset:41472\n" \
    "s_waitcnt lgkmcnt(0)\n" \
    "ds_read_b64_tr_b16 a[96:97], v244 offset:49152\n" \
    "ds_read_b64_tr_b16 a[98:99], v244 offset:49664\n" \
    "ds_read_b64_tr_b16 a[100:101], v244 offset:57344\n" \
    "ds_read_b64_tr_b16 a[102:103], v244 offset:57856\n" \
    "ds_read_b64_tr_b16 a[104:105], v245 offset:49152\n" \
    "ds_read_b64_tr_b16 a[106:107], v245 offset:49664\n" \
    "ds_read_b64_tr_b16 a[108:109], v245 offset:57344\n" \
    "ds_read_b64_tr_b16 a[110:111], v245 offset:57856\n" \
    "s_waitcnt lgkmcnt(0)\n" \
    "s_barrier\n" \
    "s_lshl_b32 s66, s41, 8\n" \
    "s_add_u32 s66, s66, 4194304\n" \
    "s_mov_b64 s[80:81], %0\n" \
    "s_add_u32 s80, s80, s66\n" \
    "s_addc_u32 s81, s81, 0\n" \
    "s_lshl_b32 s66, s42, 12\n" \
    "s_add_u32 m0, s66, 0\n" \
    "s_nop 0\n" \
    "global_load_lds_dwordx4 v240, s[80:81]\n" \
    "s_add_u32 m0, s66, 1024\n" \
    "s_nop 0\n" \
    "global_load_lds_dwordx4 v241, s[80:81]\n" \
    "s_add_u32 m0, s66, 2048\n" \
    "s_nop 0\n" \
    "global_load_lds_dwordx4 v242, s[80:81]\n" \
    "s_add_u32 m0, s66, 3072\n" \
    "s_nop 0\n" \
    "global_load_lds_dwordx4 v243, s[80:81]\n" \
    "s_lshl_b32 s66, s41, 8\n" \
    "s_add_u32 s66, s66, 4718592\n" \
    "s_mov_b64 s[80:81], %0\n" \
    "s_add_u32 s80, s80, s66\n" \
    "s_addc_u32 s81, s81, 0\n" \
    "s_lshl_b32 s66, s42, 12\n" \
    "s_add_u32 m0, s66, 16384\n" \
    "s_nop 0\n" \
    "global_load_lds_dwordx4 v240, s[80:81]\n" \
    "s_add_u32 m0, s66, 17408\n" \
    "s_nop 0\n" \
    "global_load_lds_dwordx4 v241, s[80:81]\n" \
    "s_add_u32 m0, s66, 18432\n" \
    "s_nop 0\n" \
    "global_load_lds_dwordx4 v242, s[80:81]\n" \
    "s_add_u32 m0, s66, 19456\n" \
    "s_nop 0\n" \
    "global_load_lds_dwordx4 v243, s[80:81]\n" \
    "s_lshl_b32 s66, s41, 8\n" \
    "s_add_u32 s66, s66, 0\n" \
    "s_mov_b64 s[80:81], %1\n" \
    "s_add_u32 s80, s80, s66\n" \
    "s_addc_u32 s81, s81, 0\n" \
    "s_lshl_b32 s66, s42, 12\n" \
    "s_add_u32 m0, s66, 32768\n" \
    "s_nop 0\n" \
    "global_load_lds_dwordx4 v240, s[80:81]\n" \
    "s_add_u32 m0, s66, 33792\n" \
    "s_nop 0\n" \
    "global_load_lds_dwordx4 v241, s[80:81]\n" \
    "s_add_u32 m0, s66, 34816\n" \
    "s_nop 0\n" \
    "global_load_lds_dwordx4 v242, s[80:81]\n" \
    "s_add_u32 m0, s66, 35840\n" \
    "s_nop 0\n" \
    "global_load_lds_dwordx4 v243, s[80:81]\n" \
    "s_lshl_b32 s66, s41, 8\n" \
    "s_add_u32 s66, s66, 524288\n" \
    "s_mov_b64 s[80:81], %1\n" \
    "s_add_u32 s80, s80, s66\n" \
    "s_addc_u32 s81, s81, 0\n" \
    "s_lshl_b32 s66, s42, 12\n" \
    "s_add_u32 m0, s66, 49152\n" \
    "s_nop 0\n" \
    "global_load_lds_dwordx4 v240, s[80:81]\n" \
    "s_add_u32 m0, s66, 50176\n" \
    "s_nop 0\n" \
    "global_load_lds_dwordx4 v241, s[80:81]\n" \
    "s_add_u32 m0, s66, 51200\n" \
    "s_nop 0\n" \
    "global_load_lds_dwordx4 v242, s[80:81]\n" \
    "s_add_u32 m0, s66, 52224\n" \
    "s_nop 0\n" \
    "global_load_lds_dwordx4 v243, s[80:81]\n" \
    "s_waitcnt vmcnt(16)\n" \
    "s_barrier\n" \
    "ds_read_b64_tr_b16 a[128:129], v246 offset:0\n" \
    "ds_read_b64_tr_b16 a[130:131], v246 offset:512\n" \
    "ds_read_b64_tr_b16 a[132:133], v246 offset:8192\n" \
    "ds_read_b64_tr_b16 a[134:135], v246 offset:8704\n" \
    "ds_read_b64_tr_b16 a[136:137], v247 offset:0\n" \
    "ds_read_b64_tr_b16 a[138:139], v247 offset:512\n" \
    "ds_read_b64_tr_b16 a[140:141], v247 offset:8192\n" \
    "ds_read_b64_tr_b16 a[142:143], v247 offset:8704\n" \
    "s_waitcnt lgkmcnt(0)\n" \
    "ds_read_b64_tr_b16 a[160:161], v246 offset:16384\n" \
    "ds_read_b64_tr_b16 a[162:163], v246 offset:16896\n" \
    "ds_read_b64_tr_b16 a[164:165], v246 offset:24576\n" \
    "ds_read_b64_tr_b16 a[166:167], v246 offset:25088\n" \
    "ds_read_b64_tr_b16 a[168:169], v247 offset:16384\n" \
    "ds_read_b64_tr_b16 a[170:171], v247 offset:16896\n" \
    "ds_read_b64_tr_b16 a[172:173], v247 offset:24576\n" \
    "ds_read_b64_tr_b16 a[174:175], v247 offset:25088\n" \
    "s_waitcnt lgkmcnt(0)\n" \
    "ds_read_b64_tr_b16 a[192:193], v246 offset:32768\n" \
    "ds_read_b64_tr_b16 a[194:195], v246 offset:33280\n" \
    "ds_read_b64_tr_b16 a[196:197], v246 offset:40960\n" \
    "ds_read_b64_tr_b16 a[198:199], v246 offset:41472\n" \
    "ds_read_b64_tr_b16 a[200:201], v247 offset:32768\n" \
    "ds_read_b64_tr_b16 a[202:203], v247 offset:33280\n" \
    "ds_read_b64_tr_b16 a[204:205], v247 offset:40960\n" \
    "ds_read_b64_tr_b16 a[206:207], v247 offset:41472\n" \
    "s_waitcnt lgkmcnt(0)\n" \
    "ds_read_b64_tr_b16 a[224:225], v246 offset:49152\n" \
    "ds_read_b64_tr_b16 a[226:227], v246 offset:49664\n" \
    "ds_read_b64_tr_b16 a[228:229], v246 offset:57344\n" \
    "ds_read_b64_tr_b16 a[230:231], v246 offset:57856\n" \
    "ds_read_b64_tr_b16 a[232:233], v247 offset:49152\n" \
    "ds_read_b64_tr_b16 a[234:235], v247 offset:49664\n" \
    "ds_read_b64_tr_b16 a[236:237], v247 offset:57344\n" \
    "ds_read_b64_tr_b16 a[238:239], v247 offset:57856\n" \
    "s_waitcnt lgkmcnt(0)\n" \
    "s_barrier\n" \
    "s_lshl_b32 s66, s41, 8\n" \
    "s_add_u32 s66, s66, 1048576\n" \
    "s_mov_b64 s[80:81], %1\n" \
    "s_add_u32 s80, s80, s66\n" \
    "s_addc_u32 s81, s81, 0\n" \
    "s_lshl_b32 s66, s42, 12\n" \
    "s_add_u32 m0, s66, 65536\n" \
    "s_nop 0\n" \
    "global_load_lds_dwordx4 v240, s[80:81]\n" \
    "s_add_u32 m0, s66, 66560\n" \
    "s_nop 0\n" \
    "global_load_lds_dwordx4 v241, s[80:81]\n" \
    "s_add_u32 m0, s66, 67584\n" \
    "s_nop 0\n" \
    "global_load_lds_dwordx4 v242, s[80:81]\n" \
    "s_add_u32 m0, s66, 68608\n" \
    "s_nop 0\n" \
    "global_load_lds_dwordx4 v243, s[80:81]\n" \
    "s_lshl_b32 s66, s41, 8\n" \
    "s_add_u32 s66, s66, 1572864\n" \
    "s_mov_b64 s[80:81], %1\n" \
    "s_add_u32 s80, s80, s66\n" \
    "s_addc_u32 s81, s81, 0\n" \
    "s_lshl_b32 s66, s42, 12\n" \
    "s_add_u32 m0, s66, 81920\n" \
    "s_nop 0\n" \
    "global_load_lds_dwordx4 v240, s[80:81]\n" \
    "s_add_u32 m0, s66, 82944\n" \
    "s_nop 0\n" \
    "global_load_lds_dwordx4 v241, s[80:81]\n" \
    "s_add_u32 m0, s66, 83968\n" \
    "s_nop 0\n" \
    "global_load_lds_dwordx4 v242, s[80:81]\n" \
    "s_add_u32 m0, s66, 84992\n" \
    "s_nop 0\n" \
    "global_load_lds_dwordx4 v243, s[80:81]\n" \
    "s_lshl_b32 s66, s41, 8\n" \
    "s_add_u32 s66, s66, 2097152\n" \
    "s_mov_b64 s[80:81], %1\n" \
    "s_add_u32 s80, s80, s66\n" \
    "s_addc_u32 s81, s81, 0\n" \
    "s_lshl_b32 s66, s42, 12\n" \
    "s_add_u32 m0, s66, 98304\n" \
    "s_nop 0\n" \
    "global_load_lds_dwordx4 v240, s[80:81]\n" \
    "s_add_u32 m0, s66, 99328\n" \
    "s_nop 0\n" \
    "global_load_lds_dwordx4 v241, s[80:81]\n" \
    "s_add_u32 m0, s66, 100352\n" \
    "s_nop 0\n" \
    "global_load_lds_dwordx4 v242, s[80:81]\n" \
    "s_add_u32 m0, s66, 101376\n" \
    "s_nop 0\n" \
    "global_load_lds_dwordx4 v243, s[80:81]\n" \
    "s_lshl_b32 s66, s41, 8\n" \
    "s_add_u32 s66, s66, 2621440\n" \
    "s_mov_b64 s[80:81], %1\n" \
    "s_add_u32 s80, s80, s66\n" \
    "s_addc_u32 s81, s81, 0\n" \
    "s_lshl_b32 s66, s42, 12\n" \
    "s_add_u32 m0, s66, 114688\n" \
    "s_nop 0\n" \
    "global_load_lds_dwordx4 v240, s[80:81]\n" \
    "s_add_u32 m0, s66, 115712\n" \
    "s_nop 0\n" \
    "global_load_lds_dwordx4 v241, s[80:81]\n" \
    "s_add_u32 m0, s66, 116736\n" \
    "s_nop 0\n" \
    "global_load_lds_dwordx4 v242, s[80:81]\n" \
    "s_add_u32 m0, s66, 117760\n" \
    "s_nop 0\n" \
    "global_load_lds_dwordx4 v243, s[80:81]\n" \
    "s_waitcnt vmcnt(16)\n" \
    "s_barrier\n" \
    "ds_read_b64_tr_b16 v[16:17], v244 offset:0\n" \
    "ds_read_b64_tr_b16 v[18:19], v244 offset:512\n" \
    "ds_read_b64_tr_b16 v[20:21], v244 offset:8192\n" \
    "ds_read_b64_tr_b16 v[22:23], v244 offset:8704\n" \
    "ds_read_b64_tr_b16 v[24:25], v245 offset:0\n" \
    "ds_read_b64_tr_b16 v[26:27], v245 offset:512\n" \
    "ds_read_b64_tr_b16 v[28:29], v245 offset:8192\n" \
    "ds_read_b64_tr_b16 v[30:31], v245 offset:8704\n" \
    "s_waitcnt lgkmcnt(0)\n" \
    "ds_read_b64_tr_b16 v[48:49], v244 offset:16384\n" \
    "ds_read_b64_tr_b16 v[50:51], v244 offset:16896\n" \
    "ds_read_b64_tr_b16 v[52:53], v244 offset:24576\n" \
    "ds_read_b64_tr_b16 v[54:55], v244 offset:25088\n" \
    "ds_read_b64_tr_b16 v[56:57], v245 offset:16384\n" \
    "ds_read_b64_tr_b16 v[58:59], v245 offset:16896\n" \
    "ds_read_b64_tr_b16 v[60:61], v245 offset:24576\n" \
    "ds_read_b64_tr_b16 v[62:63], v245 offset:25088\n" \
    "s_waitcnt lgkmcnt(0)\n" \
    "ds_read_b64_tr_b16 a[16:17], v244 offset:32768\n" \
    "ds_read_b64_tr_b16 a[18:19], v244 offset:33280\n" \
    "ds_read_b64_tr_b16 a[20:21], v244 offset:40960\n" \
    "ds_read_b64_tr_b16 a[22:23], v244 offset:41472\n" \
    "ds_read_b64_tr_b16 a[24:25], v245 offset:32768\n" \
    "ds_read_b64_tr_b16 a[26:27], v245 offset:33280\n" \
    "ds_read_b64_tr_b16 a[28:29], v245 offset:40960\n" \
    "ds_read_b64_tr_b16 a[30:31], v245 offset:41472\n" \
    "s_waitcnt lgkmcnt(0)\n" \
    "ds_read_b64_tr_b16 a[48:49], v244 offset:49152\n" \
    "ds_read_b64_tr_b16 a[50:51], v244 offset:49664\n" \
    "ds_read_b64_tr_b16 a[52:53], v244 offset:57344\n" \
    "ds_read_b64_tr_b16 a[54:55], v244 offset:57856\n" \
    "ds_read_b64_tr_b16 a[56:57], v245 offset:49152\n" \
    "ds_read_b64_tr_b16 a[58:59], v245 offset:49664\n" \
    "ds_read_b64_tr_b16 a[60:61], v245 offset:57344\n" \
    "ds_read_b64_tr_b16 a[62:63], v245 offset:57856\n" \
    "s_waitcnt lgkmcnt(0)\n" \
    "s_barrier\n" \
    "s_lshl_b32 s66, s41, 8\n" \
    "s_add_u32 s66, s66, 3145728\n" \
    "s_mov_b64 s[80:81], %1\n" \
    "s_add_u32 s80, s80, s66\n" \
    "s_addc_u32 s81, s81, 0\n" \
    "s_lshl_b32 s66, s42, 12\n" \
    "s_add_u32 m0, s66, 0\n" \
    "s_nop 0\n" \
    "global_load_lds_dwordx4 v240, s[80:81]\n" \
    "s_add_u32 m0, s66, 1024\n" \
    "s_nop 0\n" \
    "global_load_lds_dwordx4 v241, s[80:81]\n" \
    "s_add_u32 m0, s66, 2048\n" \
    "s_nop 0\n" \
    "global_load_lds_dwordx4 v242, s[80:81]\n" \
    "s_add_u32 m0, s66, 3072\n" \
    "s_nop 0\n" \
    "global_load_lds_dwordx4 v243, s[80:81]\n" \
    "s_lshl_b32 s66, s41, 8\n" \
    "s_add_u32 s66, s66, 3670016\n" \
    "s_mov_b64 s[80:81], %1\n" \
    "s_add_u32 s80, s80, s66\n" \
    "s_addc_u32 s81, s81, 0\n" \
    "s_lshl_b32 s66, s42, 12\n" \
    "s_add_u32 m0, s66, 16384\n" \
    "s_nop 0\n" \
    "global_load_lds_dwordx4 v240, s[80:81]\n" \
    "s_add_u32 m0, s66, 17408\n" \
    "s_nop 0\n" \
    "global_load_lds_dwordx4 v241, s[80:81]\n" \
    "s_add_u32 m0, s66, 18432\n" \
    "s_nop 0\n" \
    "global_load_lds_dwordx4 v242, s[80:81]\n" \
    "s_add_u32 m0, s66, 19456\n" \
    "s_nop 0\n" \
    "global_load_lds_dwordx4 v243, s[80:81]\n" \
    "s_lshl_b32 s66, s41, 8\n" \
    "s_add_u32 s66, s66, 4194304\n" \
    "s_mov_b64 s[80:81], %1\n" \
    "s_add_u32 s80, s80, s66\n" \
    "s_addc_u32 s81, s81, 0\n" \
    "s_lshl_b32 s66, s42, 12\n" \
    "s_add_u32 m0, s66, 32768\n" \
    "s_nop 0\n" \
    "global_load_lds_dwordx4 v240, s[80:81]\n" \
    "s_add_u32 m0, s66, 33792\n" \
    "s_nop 0\n" \
    "global_load_lds_dwordx4 v241, s[80:81]\n" \
    "s_add_u32 m0, s66, 34816\n" \
    "s_nop 0\n" \
    "global_load_lds_dwordx4 v242, s[80:81]\n" \
    "s_add_u32 m0, s66, 35840\n" \
    "s_nop 0\n" \
    "global_load_lds_dwordx4 v243, s[80:81]\n" \
    "s_lshl_b32 s66, s41, 8\n" \
    "s_add_u32 s66, s66, 4718592\n" \
    "s_mov_b64 s[80:81], %1\n" \
    "s_add_u32 s80, s80, s66\n" \
    "s_addc_u32 s81, s81, 0\n" \
    "s_lshl_b32 s66, s42, 12\n" \
    "s_add_u32 m0, s66, 49152\n" \
    "s_nop 0\n" \
    "global_load_lds_dwordx4 v240, s[80:81]\n" \
    "s_add_u32 m0, s66, 50176\n" \
    "s_nop 0\n" \
    "global_load_lds_dwordx4 v241, s[80:81]\n" \
    "s_add_u32 m0, s66, 51200\n" \
    "s_nop 0\n" \
    "global_load_lds_dwordx4 v242, s[80:81]\n" \
    "s_add_u32 m0, s66, 52224\n" \
    "s_nop 0\n" \
    "global_load_lds_dwordx4 v243, s[80:81]\n" \
    "s_waitcnt vmcnt(16)\n" \
    "s_barrier\n" \
    "ds_read_b64_tr_b16 a[80:81], v246 offset:0\n" \
    "ds_read_b64_tr_b16 a[82:83], v246 offset:512\n" \
    "ds_read_b64_tr_b16 a[84:85], v246 offset:8192\n" \
    "ds_read_b64_tr_b16 a[86:87], v246 offset:8704\n" \
    "ds_read_b64_tr_b16 a[88:89], v247 offset:0\n" \
    "ds_read_b64_tr_b16 a[90:91], v247 offset:512\n" \
    "ds_read_b64_tr_b16 a[92:93], v247 offset:8192\n" \
    "ds_read_b64_tr_b16 a[94:95], v247 offset:8704\n" \
    "s_waitcnt lgkmcnt(0)\n" \
    "ds_read_b64_tr_b16 a[112:113], v246 offset:16384\n" \
    "ds_read_b64_tr_b16 a[114:115], v246 offset:16896\n" \
    "ds_read_b64_tr_b16 a[116:117], v246 offset:24576\n" \
    "ds_read_b64_tr_b16 a[118:119], v246 offset:25088\n" \
    "ds_read_b64_tr_b16 a[120:121], v247 offset:16384\n" \
    "ds_read_b64_tr_b16 a[122:123], v247 offset:16896\n" \
    "ds_read_b64_tr_b16 a[124:125], v247 offset:24576\n" \
    "ds_read_b64_tr_b16 a[126:127], v247 offset:25088\n" \
    "s_waitcnt lgkmcnt(0)\n" \
    "ds_read_b64_tr_b16 a[144:145], v246 offset:32768\n" \
    "ds_read_b64_tr_b16 a[146:147], v246 offset:33280\n" \
    "ds_read_b64_tr_b16 a[148:149], v246 offset:40960\n" \
    "ds_read_b64_tr_b16 a[150:151], v246 offset:41472\n" \
    "ds_read_b64_tr_b16 a[152:153], v247 offset:32768\n" \
    "ds_read_b64_tr_b16 a[154:155], v247 offset:33280\n" \
    "ds_read_b64_tr_b16 a[156:157], v247 offset:40960\n" \
    "ds_read_b64_tr_b16 a[158:159], v247 offset:41472\n" \
    "s_waitcnt lgkmcnt(0)\n" \
    "ds_read_b64_tr_b16 a[176:177], v246 offset:49152\n" \
    "ds_read_b64_tr_b16 a[178:179], v246 offset:49664\n" \
    "ds_read_b64_tr_b16 a[180:181], v246 offset:57344\n" \
    "ds_read_b64_tr_b16 a[182:183], v246 offset:57856\n" \
    "ds_read_b64_tr_b16 a[184:185], v247 offset:49152\n" \
    "ds_read_b64_tr_b16 a[186:187], v247 offset:49664\n" \
    "ds_read_b64_tr_b16 a[188:189], v247 offset:57344\n" \
    "ds_read_b64_tr_b16 a[190:191], v247 offset:57856\n" \
    "s_waitcnt lgkmcnt(0)\n" \
    "s_barrier\n" \
    "s_waitcnt vmcnt(0)\n" \
    "s_barrier\n" \
    "ds_read_b64_tr_b16 a[208:209], v244 offset:0\n" \
    "ds_read_b64_tr_b16 a[210:211], v244 offset:512\n" \
    "ds_read_b64_tr_b16 a[212:213], v244 offset:8192\n" \
    "ds_read_b64_tr_b16 a[214:215], v244 offset:8704\n" \
    "ds_read_b64_tr_b16 a[216:217], v245 offset:0\n" \
    "ds_read_b64_tr_b16 a[218:219], v245 offset:512\n" \
    "ds_read_b64_tr_b16 a[220:221], v245 offset:8192\n" \
    "ds_read_b64_tr_b16 a[222:223], v245 offset:8704\n" \
    "s_waitcnt lgkmcnt(0)\n" \
    "ds_read_b64_tr_b16 a[240:241], v244 offset:16384\n" \
    "ds_read_b64_tr_b16 a[242:243], v244 offset:16896\n" \
    "ds_read_b64_tr_b16 a[244:245], v244 offset:24576\n" \
    "ds_read_b64_tr_b16 a[246:247], v244 offset:25088\n" \
    "ds_read_b64_tr_b16 a[248:249], v245 offset:16384\n" \
    "ds_read_b64_tr_b16 a[250:251], v245 offset:16896\n" \
    "ds_read_b64_tr_b16 a[252:253], v245 offset:24576\n" \
    "ds_read_b64_tr_b16 a[254:255], v245 offset:25088\n" \
    "s_waitcnt lgkmcnt(0)\n" \
    "ds_read_b64_tr_b16 v[32:33], v244 offset:32768\n" \
    "ds_read_b64_tr_b16 v[34:35], v244 offset:33280\n" \
    "ds_read_b64_tr_b16 v[36:37], v244 offset:40960\n" \
    "ds_read_b64_tr_b16 v[38:39], v244 offset:41472\n" \
    "ds_read_b64_tr_b16 v[40:41], v245 offset:32768\n" \
    "ds_read_b64_tr_b16 v[42:43], v245 offset:33280\n" \
    "ds_read_b64_tr_b16 v[44:45], v245 offset:40960\n" \
    "ds_read_b64_tr_b16 v[46:47], v245 offset:41472\n" \
    "s_waitcnt lgkmcnt(0)\n" \
    "ds_read_b64_tr_b16 v[64:65], v244 offset:49152\n" \
    "ds_read_b64_tr_b16 v[66:67], v244 offset:49664\n" \
    "ds_read_b64_tr_b16 v[68:69], v244 offset:57344\n" \
    "ds_read_b64_tr_b16 v[70:71], v244 offset:57856\n" \
    "ds_read_b64_tr_b16 v[72:73], v245 offset:49152\n" \
    "ds_read_b64_tr_b16 v[74:75], v245 offset:49664\n" \
    "ds_read_b64_tr_b16 v[76:77], v245 offset:57344\n" \
    "ds_read_b64_tr_b16 v[78:79], v245 offset:57856\n" \
    "s_waitcnt lgkmcnt(0)\n" \
    "s_barrier\n" \
    "s_mov_b32 s43, 0\n" \
    "s_lshl_b32 s66, s43, 5\n" \
    "s_add_u32 s66, s66, s41\n" \
    "s_lshl_b32 s66, s66, 4\n" \
    "s_add_u32 s66, s66, 67174400\n" \
    "v_mov_b32 v210, s66\n" \
    "global_load_dwordx2 v[208:209], v210, s[38:39] sc1\n" \
    "s_waitcnt vmcnt(0)\n" \
    "s_mov_b32 s70, 0\n" \
    "HF0C%=:\n" \
    "v_min_u32 v208, v208, v209\n" \
    "v_cmp_gt_u32 vcc, s94, v208\n" \
    "s_cbranch_vccz HF0D%=\n" \
    "global_load_dwordx2 v[208:209], v210, s[38:39] sc1\n" \
    "s_waitcnt vmcnt(0)\n" \
    "s_sleep 2\n" \
    "s_add_u32 s70, s70, 1\n" \
    "s_or_b32 s66, s70, s71\n" \
    "s_cmp_lt_u32 s66, 65536\n" \
    "s_cbranch_scc1 HF0C%=\n" \
    "s_mov_b32 s71, 65536\n" \
    "HF0D%=:\n" \
    "s_mov_b32 s81, 1\n" \
    "s_lshl_b32 s66, s81, 5\n" \
    "s_add_u32 s66, s66, s41\n" \
    "s_lshl_b32 s66, s66, 4\n" \
    "s_add_u32 s66, s66, 67174400\n" \
    "v_mov_b32 v210, s66\n" \
    "global_load_dwordx2 v[208:209], v210, s[38:39] sc1\n" \
    "s_waitcnt vmcnt(0)\n" \
    "s_mov_b32 s70, 0\n" \
    "HF1C%=:\n" \
    "v_min_u32 v208, v208, v209\n" \
    "v_cmp_gt_u32 vcc, s94, v208\n" \
    "s_cbranch_vccz HF1D%=\n" \
    "global_load_dwordx2 v[208:209], v210, s[38:39] sc1\n" \
    "s_waitcnt vmcnt(0)\n" \
    "s_sleep 2\n" \
    "s_add_u32 s70, s70, 1\n" \
    "s_or_b32 s66, s70, s71\n" \
    "s_cmp_lt_u32 s66, 65536\n" \
    "s_cbranch_scc1 HF1C%=\n" \
    "s_mov_b32 s71, 65536\n" \
    "HF1D%=:\n" \
    "s_mov_b32 s81, 2\n" \
    "s_lshl_b32 s66, s81, 5\n" \
    "s_add_u32 s66, s66, s41\n" \
    "s_lshl_b32 s66, s66, 4\n" \
    "s_add_u32 s66, s66, 67174400\n" \
    "v_mov_b32 v210, s66\n" \
    "global_load_dwordx2 v[208:209], v210, s[38:39] sc1\n" \
    "s_lshr_b32 s66, s43, 1\n" \
    "s_lshr_b32 s66, s40, s66\n" \
    "s_and_b32 s66, s66, 1\n" \
    "s_lshl_b32 s66, s66, 13\n" \
    "s_lshl_b32 s67, s43, 20\n" \
    "s_add_u32 s66, s66, s67\n" \
    "s_lshl_b32 s67, s41, 15\n" \
    "s_add_u32 s66, s66, s67\n" \
    "s_lshl_b32 s67, s42, 11\n" \
    "s_add_u32 s67, s67, s66\n" \
    "s_add_u32 s60, s38, s67\n" \
    "s_addc_u32 s61, s39, 0\n" \
    "s_add_u32 s62, s60, 16384\n" \
    "s_addc_u32 s63, s61, 0\n" \
    "s_lshl_b32 m0, s42, 11\n" \
    "global_load_dwordx4 v[240:243], v0, s[62:63] offset:0 sc1\n" \
    "global_load_dwordx4 v[244:247], v0, s[62:63] offset:1024 sc1\n" \
    "global_load_lds_dwordx4 v0, s[60:61] offset:0 sc1\n" \
    "global_load_lds_dwordx4 v0, s[60:61] offset:1024 sc1\n" \
    "s_and_b32 s66, s43, 3\n" \
    "s_lshl_b32 s67, s66, 16\n" \
    "s_add_u32 s67, s67, 544768\n" \
    "s_sub_u32 s67, s67, s99\n" \
    "s_add_u32 s50, s36, s67\n" \
    "s_addc_u32 s51, s37, 0\n" \
    "s_add_u32 s52, s50, 28672\n" \
    "s_addc_u32 s53, s51, 0\n" \
    "s_and_b32 s67, s43, 1\n" \
    "s_lshl_b32 s67, s67, 18\n" \
    "s_add_u32 s67, s67, 1048576\n" \
    "s_add_u32 s67, s67, s99\n" \
    "s_add_u32 s88, s36, s67\n" \
    "s_addc_u32 s89, s37, 0\n" \
    "s_lshl_b32 s67, s66, 17\n" \
    "s_add_u32 s54, s36, s67\n" \
    "s_addc_u32 s55, s37, 0\n" \
    "s_xor_b32 s67, s67, 262144\n" \
    "s_add_u32 s56, s36, s67\n" \
    "s_addc_u32 s57, s37, 0\n" \
    "s_lshr_b32 s67, s43, 1\n" \
    "s_lshl_b32 s67, s67, 14\n" \
    "s_and_b32 s66, s43, 1\n" \
    "s_lshl_b32 s66, s66, 12\n" \
    "s_add_u32 s67, s67, s66\n" \
    "s_add_u32 s67, s67, 1572864\n" \
    "s_add_u32 s58, s36, s67\n" \
    "s_addc_u32 s59, s37, 0\n" \
    "s_lshr_b32 s66, s43, 1\n" \
    "s_lshr_b32 s66, s40, s66\n" \
    "s_and_b32 s66, s66, 1\n" \
    "s_lshl_b32 s66, s66, 13\n" \
    "s_lshl_b32 s67, s43, 20\n" \
    "s_add_u32 s66, s66, s67\n" \
    "s_lshl_b32 s67, s41, 15\n" \
    "s_add_u32 s66, s66, s67\n" \
    "s_xor_b32 s66, s66, 8192\n" \
    "s_add_u32 s66, s66, 16384\n" \
    "s_add_u32 s64, s38, s66\n" \
    "s_addc_u32 s65, s39, 0\n" \
    "v_mov_b64 v[120:121], 0\n" \
    "v_mov_b64 v[122:123], 0\n" \
    "v_mov_b64 v[124:125], 0\n" \
    "v_mov_b64 v[126:127], 0\n" \
    "v_mov_b64 v[128:129], 0\n" \
    "v_mov_b64 v[130:131], 0\n" \
    "v_mov_b64 v[132:133], 0\n" \
    "v_mov_b64 v[134:135], 0\n" \
    "s_mov_b32 s70, 1\n" \
    "s_lshr_b32 s66, s70, 1\n" \
    "s_lshr_b32 s66, s40, s66\n" \
    "s_and_b32 s66, s66, 1\n" \
    "s_lshl_b32 s66, s66, 13\n" \
    "s_lshl_b32 s67, s70, 20\n" \
    "s_add_u32 s66, s66, s67\n" \
    "s_lshl_b32 s67, s41, 15\n" \
    "s_add_u32 s66, s66, s67\n" \
    "s_lshl_b32 s67, s42, 11\n" \
    "s_add_u32 s67, s67, s66\n" \
    "s_add_u32 s60, s38, s67\n" \
    "s_addc_u32 s61, s39, 0\n" \
    "s_add_u32 s62, s60, 16384\n" \
    "s_addc_u32 s63, s61, 0\n" \
    "s_waitcnt vmcnt(0)\n" \
    "s_barrier\n" \
    "ds_read_b128 v[136:139], v0 offset:0\n" \
    "ds_read_b128 v[140:143], v0 offset:2048\n" \
    "ds_read_b128 v[144:147], v0 offset:4096\n" \
    "ds_read_b128 v[148:151], v0 offset:6144\n" \
    "ds_read_b128 v[152:155], v0 offset:1024\n" \
    "ds_read_b128 v[156:159], v0 offset:3072\n" \
    "ds_read_b128 v[160:163], v0 offset:5120\n" \
    "ds_read_b128 v[164:167], v0 offset:7168\n" \
    "v_mov_b64 v[168:169], v[240:241]\n" \
    "v_mov_b64 v[170:171], v[242:243]\n" \
    "v_mov_b64 v[172:173], v[244:245]\n" \
    "v_mov_b64 v[174:175], v[246:247]\n" \
    "s_waitcnt lgkmcnt(0)\n" \
    "s_barrier\n" \
    "s_memrealtime s[48:49]\n" \
    "s_waitcnt lgkmcnt(0)\n" \
    "s_lshl_b32 s47, s48, 5\n" \
    "s_sub_u32 s47, s47, 320000\n" \
    "HSTEP%=:\n" \
    "s_mov_b32 s95, 0\n" \
    "s_mov_b64 s[86:87], -1\n" \
    "s_mov_b32 s80, s78\n" \
    "s_sub_u32 s66, s47, 192\n" \
    "s_lshr_b32 s66, s66, 5\n" \
    "s_memrealtime s[28:29]\n" \
    "HWAIT%=:\n" \
    "s_memrealtime s[30:31]\n" \
    "s_waitcnt lgkmcnt(1)\n" \
    "s_sub_u32 s67, s28, s66\n" \
    "s_bitcmp0_b32 s67, 26\n" \
    "s_cbranch_scc1 HWAIT%=X\n" \
    "s_memrealtime s[28:29]\n" \
    "s_waitcnt lgkmcnt(1)\n" \
    "s_sub_u32 s67, s30, s66\n" \
    "s_bitcmp0_b32 s67, 26\n" \
    "s_cbranch_scc1 HWAIT%=X\n" \
    "s_memrealtime s[30:31]\n" \
    "s_waitcnt lgkmcnt(1)\n" \
    "s_sub_u32 s67, s28, s66\n" \
    "s_bitcmp0_b32 s67, 26\n" \
    "s_cbranch_scc1 HWAIT%=X\n" \
    "s_memrealtime s[28:29]\n" \
    "s_waitcnt lgkmcnt(1)\n" \
    "s_sub_u32 s67, s30, s66\n" \
    "s_bitcmp0_b32 s67, 26\n" \
    "s_cbranch_scc1 HWAIT%=X\n" \
    "s_memrealtime s[30:31]\n" \
    "s_waitcnt lgkmcnt(1)\n" \
    "s_sub_u32 s67, s28, s66\n" \
    "s_bitcmp0_b32 s67, 26\n" \
    "s_cbranch_scc1 HWAIT%=X\n" \
    "s_memrealtime s[28:29]\n" \
    "s_waitcnt lgkmcnt(1)\n" \
    "s_sub_u32 s67, s30, s66\n" \
    "s_bitcmp1_b32 s67, 26\n" \
    "s_cbranch_scc1 HWAIT%=\n" \
    "HWAIT%=X:\n" \
    "s_add_u32 s67, s67, s66\n" \
    "s_lshl_b32 s67, s67, 5\n" \
    "s_sub_u32 s67, s47, s67\n" \
    "s_sub_i32 s67, s67, 96\n" \
    "s_sub_u32 s81, s47, s67\n" \
    "s_add_u32 s81, s81, 96\n" \
    "s_cmp_lt_i32 s67, 0\n" \
    "s_cselect_b32 s81, s81, s47\n" \
    "s_max_i32 s67, s67, 0\n" \
    "s_mul_i32 s67, s67, 3\n" \
    "s_lshr_b32 s67, s67, 2\n" \
    "s_and_b32 s67, s67, 0x7c\n" \
    "s_sub_u32 s67, 140, s67\n" \
    "s_waitcnt lgkmcnt(0)\n" \
    "s_getpc_b64 s[28:29]\n" \
    "s_add_u32 s28, s28, s67\n" \
    "s_addc_u32 s29, s29, 0\n" \
    "s_setpc_b64 s[28:29]\n" \
    "s_nop 0\n" \
    "s_nop 0\n" \
    "s_nop 0\n" \
    "s_nop 0\n" \
    "s_nop 0\n" \
    "s_nop 0\n" \
    "s_nop 0\n" \
    "s_nop 0\n" \
    "s_nop 0\n" \
    "s_nop 0\n" \
    "s_nop 0\n" \
    "s_nop 0\n" \
    "s_nop 0\n" \
    "s_nop 0\n" \
    "s_nop 0\n" \
    "s_nop 0\n" \
    "s_nop 0\n" \
    "s_nop 0\n" \
    "s_nop 0\n" \
    "s_nop 0\n" \
    "s_nop 0\n" \
    "s_nop 0\n" \
    "s_nop 0\n" \
    "s_nop 0\n" \
    "s_nop 0\n" \
    "s_nop 0\n" \
    "s_nop 0\n" \
    "s_nop 0\n" \
    "s_nop 0\n" \
    "s_nop 0\n" \
    "s_nop 0\n" \
    "s_nop 0\n" \
    "s_memtime s[32:33]\n" \
    "HSHOT%=:\n" \
    "global_load_dwordx4 v[80:83], v0, s[50:51] offset:-4096 sc1\n" \
    "s_nop 15\n" \
    "global_load_dwordx4 v[84:87], v0, s[50:51] offset:-3072 sc1\n" \
    "s_nop 15\n" \
    "global_load_dwordx4 v[88:91], v0, s[50:51] offset:-2048 sc1\n" \
    "s_nop 15\n" \
    "global_load_dwordx4 v[92:95], v0, s[50:51] offset:-1024 sc1\n" \
    "s_nop 15\n" \
    "global_load_dwordx4 v[96:99], v0, s[50:51] offset:0 sc1\n" \
    "s_nop 15\n" \
    "global_load_dwordx4 v[100:103], v0, s[50:51] offset:1024 sc1\n" \
    "s_nop 15\n" \
    "global_load_dwordx4 v[104:107], v0, s[50:51] offset:2048 sc1\n" \
    "s_waitcnt vmcnt(6)\n" \
    "global_load_dwordx4 v[108:111], v0, s[50:51] offset:3072 sc1\n" \
    "v_smfmac_f32_16x16x64_bf16 v[120:123], v[80:83], a[0:7], v1\n" \
    "v_smfmac_f32_16x16x64_bf16 v[124:127], v[80:83], a[8:15], v1\n" \
    "v_smfmac_f32_16x16x64_bf16 v[128:131], v[80:83], a[16:23], v1\n" \
    "v_smfmac_f32_16x16x64_bf16 v[132:135], v[80:83], a[24:31], v1\n" \
    "s_waitcnt vmcnt(6)\n" \
    "global_load_dwordx4 v[112:115], v0, s[52:53] offset:0 sc1\n" \
    "v_smfmac_f32_16x16x64_bf16 v[120:123], v[84:87], a[32:39], v1\n" \
    "v_smfmac_f32_16x16x64_bf16 v[124:127], v[84:87], a[40:47], v1\n" \
    "v_smfmac_f32_16x16x64_bf16 v[128:131], v[84:87], a[48:55], v1\n" \
    "v_smfmac_f32_16x16x64_bf16 v[132:135], v[84:87], a[56:63], v1\n" \
    "s_waitcnt vmcnt(6)\n" \
    "global_load_dwordx4 v[116:119], v0, s[52:53] offset:1024 sc1\n" \
    "v_smfmac_f32_16x16x64_bf16 v[120:123], v[88:91], a[64:71], v1\n" \
    "v_smfmac_f32_16x16x64_bf16 v[124:127], v[88:91], a[72:79], v1\n" \
    "v_smfmac_f32_16x16x64_bf16 v[128:131], v[88:91], a[80:87], v1\n" \
    "v_smfmac_f32_16x16x64_bf16 v[132:135], v[88:91], a[88:95], v1\n" \
    "s_waitcnt vmcnt(6)\n" \
    "v_smfmac_f32_16x16x64_bf16 v[120:123], v[92:95], a[96:103], v1\n" \
    "v_smfmac_f32_16x16x64_bf16 v[124:127], v[92:95], a[104:111], v1\n" \
    "v_smfmac_f32_16x16x64_bf16 v[128:131], v[92:95], a[112:119], v1\n" \
    "v_smfmac_f32_16x16x64_bf16 v[132:135], v[92:95], a[120:127], v1\n" \
    "s_waitcnt vmcnt(5)\n" \
    "v_smfmac_f32_16x16x64_bf16 v[120:123], v[96:99], a[128:135], v1\n" \
    "v_smfmac_f32_16x16x64_bf16 v[124:127], v[96:99], a[136:143], v1\n" \
    "v_smfmac_f32_16x16x64_bf16 v[128:131], v[96:99], a[144:151], v1\n" \
    "v_smfmac_f32_16x16x64_bf16 v[132:135], v[96:99], a[152:159], v1\n" \
    "s_waitcnt vmcnt(4)\n" \
    "v_smfmac_f32_16x16x64_bf16 v[120:123], v[100:103], a[160:167], v1\n" \
    "v_smfmac_f32_16x16x64_bf16 v[124:127], v[100:103], a[168:175], v1\n" \
    "v_smfmac_f32_16x16x64_bf16 v[128:131], v[100:103], a[176:183], v1\n" \
    "v_smfmac_f32_16x16x64_bf16 v[132:135], v[100:103], a[184:191], v1\n" \
    "s_waitcnt vmcnt(3)\n" \
    "v_smfmac_f32_16x16x64_bf16 v[120:123], v[104:107], a[192:199], v1\n" \
    "v_smfmac_f32_16x16x64_bf16 v[124:127], v[104:107], a[200:207], v1\n" \
    "v_smfmac_f32_16x16x64_bf16 v[128:131], v[104:107], a[208:215], v1\n" \
    "v_smfmac_f32_16x16x64_bf16 v[132:135], v[104:107], a[216:223], v1\n" \
    "s_waitcnt vmcnt(2)\n" \
    "v_smfmac_f32_16x16x64_bf16 v[120:123], v[108:111], a[224:231], v1\n" \
    "v_smfmac_f32_16x16x64_bf16 v[124:127], v[108:111], a[232:239], v1\n" \
    "v_smfmac_f32_16x16x64_bf16 v[128:131], v[108:111], a[240:247], v1\n" \
    "v_smfmac_f32_16x16x64_bf16 v[132:135], v[108:111], a[248:255], v1\n" \
    "s_waitcnt vmcnt(1)\n" \
    "v_smfmac_f32_16x16x64_bf16 v[120:123], v[112:115], v[16:23], v1\n" \
    "v_smfmac_f32_16x16x64_bf16 v[124:127], v[112:115], v[24:31], v1\n" \
    "v_smfmac_f32_16x16x64_bf16 v[128:131], v[112:115], v[32:39], v1\n" \
    "v_smfmac_f32_16x16x64_bf16 v[132:135], v[112:115], v[40:47], v1\n" \
    "s_waitcnt vmcnt(0)\n" \
    "s_memtime s[34:35]\n" \
    "v_smfmac_f32_16x16x64_bf16 v[120:123], v[116:119], v[48:55], v1\n" \
    "v_smfmac_f32_16x16x64_bf16 v[128:131], v[116:119], v[64:71], v1\n" \
    "v_smfmac_f32_16x16x64_bf16 v[124:127], v[116:119], v[56:63], v1\n" \
    "v_smfmac_f32_16x16x64_bf16 v[132:135], v[116:119], v[72:79], v1\n" \
    "s_nop 4\n" \
    "v_pk_add_f32 v[120:121], v[120:121], v[122:123]\n" \
    "v_pk_add_f32 v[128:129], v[128:129], v[130:131]\n" \
    "v_cvt_pk_bf16_f32 v122, v120, v121\n" \
    "v_pk_add_f32 v[124:125], v[124:125], v[126:127]\n" \
    "v_cvt_pk_bf16_f32 v123, v128, v129\n" \
    "v_pk_add_f32 v[132:133], v[132:133], v[134:135]\n" \
    "ds_write_b64 v4, v[122:123]\n" \
    "v_cvt_pk_bf16_f32 v126, v124, v125\n" \
    "v_cvt_pk_bf16_f32 v127, v132, v133\n" \
    "v_cmp_u_f32 vcc, v120, v121\n" \
    "ds_write_b64 v4, v[126:127] offset:512\n" \
    "s_sub_u32 s66, s34, s32\n" \
    "s_and_b64 vcc, vcc, s[86:87]\n" \
    "s_cbranch_vccnz HSLOW%=\n" \
    "s_cmp_lt_u32 s66, s44\n" \
    "s_cbranch_scc1 HGATE%=\n" \
    "HBACK%=:\n" \
    "s_waitcnt lgkmcnt(0)\n" \
    "s_barrier\n" \
    "ds_read_b64_tr_b16 v[176:177], v5 offset:0\n" \
    "ds_read_b64_tr_b16 v[178:179], v5 offset:256\n" \
    "ds_read_b64_tr_b16 v[180:181], v5 offset:1024\n" \
    "ds_read_b64_tr_b16 v[182:183], v5 offset:1280\n" \
    "ds_read_b64_tr_b16 v[184:185], v5 offset:2048\n" \
    "ds_read_b64_tr_b16 v[186:187], v5 offset:2304\n" \
    "ds_read_b64_tr_b16 v[188:189], v5 offset:3072\n" \
    "ds_read_b64_tr_b16 v[190:191], v5 offset:3328\n" \
    "s_waitcnt lgkmcnt(6)\n" \
    "v_mfma_f32_16x16x32_bf16 v[128:131], v[176:179], v[176:179], 0\n" \
    "v_mfma_f32_16x16x32_bf16 v[120:123], v[136:139], v[176:179], 0\n" \
    "v_mfma_f32_16x16x32_bf16 v[124:127], v[152:155], v[176:179], 0\n" \
    "s_waitcnt lgkmcnt(4)\n" \
    "v_mfma_f32_16x16x32_bf16 v[128:131], v[180:183], v[180:183], v[128:131]\n" \
    "v_mfma_f32_16x16x32_bf16 v[120:123], v[140:143], v[180:183], v[120:123]\n" \
    "v_mfma_f32_16x16x32_bf16 v[124:127], v[156:159], v[180:183], v[124:127]\n" \
    "global_load_dwordx2 v[192:193], v3, s[88:89] sc1\n" \
    "s_waitcnt lgkmcnt(2)\n" \
    "v_mfma_f32_16x16x32_bf16 v[128:131], v[184:187], v[184:187], v[128:131]\n" \
    "v_mfma_f32_16x16x32_bf16 v[120:123], v[144:147], v[184:187], v[120:123]\n" \
    "v_mfma_f32_16x16x32_bf16 v[124:127], v[160:163], v[184:187], v[124:127]\n" \
    "s_waitcnt lgkmcnt(0)\n" \
    "v_mfma_f32_16x16x32_bf16 v[128:131], v[188:191], v[188:191], v[128:131]\n" \
    "v_mfma_f32_16x16x32_bf16 v[120:123], v[148:151], v[188:191], v[120:123]\n" \
    "v_mfma_f32_16x16x32_bf16 v[124:127], v[164:167], v[188:191], v[124:127]\n" \
    "global_load_dwordx4 v[240:243], v0, s[62:63] offset:0 sc1\n" \
    "global_load_dwordx4 v[244:247], v0, s[62:63] offset:1024 sc1\n" \
    "s_nop 4\n" \
    "v_max3_f32 v200, v130, v131, v120\n" \
    "v_max3_f32 v201, v121, v122, v123\n" \
    "v_max3_f32 v200, v200, v124, v125\n" \
    "v_max3_f32 v201, v201, v126, v127\n" \
    "v_max_f32 v200, v200, v201\n" \
    "v_mov_b32 v201, v200\n" \
    "global_load_lds_dwordx4 v0, s[60:61] offset:0 sc1\n" \
    "s_nop 0\n" \
    "v_permlane32_swap_b32 v201, v200\n" \
    "v_max_f32 v200, v200, v201\n" \
    "v_mov_b32 v201, v200\n" \
    "global_load_lds_dwordx4 v0, s[60:61] offset:1024 sc1\n" \
    "s_nop 0\n" \
    "v_permlane16_swap_b32 v201, v200\n" \
    "v_max_f32 v200, v200, v201\n" \
    "v_mul_f32 v200, s68, v200\n" \
    "v_pk_fma_f32 v[120:121], v[120:121], s[68:69], v[200:201] op_sel_hi:[1,1,0] neg_lo:[0,0,1] neg_hi:[0,0,1]\n" \
    "v_pk_fma_f32 v[122:123], v[122:123], s[68:69], v[200:201] op_sel_hi:[1,1,0] neg_lo:[0,0,1] neg_hi:[0,0,1]\n" \
    "v_pk_fma_f32 v[124:125], v[124:125], s[68:69], v[200:201] op_sel_hi:[1,1,0] neg_lo:[0,0,1] neg_hi:[0,0,1]\n" \
    "v_pk_fma_f32 v[126:127], v[126:127], s[68:69], v[200:201] op_sel_hi:[1,1,0] neg_lo:[0,0,1] neg_hi:[0,0,1]\n" \
    "v_pk_fma_f32 v[130:131], v[130:131], s[68:69], v[200:201] op_sel_hi:[1,1,0] neg_lo:[0,0,1] neg_hi:[0,0,1]\n" \
    "v_exp_f32 v120, v120\n" \
    "v_exp_f32 v121, v121\n" \
    "v_exp_f32 v122, v122\n" \
    "v_exp_f32 v123, v123\n" \
    "v_exp_f32 v124, v124\n" \
    "v_exp_f32 v125, v125\n" \
    "v_exp_f32 v126, v126\n" \
    "v_exp_f32 v127, v127\n" \
    "v_cvt_pk_bf16_f32 v204, v120, v121\n" \
    "v_cvt_pk_bf16_f32 v205, v122, v123\n" \
    "v_cvt_pk_bf16_f32 v206, v124, v125\n" \
    "v_cvt_pk_bf16_f32 v207, v126, v127\n" \
    "v_exp_f32 v130, v130\n" \
    "v_exp_f32 v131, v131\n" \
    "v_mfma_f32_16x16x32_bf16 v[236:239], v[196:199], v[204:207], 0\n" \
    "v_cvt_pk_bf16_f32 v12, v130, v131\n" \
    "v_mfma_f32_16x16x32_bf16 v[228:231], v[168:171], v[204:207], 0\n" \
    "v_cvt_pk_bf16_f32 v15, v130, v131\n" \
    "v_mfma_f32_16x16x32_bf16 v[232:235], v[172:175], v[204:207], 0\n" \
    "s_waitcnt vmcnt(4)\n" \
    "HVCONT%=:\n" \
    "v_mfma_f32_16x16x16_bf16 v[220:223], v[196:197], v[12:13], v[236:239]\n" \
    "v_max_u32 v201, v192, v193\n" \
    "v_mfma_f32_16x16x16_bf16 v[212:215], v[192:193], v[12:13], v[228:231]\n" \
    "v_cmp_le_u32 vcc, s80, v201\n" \
    "s_nop 0\n" \
    "v_mfma_f32_16x16x16_bf16 v[216:219], v[192:193], v[14:15], v[232:235]\n" \
    "s_mov_b64 exec, s[74:75]\n" \
    "s_nop 1\n" \
    "v_rcp_f32 v220, v220\n" \
    "s_cbranch_vccnz HVSLOW%=\n" \
    "v_pk_mul_f32 v[212:213], v[212:213], v[220:221] op_sel_hi:[1,0]\n" \
    "v_pk_mul_f32 v[214:215], v[214:215], v[220:221] op_sel_hi:[1,0]\n" \
    "s_nop 0\n" \
    "v_pk_mul_f32 v[216:217], v[216:217], v[220:221] op_sel_hi:[1,0]\n" \
    "v_pk_mul_f32 v[218:219], v[218:219], v[220:221] op_sel_hi:[1,0]\n" \
    "v_cvt_pk_bf16_f32 v204, v212, v213\n" \
    "v_cvt_pk_bf16_f32 v205, v214, v215\n" \
    "v_cvt_pk_bf16_f32 v206, v216, v217\n" \
    "v_cvt_pk_bf16_f32 v207, v218, v219\n" \
    "global_store_dwordx4 v2, v[204:207], s[54:55] sc1\n" \
    "s_memtime s[48:49]\n" \
    "s_mov_b64 exec, -1\n" \
    "v_mov_b32 v212, s78\n" \
    "v_mov_b32 v213, s78\n" \
    "v_mov_b32 v214, s78\n" \
    "v_mov_b32 v215, s78\n" \
    "s_waitcnt lgkmcnt(0)\n" \
    "s_sub_u32 s66, s48, s32\n" \
    "s_mul_i32 s66, s66, s98\n" \
    "s_lshr_b32 s66, s66, 10\n" \
    "s_add_u32 s66, s66, s81\n" \
    "s_mov_b32 s72, s66\n" \
    "s_cmp_lg_u32 s42, 0\n" \
    "s_cbranch_scc1 HNTS%=\n" \
    "s_add_u32 s67, s43, 1\n" \
    "v_mov_b32 v8, s66\n" \
    "v_mov_b32 v9, s67\n" \
    "s_mov_b64 exec, 1\n" \
    "global_store_dwordx2 v10, v[8:9], s[58:59] sc1\n" \
    "s_mov_b64 exec, -1\n" \
    "HNTS%=:\n" \
    "s_sub_u32 s66, s32, s97\n" \
    "s_mul_i32 s66, s66, s98\n" \
    "s_lshr_b32 s66, s66, 10\n" \
    "s_sub_u32 s67, s81, s96\n" \
    "s_sub_i32 s66, s67, s66\n" \
    "s_ashr_i32 s66, s66, 3\n" \
    "s_abs_i32 s67, s66\n" \
    "s_cmp_lt_u32 s67, 100\n" \
    "s_cselect_b32 s66, s66, 0\n" \
    "s_cmp_eq_u32 s81, s47\n" \
    "s_cselect_b32 s66, s66, 0\n" \
    "s_cselect_b32 s96, s81, 0\n" \
    "s_add_i32 s98, s98, s66\n" \
    "s_mov_b32 s97, s32\n" \
    "s_mov_b64 exec, s[74:75]\n" \
    "global_store_dwordx4 v2, v[212:215], s[56:57] sc1\n" \
    "s_mov_b64 exec, s[76:77]\n" \
    "s_cmp_eq_u32 s42, 0\n" \
    "s_cbranch_scc0 HKM0%=\n" \
    "global_store_dwordx4 v7, v[176:179], s[64:65] sc1\n" \
    "HKM0%=:\n" \
    "s_cmp_eq_u32 s42, 1\n" \
    "s_cbranch_scc0 HKM1%=\n" \
    "global_store_dwordx4 v7, v[180:183], s[64:65] sc1\n" \
    "HKM1%=:\n" \
    "s_cmp_eq_u32 s42, 2\n" \
    "s_cbranch_scc0 HKM2%=\n" \
    "global_store_dwordx4 v7, v[184:187], s[64:65] sc1\n" \
    "HKM2%=:\n" \
    "s_cmp_eq_u32 s42, 3\n" \
    "s_cbranch_scc0 HKM3%=\n" \
    "global_store_dwordx4 v7, v[188:191], s[64:65] sc1\n" \
    "HKM3%=:\n" \
    "s_mov_b64 exec, -1\n" \
    "s_add_u32 s43, s43, 1\n" \
    "s_cmp_ge_u32 s43, 64\n" \
    "s_cbranch_scc1 HEND%=\n" \
    "s_mov_b64 s[100:101], s[58:59]\n" \
    "s_add_u32 s81, s47, s46\n" \
    "s_add_u32 s81, s81, 448\n" \
    "s_lshr_b32 s81, s81, 5\n" \
    "HTW%=:\n" \
    "s_memrealtime s[28:29]\n" \
    "s_waitcnt lgkmcnt(0)\n" \
    "s_sub_u32 s66, s28, s81\n" \
    "s_bitcmp1_b32 s66, 26\n" \
    "s_cbranch_scc1 HTW%=\n" \
    "global_load_dwordx2 v[224:225], v6, s[100:101] sc1\n" \
    "s_mov_b32 s70, 0\n" \
    "HF2C%=:\n" \
    "v_min_u32 v208, v208, v209\n" \
    "v_cmp_gt_u32 vcc, s94, v208\n" \
    "s_cbranch_vccz HF2D%=\n" \
    "global_load_dwordx2 v[208:209], v210, s[38:39] sc1\n" \
    "s_waitcnt vmcnt(0)\n" \
    "s_sleep 2\n" \
    "s_add_u32 s70, s70, 1\n" \
    "s_or_b32 s66, s70, s71\n" \
    "s_cmp_lt_u32 s66, 65536\n" \
    "s_cbranch_scc1 HF2C%=\n" \
    "s_mov_b32 s71, 65536\n" \
    "HF2D%=:\n" \
    "s_add_u32 s70, s43, 2\n" \
    "s_min_u32 s70, s70, 63\n" \
    "s_lshl_b32 s66, s70, 5\n" \
    "s_add_u32 s66, s66, s41\n" \
    "s_lshl_b32 s66, s66, 4\n" \
    "s_add_u32 s66, s66, 67174400\n" \
    "v_mov_b32 v210, s66\n" \
    "global_load_dwordx2 v[208:209], v210, s[38:39] sc1\n" \
    "s_add_u32 s70, s43, 1\n" \
    "s_min_u32 s70, s70, 63\n" \
    "s_lshr_b32 s66, s70, 1\n" \
    "s_lshr_b32 s66, s40, s66\n" \
    "s_and_b32 s66, s66, 1\n" \
    "s_lshl_b32 s66, s66, 13\n" \
    "s_lshl_b32 s67, s70, 20\n" \
    "s_add_u32 s66, s66, s67\n" \
    "s_lshl_b32 s67, s41, 15\n" \
    "s_add_u32 s66, s66, s67\n" \
    "s_lshl_b32 s67, s42, 11\n" \
    "s_add_u32 s67, s67, s66\n" \
    "s_add_u32 s60, s38, s67\n" \
    "s_addc_u32 s61, s39, 0\n" \
    "s_add_u32 s62, s60, 16384\n" \
    "s_addc_u32 s63, s61, 0\n" \
    "s_and_b32 s66, s43, 3\n" \
    "s_lshl_b32 s67, s66, 16\n" \
    "s_add_u32 s67, s67, 544768\n" \
    "s_sub_u32 s67, s67, s99\n" \
    "s_add_u32 s50, s36, s67\n" \
    "s_addc_u32 s51, s37, 0\n" \
    "s_add_u32 s52, s50, 28672\n" \
    "s_addc_u32 s53, s51, 0\n" \
    "s_and_b32 s67, s43, 1\n" \
    "s_lshl_b32 s67, s67, 18\n" \
    "s_add_u32 s67, s67, 1048576\n" \
    "s_add_u32 s67, s67, s99\n" \
    "s_add_u32 s88, s36, s67\n" \
    "s_addc_u32 s89, s37, 0\n" \
    "s_lshl_b32 s67, s66, 17\n" \
    "s_add_u32 s54, s36, s67\n" \
    "s_addc_u32 s55, s37, 0\n" \
    "s_xor_b32 s67, s67, 262144\n" \
    "s_add_u32 s56, s36, s67\n" \
    "s_addc_u32 s57, s37, 0\n" \
    "s_lshr_b32 s67, s43, 1\n" \
    "s_lshl_b32 s67, s67, 14\n" \
    "s_and_b32 s66, s43, 1\n" \
    "s_lshl_b32 s66, s66, 12\n" \
    "s_add_u32 s67, s67, s66\n" \
    "s_add_u32 s67, s67, 1572864\n" \
    "s_add_u32 s58, s36, s67\n" \
    "s_addc_u32 s59, s37, 0\n" \
    "s_lshr_b32 s66, s43, 1\n" \
    "s_lshr_b32 s66, s40, s66\n" \
    "s_and_b32 s66, s66, 1\n" \
    "s_lshl_b32 s66, s66, 13\n" \
    "s_lshl_b32 s67, s43, 20\n" \
    "s_add_u32 s66, s66, s67\n" \
    "s_lshl_b32 s67, s41, 15\n" \
    "s_add_u32 s66, s66, s67\n" \
    "s_xor_b32 s66, s66, 8192\n" \
    "s_add_u32 s66, s66, 16384\n" \
    "s_add_u32 s64, s38, s66\n" \
    "s_addc_u32 s65, s39, 0\n" \
    "v_mov_b64 v[120:121], 0\n" \
    "v_mov_b64 v[122:123], 0\n" \
    "v_mov_b64 v[124:125], 0\n" \
    "v_mov_b64 v[126:127], 0\n" \
    "v_mov_b64 v[128:129], 0\n" \
    "v_mov_b64 v[130:131], 0\n" \
    "v_mov_b64 v[132:133], 0\n" \
    "v_mov_b64 v[134:135], 0\n" \
    "s_waitcnt vmcnt(5)\n" \
    "s_barrier\n" \
    "ds_read_b128 v[136:139], v0 offset:0\n" \
    "ds_read_b128 v[140:143], v0 offset:2048\n" \
    "ds_read_b128 v[144:147], v0 offset:4096\n" \
    "ds_read_b128 v[148:151], v0 offset:6144\n" \
    "ds_read_b128 v[152:155], v0 offset:1024\n" \
    "ds_read_b128 v[156:159], v0 offset:3072\n" \
    "ds_read_b128 v[160:163], v0 offset:5120\n" \
    "ds_read_b128 v[164:167], v0 offset:7168\n" \
    "v_mov_b64 v[168:169], v[240:241]\n" \
    "v_mov_b64 v[170:171], v[242:243]\n" \
    "v_mov_b64 v[172:173], v[244:245]\n" \
    "v_mov_b64 v[174:175], v[246:247]\n" \
    "s_waitcnt vmcnt(1) lgkmcnt(0)\n" \
    "s_mov_b32 s70, 0\n" \
    "s_add_u32 s67, s43, 0\n" \
    "s_branch HTSC%=\n" \
    "HTSL%=:\n" \
    "global_load_dwordx2 v[224:225], v6, s[100:101] sc1\n" \
    "s_waitcnt vmcnt(0)\n" \
    "HTSC%=:\n" \
    "v_cmp_ne_u32 vcc, s67, v225\n" \
    "v_subrev_u32 v224, s47, v224\n" \
    "s_cbranch_vccz HTSD%=\n" \
    "s_sleep 2\n" \
    "s_add_u32 s70, s70, 1\n" \
    "s_or_b32 s66, s70, s71\n" \
    "s_cmp_lt_u32 s66, 65536\n" \
    "s_cbranch_scc1 HTSL%=\n" \
    "s_mov_b32 s71, 65536\n" \
    "v_mov_b32 v224, 0\n" \
    "HTSD%=:\n" \
    "s_nop 1\n" \
    "v_max_i32_dpp v224, v224, v224 row_ror:1 row_mask:0xf bank_mask:0xf\n" \
    "s_nop 1\n" \
    "v_max_i32_dpp v224, v224, v224 row_ror:2 row_mask:0xf bank_mask:0xf\n" \
    "s_nop 1\n" \
    "v_max_i32_dpp v224, v224, v224 row_ror:4 row_mask:0xf bank_mask:0xf\n" \
    "s_nop 1\n" \
    "v_max_i32_dpp v224, v224, v224 row_ror:8 row_mask:0xf bank_mask:0xf\n" \
    "v_mov_b32 v225, v224\n" \
    "s_nop 1\n" \
    "v_permlane16_swap_b32 v225, v224\n" \
    "v_max_i32 v224, v225, v224\n" \
    "s_nop 1\n" \
    "v_readfirstlane_b32 s66, v224\n" \
    "s_add_u32 s66, s66, s47\n" \
    "s_add_u32 s73, s47, s46\n" \
    "s_sub_i32 s67, s66, s73\n" \
    "s_lshl_b32 s79, s79, 1\n" \
    "s_cmp_lt_i32 s67, 96\n" \
    "s_cbranch_scc1 PL0A%=\n" \
    "s_cmp_eq_u32 s43, 1\n" \
    "s_movk_i32 s70, 2240\n" \
    "s_cselect_b32 s70, 3840, s70\n" \
    "s_add_u32 s70, s70, s66\n" \
    "s_sub_u32 s70, s70, s73\n" \
    "s_max_i32 s70, s70, 3840\n" \
    "s_add_u32 s73, s73, s70\n" \
    "s_add_u32 s73, s73, 960\n" \
    "s_or_b32 s79, s79, 1\n" \
    "s_cmp_le_i32 s67, 256\n" \
    "s_cbranch_scc1 PL0A%=\n" \
    "s_bitcmp0_b32 s79, 1\n" \
    "s_cselect_b32 s70, 32, 0\n" \
    "s_add_u32 s45, s45, s70\n" \
    "s_branch PL0B%=\n" \
    "PL0A%=:\n" \
    "s_sub_u32 s66, s66, s47\n" \
    "s_cmp_lt_u32 s43, 6\n" \
    "s_cselect_b32 s67, 63, 4\n" \
    "s_lshl_b32 s67, s67, 2\n" \
    "s_sub_u32 s46, s46, s67\n" \
    "s_max_u32 s46, s46, s66\n" \
    "PL0B%=:\n" \
    "s_add_u32 s47, s73, s45\n" \
    "s_or_b32 s66, s41, s42\n" \
    "s_cmp_lg_u32 s66, 0\n" \
    "s_cbranch_scc1 HSTEP%=\n" \
    "s_add_u32 s66, s47, s46\n" \
    "s_lshl_b32 s67, s43, 7\n" \
    "s_add_u32 s67, s67, 786432\n" \
    "v_mov_b32 v200, s67\n" \
    "s_add_u32 s67, s43, 1\n" \
    "v_mov_b32 v8, s66\n" \
    "s_lshl_b32 s66, s45, 8\n" \
    "s_or_b32 s67, s67, s66\n" \
    "v_mov_b32 v9, s67\n" \
    "s_mov_b64 exec, 1\n" \
    "global_store_dwordx2 v200, v[8:9], s[36:37] sc1\n" \
    "s_mov_b64 exec, -1\n" \
    "s_branch HSTEP%=\n" \
    "HGATE%=:\n" \
    "s_memtime s[28:29]\n" \
    "s_waitcnt lgkmcnt(0)\n" \
    "s_sub_u32 s66, s28, s32\n" \
    "s_sub_u32 s66, s66, 130\n" \
    "s_cmp_lt_u32 s66, s44\n" \
    "s_cbranch_scc1 HGATE%=\n" \
    "s_branch HBACK%=\n" \
    "HSLOW%=:\n" \
    "s_add_u32 s82, s82, 1\n" \
    "s_add_u32 s95, s95, 1\n" \
    "s_cmp_gt_u32 s95, 6\n" \
    "s_cbranch_scc1 HSLW2%=\n" \
    "s_waitcnt lgkmcnt(0)\n" \
    "v_mov_b64 v[120:121], 0\n" \
    "v_mov_b64 v[122:123], 0\n" \
    "v_mov_b64 v[124:125], 0\n" \
    "v_mov_b64 v[126:127], 0\n" \
    "v_mov_b64 v[128:129], 0\n" \
    "v_mov_b64 v[130:131], 0\n" \
    "v_mov_b64 v[132:133], 0\n" \
    "v_mov_b64 v[134:135], 0\n" \
    "s_branch HSHOT%=\n" \
    "HSLW2%=:\n" \
    "s_mov_b32 s70, 0\n" \
    "HMP%=:\n" \
    "global_load_dwordx4 v[80:83], v0, s[50:51] offset:-4096 sc1\n" \
    "global_load_dwordx4 v[84:87], v0, s[50:51] offset:-3072 sc1\n" \
    "global_load_dwordx4 v[88:91], v0, s[50:51] offset:-2048 sc1\n" \
    "global_load_dwordx4 v[92:95], v0, s[50:51] offset:-1024 sc1\n" \
    "global_load_dwordx4 v[96:99], v0, s[50:51] offset:0 sc1\n" \
    "global_load_dwordx4 v[100:103], v0, s[50:51] offset:1024 sc1\n" \
    "global_load_dwordx4 v[104:107], v0, s[50:51] offset:2048 sc1\n" \
    "global_load_dwordx4 v[108:111], v0, s[50:51] offset:3072 sc1\n" \
    "global_load_dwordx4 v[112:115], v0, s[52:53] offset:0 sc1\n" \
    "global_load_dwordx4 v[116:119], v0, s[52:53] offset:1024 sc1\n" \
    "s_waitcnt vmcnt(0)\n" \
    "v_mov_b32 v200, -1\n" \
    "v_xor_b32 v201, s78, v80\n" \
    "v_min_u32 v200, v200, v201\n" \
    "v_xor_b32 v201, s78, v81\n" \
    "v_min_u32 v200, v200, v201\n" \
    "v_xor_b32 v201, s78, v82\n" \
    "v_min_u32 v200, v200, v201\n" \
    "v_xor_b32 v201, s78, v83\n" \
    "v_min_u32 v200, v200, v201\n" \
    "v_xor_b32 v201, s78, v84\n" \
    "v_min_u32 v200, v200, v201\n" \
    "v_xor_b32 v201, s78, v85\n" \
    "v_min_u32 v200, v200, v201\n" \
    "v_xor_b32 v201, s78, v86\n" \
    "v_min_u32 v200, v200, v201\n" \
    "v_xor_b32 v201, s78, v87\n" \
    "v_min_u32 v200, v200, v201\n" \
    "v_xor_b32 v201, s78, v88\n" \
    "v_min_u32 v200, v200, v201\n" \
    "v_xor_b32 v201, s78, v89\n" \
    "v_min_u32 v200, v200, v201\n" \
    "v_xor_b32 v201, s78, v90\n" \
    "v_min_u32 v200, v200, v201\n" \
    "v_xor_b32 v201, s78, v91\n" \
    "v_min_u32 v200, v200, v201\n" \
    "v_xor_b32 v201, s78, v92\n" \
    "v_min_u32 v200, v200, v201\n" \
    "v_xor_b32 v201, s78, v93\n" \
    "v_min_u32 v200, v200, v201\n" \
    "v_xor_b32 v201, s78, v94\n" \
    "v_min_u32 v200, v200, v201\n" \
    "v_xor_b32 v201, s78, v95\n" \
    "v_min_u32 v200, v200, v201\n" \
    "v_xor_b32 v201, s78, v96\n" \
    "v_min_u32 v200, v200, v201\n" \
    "v_xor_b32 v201, s78, v97\n" \
    "v_min_u32 v200, v200, v201\n" \
    "v_xor_b32 v201, s78, v98\n" \
    "v_min_u32 v200, v200, v201\n" \
    "v_xor_b32 v201, s78, v99\n" \
    "v_min_u32 v200, v200, v201\n" \
    "v_xor_b32 v201, s78, v100\n" \
    "v_min_u32 v200, v200, v201\n" \
    "v_xor_b32 v201, s78, v101\n" \
    "v_min_u32 v200, v200, v201\n" \
    "v_xor_b32 v201, s78, v102\n" \
    "v_min_u32 v200, v200, v201\n" \
    "v_xor_b32 v201, s78, v103\n" \
    "v_min_u32 v200, v200, v201\n" \
    "v_xor_b32 v201, s78, v104\n" \
    "v_min_u32 v200, v200, v201\n" \
    "v_xor_b32 v201, s78, v105\n" \
    "v_min_u32 v200, v200, v201\n" \
    "v_xor_b32 v201, s78, v106\n" \
    "v_min_u32 v200, v200, v201\n" \
    "v_xor_b32 v201, s78, v107\n" \
    "v_min_u32 v200, v200, v201\n" \
    "v_xor_b32 v201, s78, v108\n" \
    "v_min_u32 v200, v200, v201\n" \
    "v_xor_b32 v201, s78, v109\n" \
    "v_min_u32 v200, v200, v201\n" \
    "v_xor_b32 v201, s78, v110\n" \
    "v_min_u32 v200, v200, v201\n" \
    "v_xor_b32 v201, s78, v111\n" \
    "v_min_u32 v200, v200, v201\n" \
    "v_xor_b32 v201, s78, v112\n" \
    "v_min_u32 v200, v200, v201\n" \
    "v_xor_b32 v201, s78, v113\n" \
    "v_min_u32 v200, v200, v201\n" \
    "v_xor_b32 v201, s78, v114\n" \
    "v_min_u32 v200, v200, v201\n" \
    "v_xor_b32 v201, s78, v115\n" \
    "v_min_u32 v200, v200, v201\n" \
    "v_xor_b32 v201, s78, v116\n" \
    "v_min_u32 v200, v200, v201\n" \
    "v_xor_b32 v201, s78, v117\n" \
    "v_min_u32 v200, v200, v201\n" \
    "v_xor_b32 v201, s78, v118\n" \
    "v_min_u32 v200, v200, v201\n" \
    "v_xor_b32 v201, s78, v119\n" \
    "v_min_u32 v200, v200, v201\n" \
    "v_cmp_eq_u32 vcc, 0, v200\n" \
    "s_cbranch_vccz HMK%=\n" \
    "s_sleep 2\n" \
    "s_add_u32 s70, s70, 1\n" \
    "s_or_b32 s66, s70, s71\n" \
    "s_cmp_lt_u32 s66, 65536\n" \
    "s_cbranch_scc1 HMP%=\n" \
    "s_mov_b32 s71, 65536\n" \
    "HMK%=:\n" \
    "s_waitcnt lgkmcnt(0)\n" \
    "v_mov_b64 v[120:121], 0\n" \
    "v_mov_b64 v[122:123], 0\n" \
    "v_mov_b64 v[124:125], 0\n" \
    "v_mov_b64 v[126:127], 0\n" \
    "v_mov_b64 v[128:129], 0\n" \
    "v_mov_b64 v[130:131], 0\n" \
    "v_mov_b64 v[132:133], 0\n" \
    "v_mov_b64 v[134:135], 0\n" \
    "s_mov_b64 s[86:87], 0\n" \
    "s_nop 1\n" \
    "s_branch HSHOT%=\n" \
    "HVSLOW%=:\n" \
    "s_add_u32 s83, s83, 1\n" \
    "s_and_b32 s66, s79, 3\n" \
    "s_or_b32 s66, s66, s95\n" \
    "s_cmp_eq_u32 s66, 0\n" \
    "s_cselect_b32 s66, 24, 0\n" \
    "s_add_u32 s44, s44, s66\n" \
    "s_mov_b64 exec, -1\n" \
    "s_mov_b32 s70, 0\n" \
    "HVP%=:\n" \
    "global_load_dwordx2 v[192:193], v3, s[88:89] sc1\n" \
    "s_waitcnt vmcnt(0)\n" \
    "v_mov_b32 v200, -1\n" \
    "v_xor_b32 v201, s78, v192\n" \
    "v_min_u32 v200, v200, v201\n" \
    "v_xor_b32 v201, s78, v193\n" \
    "v_min_u32 v200, v200, v201\n" \
    "v_cmp_eq_u32 vcc, 0, v200\n" \
    "s_cbranch_vccz HVK%=\n" \
    "s_sleep 2\n" \
    "s_add_u32 s70, s70, 1\n" \
    "s_or_b32 s66, s70, s71\n" \
    "s_cmp_lt_u32 s66, 65536\n" \
    "s_cbranch_scc1 HVP%=\n" \
    "s_mov_b32 s71, 65536\n" \
    "HVK%=:\n" \
    "s_mov_b32 s80, -1\n" \
    "v_min_u32 v192, -2, v192\n" \
    "v_min_u32 v193, -2, v193\n" \
    "s_nop 1\n" \
    "s_branch HVCONT%=\n" \
    "HEND%=:\n" \
    "s_waitcnt vmcnt(0)\n" \
    "s_lshl_b32 s66, s94, 5\n" \
    "s_add_u32 s66, s66, s41\n" \
    "s_lshl_b32 s66, s66, 7\n" \
    "s_add_u32 s66, s66, 67239936\n" \
    "v_mov_b32 v8, s66\n" \
    "v_mov_b32 v9, 1\n" \
    "s_mov_b64 exec, 1\n" \
    "global_atomic_add v8, v9, s[38:39] sc1\n" \
    "s_mov_b64 exec, -1\n" \
    ""
#define RED_ASM \
    "s_mov_b64 s[36:37], %2\n" \
    "s_mov_b64 s[38:39], %3\n" \
    "s_mov_b32 s42, %4\n" \
    "s_and_b32 s40, %5, 255\n" \
    "s_lshr_b32 s98, %5, 8\n" \
    "s_mov_b32 s41, %6\n" \
    "s_mov_b32 s45, %7\n" \
    "s_mov_b32 s44, %8\n" \
    "s_mov_b32 s99, %9\n" \
    "s_mov_b64 s[84:85], %10\n" \
    "s_mov_b64 exec, -1\n" \
    "s_mov_b32 s78, 0xffbfffbf\n" \
    "s_mov_b32 s71, 0\n" \
    "s_mov_b32 s82, 0\n" \
    "s_mov_b32 s83, 0\n" \
    "v_mbcnt_lo_u32_b32 v192, -1, 0\n" \
    "v_mbcnt_hi_u32_b32 v192, -1, v192\n" \
    "v_lshlrev_b32 v0, 4, v192\n" \
    "v_lshrrev_b32 v193, 4, v192\n" \
    "v_and_b32 v194, 15, v192\n" \
    "v_bfe_u32 v195, v192, 2, 2\n" \
    "v_and_b32 v196, 3, v192\n" \
    "v_and_b32 v6, 2, v192\n" \
    "v_cmp_ne_u32 vcc, 0, v6\n" \
    "v_mov_b32 v6, 0x4444\n" \
    "v_mov_b32 v7, 0xeeee\n" \
    "v_cndmask_b32 v1, v6, v7, vcc\n" \
    "v_and_b32 v6, 31, v192\n" \
    "v_lshlrev_b32 v2, 7, v6\n" \
    "s_lshl_b32 s66, s41, 2\n" \
    "s_add_u32 s66, s66, 131072\n" \
    "v_add_u32 v3, s66, v0\n" \
    "v_and_b32 v6, 1, v193\n" \
    "v_lshlrev_b32 v6, 1, v6\n" \
    "v_and_b32 v7, 1, v195\n" \
    "v_add_lshl_u32 v6, v6, v7, 4\n" \
    "v_lshrrev_b32 v7, 1, v195\n" \
    "v_lshl_add_u32 v6, v7, 3, v6\n" \
    "v_lshl_add_u32 v6, v196, 1, v6\n" \
    "v_lshlrev_b32 v6, 4, v6\n" \
    "v_add_u32 v4, 131072, v6\n" \
    "s_lshr_b32 s66, s40, 3\n" \
    "s_lshr_b32 s67, s66, 3\n" \
    "s_lshl_b32 s67, s67, 15\n" \
    "s_and_b32 s66, s66, 7\n" \
    "s_lshl_b32 s66, s66, 10\n" \
    "s_add_u32 s66, s66, s67\n" \
    "s_and_b32 s67, s40, 7\n" \
    "s_lshl_b32 s67, s67, 7\n" \
    "s_add_u32 s66, s66, s67\n" \
    "v_lshl_add_u32 v5, v192, 2, s66\n" \
    "s_lshl_b32 s76, s41, 3\n" \
    "s_add_u32 s66, s76, 0\n" \
    "s_min_u32 s66, s66, 31\n" \
    "s_lshr_b32 s67, s66, 3\n" \
    "s_lshl_b32 s67, s67, 14\n" \
    "s_and_b32 s66, s66, 7\n" \
    "s_lshl_b32 s66, s66, 10\n" \
    "s_add_u32 s66, s66, s67\n" \
    "s_and_b32 s67, s40, 1\n" \
    "s_lshl_b32 s67, s67, 16\n" \
    "s_add_u32 s66, s66, s67\n" \
    "v_add_u32 v224, s66, v0\n" \
    "s_add_u32 s66, s76, 1\n" \
    "s_min_u32 s66, s66, 31\n" \
    "s_lshr_b32 s67, s66, 3\n" \
    "s_lshl_b32 s67, s67, 14\n" \
    "s_and_b32 s66, s66, 7\n" \
    "s_lshl_b32 s66, s66, 10\n" \
    "s_add_u32 s66, s66, s67\n" \
    "s_and_b32 s67, s40, 1\n" \
    "s_lshl_b32 s67, s67, 16\n" \
    "s_add_u32 s66, s66, s67\n" \
    "v_add_u32 v225, s66, v0\n" \
    "s_add_u32 s66, s76, 2\n" \
    "s_min_u32 s66, s66, 31\n" \
    "s_lshr_b32 s67, s66, 3\n" \
    "s_lshl_b32 s67, s67, 14\n" \
    "s_and_b32 s66, s66, 7\n" \
    "s_lshl_b32 s66, s66, 10\n" \
    "s_add_u32 s66, s66, s67\n" \
    "s_and_b32 s67, s40, 1\n" \
    "s_lshl_b32 s67, s67, 16\n" \
    "s_add_u32 s66, s66, s67\n" \
    "v_add_u32 v226, s66, v0\n" \
    "s_add_u32 s66, s76, 3\n" \
    "s_min_u32 s66, s66, 31\n" \
    "s_lshr_b32 s67, s66, 3\n" \
    "s_lshl_b32 s67, s67, 14\n" \
    "s_and_b32 s66, s66, 7\n" \
    "s_lshl_b32 s66, s66, 10\n" \
    "s_add_u32 s66, s66, s67\n" \
    "s_and_b32 s67, s40, 1\n" \
    "s_lshl_b32 s67, s67, 16\n" \
    "s_add_u32 s66, s66, s67\n" \
    "v_add_u32 v227, s66, v0\n" \
    "s_add_u32 s66, s76, 4\n" \
    "s_min_u32 s66, s66, 31\n" \
    "s_lshr_b32 s67, s66, 3\n" \
    "s_lshl_b32 s67, s67, 14\n" \
    "s_and_b32 s66, s66, 7\n" \
    "s_lshl_b32 s66, s66, 10\n" \
    "s_add_u32 s66, s66, s67\n" \
    "s_and_b32 s67, s40, 1\n" \
    "s_lshl_b32 s67, s67, 16\n" \
    "s_add_u32 s66, s66, s67\n" \
    "v_add_u32 v228, s66, v0\n" \
    "s_add_u32 s66, s76, 5\n" \
    "s_min_u32 s66, s66, 31\n" \
    "s_lshr_b32 s67, s66, 3\n" \
    "s_lshl_b32 s67, s67, 14\n" \
    "s_and_b32 s66, s66, 7\n" \
    "s_lshl_b32 s66, s66, 10\n" \
    "s_add_u32 s66, s66, s67\n" \
    "s_and_b32 s67, s40, 1\n" \
    "s_lshl_b32 s67, s67, 16\n" \
    "s_add_u32 s66, s66, s67\n" \
    "v_add_u32 v229, s66, v0\n" \
    "s_add_u32 s66, s76, 6\n" \
    "s_min_u32 s66, s66, 31\n" \
    "s_lshr_b32 s67, s66, 3\n" \
    "s_lshl_b32 s67, s67, 14\n" \
    "s_and_b32 s66, s66, 7\n" \
    "s_lshl_b32 s66, s66, 10\n" \
    "s_add_u32 s66, s66, s67\n" \
    "s_and_b32 s67, s40, 1\n" \
    "s_lshl_b32 s67, s67, 16\n" \
    "s_add_u32 s66, s66, s67\n" \
    "v_add_u32 v230, s66, v0\n" \
    "s_add_u32 s66, s76, 7\n" \
    "s_min_u32 s66, s66, 31\n" \
    "s_lshr_b32 s67, s66, 3\n" \
    "s_lshl_b32 s67, s67, 14\n" \
    "s_and_b32 s66, s66, 7\n" \
    "s_lshl_b32 s66, s66, 10\n" \
    "s_add_u32 s66, s66, s67\n" \
    "s_and_b32 s67, s40, 1\n" \
    "s_lshl_b32 s67, s67, 16\n" \
    "s_add_u32 s66, s66, s67\n" \
    "v_add_u32 v231, s66, v0\n" \
    "s_lshl_b32 s66, s41, 11\n" \
    "s_add_u32 s66, s66, 133120\n" \
    "v_lshl_add_u32 v8, v192, 3, s66\n" \
    "s_and_b32 s66, s41, 1\n" \
    "s_lshl_b32 s66, s66, 10\n" \
    "s_add_u32 s66, s66, 133120\n" \
    "v_lshl_add_u32 v9, v192, 3, s66\n" \
    "s_and_b32 s66, s98, 1\n" \
    "s_lshl_b32 s66, s66, 1\n" \
    "s_and_b32 s67, s41, 1\n" \
    "s_add_u32 s66, s66, s67\n" \
    "s_lshl_b32 s66, s66, 9\n" \
    "v_lshl_add_u32 v10, v192, 3, s66\n" \
    "s_lshr_b32 s66, s98, 3\n" \
    "s_lshl_b32 s66, s66, 15\n" \
    "s_bfe_u32 s67, s98, 0x20001\n" \
    "s_lshl_b32 s67, s67, 11\n" \
    "s_add_u32 s77, s66, s67\n" \
    "s_add_u32 s77, s77, 1048576\n" \
    "s_add_u32 s77, s77, s99\n" \
    "s_cmp_ge_u32 s40, 80\n" \
    "s_cbranch_scc1 RNOW%=\n" \
    "v_lshrrev_b32 v6, 1, v192\n" \
    "v_mul_u32_u24 v6, 1280, v6\n" \
    "v_and_b32 v7, 1, v192\n" \
    "v_lshl_add_u32 v200, v7, 4, v6\n" \
    "s_lshr_b32 s66, s40, 1\n" \
    "s_lshl_b32 s66, s66, 5\n" \
    "s_mul_i32 s67, s76, 163840\n" \
    "s_add_u32 s66, s66, s67\n" \
    "s_mov_b64 s[80:81], %0\n" \
    "s_add_u32 s80, s80, s66\n" \
    "s_addc_u32 s81, s81, 0\n" \
    "s_lshl_b32 s66, s76, 12\n" \
    "s_add_u32 m0, s66, 0\n" \
    "s_nop 0\n" \
    "global_load_lds_dwordx4 v200, s[80:81]\n" \
    "s_add_u32 s80, s80, 40960\n" \
    "s_addc_u32 s81, s81, 0\n" \
    "s_add_u32 m0, s66, 1024\n" \
    "s_nop 0\n" \
    "global_load_lds_dwordx4 v200, s[80:81]\n" \
    "s_add_u32 s80, s80, 40960\n" \
    "s_addc_u32 s81, s81, 0\n" \
    "s_add_u32 m0, s66, 2048\n" \
    "s_nop 0\n" \
    "global_load_lds_dwordx4 v200, s[80:81]\n" \
    "s_add_u32 s80, s80, 40960\n" \
    "s_addc_u32 s81, s81, 0\n" \
    "s_add_u32 m0, s66, 3072\n" \
    "s_nop 0\n" \
    "global_load_lds_dwordx4 v200, s[80:81]\n" \
    "s_add_u32 s80, s80, 40960\n" \
    "s_addc_u32 s81, s81, 0\n" \
    "s_add_u32 m0, s66, 4096\n" \
    "s_nop 0\n" \
    "global_load_lds_dwordx4 v200, s[80:81]\n" \
    "s_add_u32 s80, s80, 40960\n" \
    "s_addc_u32 s81, s81, 0\n" \
    "s_add_u32 m0, s66, 5120\n" \
    "s_nop 0\n" \
    "global_load_lds_dwordx4 v200, s[80:81]\n" \
    "s_add_u32 s80, s80, 40960\n" \
    "s_addc_u32 s81, s81, 0\n" \
    "s_add_u32 m0, s66, 6144\n" \
    "s_nop 0\n" \
    "global_load_lds_dwordx4 v200, s[80:81]\n" \
    "s_add_u32 s80, s80, 40960\n" \
    "s_addc_u32 s81, s81, 0\n" \
    "s_add_u32 m0, s66, 7168\n" \
    "s_nop 0\n" \
    "global_load_lds_dwordx4 v200, s[80:81]\n" \
    "s_add_u32 s80, s80, 40960\n" \
    "s_addc_u32 s81, s81, 0\n" \
    "s_add_u32 m0, s66, 8192\n" \
    "s_nop 0\n" \
    "global_load_lds_dwordx4 v200, s[80:81]\n" \
    "s_add_u32 s80, s80, 40960\n" \
    "s_addc_u32 s81, s81, 0\n" \
    "s_add_u32 m0, s66, 9216\n" \
    "s_nop 0\n" \
    "global_load_lds_dwordx4 v200, s[80:81]\n" \
    "s_add_u32 s80, s80, 40960\n" \
    "s_addc_u32 s81, s81, 0\n" \
    "s_add_u32 m0, s66, 10240\n" \
    "s_nop 0\n" \
    "global_load_lds_dwordx4 v200, s[80:81]\n" \
    "s_add_u32 s80, s80, 40960\n" \
    "s_addc_u32 s81, s81, 0\n" \
    "s_add_u32 m0, s66, 11264\n" \
    "s_nop 0\n" \
    "global_load_lds_dwordx4 v200, s[80:81]\n" \
    "s_add_u32 s80, s80, 40960\n" \
    "s_addc_u32 s81, s81, 0\n" \
    "s_add_u32 m0, s66, 12288\n" \
    "s_nop 0\n" \
    "global_load_lds_dwordx4 v200, s[80:81]\n" \
    "s_add_u32 s80, s80, 40960\n" \
    "s_addc_u32 s81, s81, 0\n" \
    "s_add_u32 m0, s66, 13312\n" \
    "s_nop 0\n" \
    "global_load_lds_dwordx4 v200, s[80:81]\n" \
    "s_add_u32 s80, s80, 40960\n" \
    "s_addc_u32 s81, s81, 0\n" \
    "s_add_u32 m0, s66, 14336\n" \
    "s_nop 0\n" \
    "global_load_lds_dwordx4 v200, s[80:81]\n" \
    "s_add_u32 s80, s80, 40960\n" \
    "s_addc_u32 s81, s81, 0\n" \
    "s_add_u32 m0, s66, 15360\n" \
    "s_nop 0\n" \
    "global_load_lds_dwordx4 v200, s[80:81]\n" \
    "s_add_u32 s80, s80, 40960\n" \
    "s_addc_u32 s81, s81, 0\n" \
    "s_add_u32 m0, s66, 16384\n" \
    "s_nop 0\n" \
    "global_load_lds_dwordx4 v200, s[80:81]\n" \
    "s_add_u32 s80, s80, 40960\n" \
    "s_addc_u32 s81, s81, 0\n" \
    "s_add_u32 m0, s66, 17408\n" \
    "s_nop 0\n" \
    "global_load_lds_dwordx4 v200, s[80:81]\n" \
    "s_add_u32 s80, s80, 40960\n" \
    "s_addc_u32 s81, s81, 0\n" \
    "s_add_u32 m0, s66, 18432\n" \
    "s_nop 0\n" \
    "global_load_lds_dwordx4 v200, s[80:81]\n" \
    "s_add_u32 s80, s80, 40960\n" \
    "s_addc_u32 s81, s81, 0\n" \
    "s_add_u32 m0, s66, 19456\n" \
    "s_nop 0\n" \
    "global_load_lds_dwordx4 v200, s[80:81]\n" \
    "s_add_u32 s80, s80, 40960\n" \
    "s_addc_u32 s81, s81, 0\n" \
    "s_add_u32 m0, s66, 20480\n" \
    "s_nop 0\n" \
    "global_load_lds_dwordx4 v200, s[80:81]\n" \
    "s_add_u32 s80, s80, 40960\n" \
    "s_addc_u32 s81, s81, 0\n" \
    "s_add_u32 m0, s66, 21504\n" \
    "s_nop 0\n" \
    "global_load_lds_dwordx4 v200, s[80:81]\n" \
    "s_add_u32 s80, s80, 40960\n" \
    "s_addc_u32 s81, s81, 0\n" \
    "s_add_u32 m0, s66, 22528\n" \
    "s_nop 0\n" \
    "global_load_lds_dwordx4 v200, s[80:81]\n" \
    "s_add_u32 s80, s80, 40960\n" \
    "s_addc_u32 s81, s81, 0\n" \
    "s_add_u32 m0, s66, 23552\n" \
    "s_nop 0\n" \
    "global_load_lds_dwordx4 v200, s[80:81]\n" \
    "s_add_u32 s80, s80, 40960\n" \
    "s_addc_u32 s81, s81, 0\n" \
    "s_add_u32 m0, s66, 24576\n" \
    "s_nop 0\n" \
    "global_load_lds_dwordx4 v200, s[80:81]\n" \
    "s_add_u32 s80, s80, 40960\n" \
    "s_addc_u32 s81, s81, 0\n" \
    "s_add_u32 m0, s66, 25600\n" \
    "s_nop 0\n" \
    "global_load_lds_dwordx4 v200, s[80:81]\n" \
    "s_add_u32 s80, s80, 40960\n" \
    "s_addc_u32 s81, s81, 0\n" \
    "s_add_u32 m0, s66, 26624\n" \
    "s_nop 0\n" \
    "global_load_lds_dwordx4 v200, s[80:81]\n" \
    "s_add_u32 s80, s80, 40960\n" \
    "s_addc_u32 s81, s81, 0\n" \
    "s_add_u32 m0, s66, 27648\n" \
    "s_nop 0\n" \
    "global_load_lds_dwordx4 v200, s[80:81]\n" \
    "s_add_u32 s80, s80, 40960\n" \
    "s_addc_u32 s81, s81, 0\n" \
    "s_add_u32 m0, s66, 28672\n" \
    "s_nop 0\n" \
    "global_load_lds_dwordx4 v200, s[80:81]\n" \
    "s_add_u32 s80, s80, 40960\n" \
    "s_addc_u32 s81, s81, 0\n" \
    "s_add_u32 m0, s66, 29696\n" \
    "s_nop 0\n" \
    "global_load_lds_dwordx4 v200, s[80:81]\n" \
    "s_add_u32 s80, s80, 40960\n" \
    "s_addc_u32 s81, s81, 0\n" \
    "s_add_u32 m0, s66, 30720\n" \
    "s_nop 0\n" \
    "global_load_lds_dwordx4 v200, s[80:81]\n" \
    "s_add_u32 s80, s80, 40960\n" \
    "s_addc_u32 s81, s81, 0\n" \
    "s_add_u32 m0, s66, 31744\n" \
    "s_nop 0\n" \
    "global_load_lds_dwordx4 v200, s[80:81]\n" \
    "s_add_u32 s80, s80, 40960\n" \
    "s_addc_u32 s81, s81, 0\n" \
    "v_lshrrev_b32 v6, 1, v195\n" \
    "v_lshlrev_b32 v6, 3, v6\n" \
    "v_lshl_add_u32 v6, v193, 4, v6\n" \
    "v_and_b32 v7, 1, v195\n" \
    "v_add_lshl_u32 v6, v6, v7, 5\n" \
    "v_lshl_add_u32 v6, v196, 3, v6\n" \
    "v_add_u32 v201, s66, v6\n" \
    "s_waitcnt vmcnt(0)\n" \
    "ds_read_b64_tr_b16 v[64:65], v201 offset:0\n" \
    "ds_read_b64_tr_b16 v[66:67], v201 offset:64\n" \
    "ds_read_b64_tr_b16 v[68:69], v201 offset:2048\n" \
    "ds_read_b64_tr_b16 v[70:71], v201 offset:2112\n" \
    "ds_read_b64_tr_b16 v[72:73], v201 offset:128\n" \
    "ds_read_b64_tr_b16 v[74:75], v201 offset:192\n" \
    "ds_read_b64_tr_b16 v[76:77], v201 offset:2176\n" \
    "ds_read_b64_tr_b16 v[78:79], v201 offset:2240\n" \
    "s_waitcnt lgkmcnt(0)\n" \
    "ds_read_b64_tr_b16 v[80:81], v201 offset:4096\n" \
    "ds_read_b64_tr_b16 v[82:83], v201 offset:4160\n" \
    "ds_read_b64_tr_b16 v[84:85], v201 offset:6144\n" \
    "ds_read_b64_tr_b16 v[86:87], v201 offset:6208\n" \
    "ds_read_b64_tr_b16 v[88:89], v201 offset:4224\n" \
    "ds_read_b64_tr_b16 v[90:91], v201 offset:4288\n" \
    "ds_read_b64_tr_b16 v[92:93], v201 offset:6272\n" \
    "ds_read_b64_tr_b16 v[94:95], v201 offset:6336\n" \
    "s_waitcnt lgkmcnt(0)\n" \
    "ds_read_b64_tr_b16 v[96:97], v201 offset:8192\n" \
    "ds_read_b64_tr_b16 v[98:99], v201 offset:8256\n" \
    "ds_read_b64_tr_b16 v[100:101], v201 offset:10240\n" \
    "ds_read_b64_tr_b16 v[102:103], v201 offset:10304\n" \
    "ds_read_b64_tr_b16 v[104:105], v201 offset:8320\n" \
    "ds_read_b64_tr_b16 v[106:107], v201 offset:8384\n" \
    "ds_read_b64_tr_b16 v[108:109], v201 offset:10368\n" \
    "ds_read_b64_tr_b16 v[110:111], v201 offset:10432\n" \
    "s_waitcnt lgkmcnt(0)\n" \
    "ds_read_b64_tr_b16 v[112:113], v201 offset:12288\n" \
    "ds_read_b64_tr_b16 v[114:115], v201 offset:12352\n" \
    "ds_read_b64_tr_b16 v[116:117], v201 offset:14336\n" \
    "ds_read_b64_tr_b16 v[118:119], v201 offset:14400\n" \
    "ds_read_b64_tr_b16 v[120:121], v201 offset:12416\n" \
    "ds_read_b64_tr_b16 v[122:123], v201 offset:12480\n" \
    "ds_read_b64_tr_b16 v[124:125], v201 offset:14464\n" \
    "ds_read_b64_tr_b16 v[126:127], v201 offset:14528\n" \
    "s_waitcnt lgkmcnt(0)\n" \
    "ds_read_b64_tr_b16 v[128:129], v201 offset:16384\n" \
    "ds_read_b64_tr_b16 v[130:131], v201 offset:16448\n" \
    "ds_read_b64_tr_b16 v[132:133], v201 offset:18432\n" \
    "ds_read_b64_tr_b16 v[134:135], v201 offset:18496\n" \
    "ds_read_b64_tr_b16 v[136:137], v201 offset:16512\n" \
    "ds_read_b64_tr_b16 v[138:139], v201 offset:16576\n" \
    "ds_read_b64_tr_b16 v[140:141], v201 offset:18560\n" \
    "ds_read_b64_tr_b16 v[142:143], v201 offset:18624\n" \
    "s_waitcnt lgkmcnt(0)\n" \
    "ds_read_b64_tr_b16 v[144:145], v201 offset:20480\n" \
    "ds_read_b64_tr_b16 v[146:147], v201 offset:20544\n" \
    "ds_read_b64_tr_b16 v[148:149], v201 offset:22528\n" \
    "ds_read_b64_tr_b16 v[150:151], v201 offset:22592\n" \
    "ds_read_b64_tr_b16 v[152:153], v201 offset:20608\n" \
    "ds_read_b64_tr_b16 v[154:155], v201 offset:20672\n" \
    "ds_read_b64_tr_b16 v[156:157], v201 offset:22656\n" \
    "ds_read_b64_tr_b16 v[158:159], v201 offset:22720\n" \
    "s_waitcnt lgkmcnt(0)\n" \
    "ds_read_b64_tr_b16 v[160:161], v201 offset:24576\n" \
    "ds_read_b64_tr_b16 v[162:163], v201 offset:24640\n" \
    "ds_read_b64_tr_b16 v[164:165], v201 offset:26624\n" \
    "ds_read_b64_tr_b16 v[166:167], v201 offset:26688\n" \
    "ds_read_b64_tr_b16 v[168:169], v201 offset:24704\n" \
    "ds_read_b64_tr_b16 v[170:171], v201 offset:24768\n" \
    "ds_read_b64_tr_b16 v[172:173], v201 offset:26752\n" \
    "ds_read_b64_tr_b16 v[174:175], v201 offset:26816\n" \
    "s_waitcnt lgkmcnt(0)\n" \
    "ds_read_b64_tr_b16 v[176:177], v201 offset:28672\n" \
    "ds_read_b64_tr_b16 v[178:179], v201 offset:28736\n" \
    "ds_read_b64_tr_b16 v[180:181], v201 offset:30720\n" \
    "ds_read_b64_tr_b16 v[182:183], v201 offset:30784\n" \
    "ds_read_b64_tr_b16 v[184:185], v201 offset:28800\n" \
    "ds_read_b64_tr_b16 v[186:187], v201 offset:28864\n" \
    "ds_read_b64_tr_b16 v[188:189], v201 offset:30848\n" \
    "ds_read_b64_tr_b16 v[190:191], v201 offset:30912\n" \
    "s_waitcnt lgkmcnt(0)\n" \
    "RNOW%=:\n" \
    "s_lshl_b32 s66, s41, 15\n" \
    "s_cmp_ge_u32 s98, 64\n" \
    "s_cbranch_scc1 RNOH%=\n" \
    "v_lshrrev_b32 v6, 3, v192\n" \
    "v_lshlrev_b32 v6, 13, v6\n" \
    "v_and_b32 v7, 7, v192\n" \
    "v_lshl_add_u32 v200, v7, 4, v6\n" \
    "s_add_u32 s67, s41, 0\n" \
    "s_min_u32 s67, s67, 9\n" \
    "s_lshl_b32 s67, s67, 19\n" \
    "s_lshl_b32 s70, s98, 7\n" \
    "s_add_u32 s67, s67, s70\n" \
    "s_mov_b64 s[80:81], %1\n" \
    "s_add_u32 s80, s80, s67\n" \
    "s_addc_u32 s81, s81, 0\n" \
    "s_add_u32 m0, s66, 0\n" \
    "s_nop 0\n" \
    "global_load_lds_dwordx4 v200, s[80:81]\n" \
    "s_add_u32 s80, s80, 65536\n" \
    "s_addc_u32 s81, s81, 0\n" \
    "s_add_u32 m0, s66, 1024\n" \
    "s_nop 0\n" \
    "global_load_lds_dwordx4 v200, s[80:81]\n" \
    "s_add_u32 s80, s80, 65536\n" \
    "s_addc_u32 s81, s81, 0\n" \
    "s_add_u32 m0, s66, 2048\n" \
    "s_nop 0\n" \
    "global_load_lds_dwordx4 v200, s[80:81]\n" \
    "s_add_u32 s80, s80, 65536\n" \
    "s_addc_u32 s81, s81, 0\n" \
    "s_add_u32 m0, s66, 3072\n" \
    "s_nop 0\n" \
    "global_load_lds_dwordx4 v200, s[80:81]\n" \
    "s_add_u32 s80, s80, 65536\n" \
    "s_addc_u32 s81, s81, 0\n" \
    "s_add_u32 m0, s66, 4096\n" \
    "s_nop 0\n" \
    "global_load_lds_dwordx4 v200, s[80:81]\n" \
    "s_add_u32 s80, s80, 65536\n" \
    "s_addc_u32 s81, s81, 0\n" \
    "s_add_u32 m0, s66, 5120\n" \
    "s_nop 0\n" \
    "global_load_lds_dwordx4 v200, s[80:81]\n" \
    "s_add_u32 s80, s80, 65536\n" \
    "s_addc_u32 s81, s81, 0\n" \
    "s_add_u32 m0, s66, 6144\n" \
    "s_nop 0\n" \
    "global_load_lds_dwordx4 v200, s[80:81]\n" \
    "s_add_u32 s80, s80, 65536\n" \
    "s_addc_u32 s81, s81, 0\n" \
    "s_add_u32 m0, s66, 7168\n" \
    "s_nop 0\n" \
    "global_load_lds_dwordx4 v200, s[80:81]\n" \
    "s_add_u32 s80, s80, 65536\n" \
    "s_addc_u32 s81, s81, 0\n" \
    "s_add_u32 s67, s41, 4\n" \
    "s_min_u32 s67, s67, 9\n" \
    "s_lshl_b32 s67, s67, 19\n" \
    "s_lshl_b32 s70, s98, 7\n" \
    "s_add_u32 s67, s67, s70\n" \
    "s_mov_b64 s[80:81], %1\n" \
    "s_add_u32 s80, s80, s67\n" \
    "s_addc_u32 s81, s81, 0\n" \
    "s_add_u32 m0, s66, 8192\n" \
    "s_nop 0\n" \
    "global_load_lds_dwordx4 v200, s[80:81]\n" \
    "s_add_u32 s80, s80, 65536\n" \
    "s_addc_u32 s81, s81, 0\n" \
    "s_add_u32 m0, s66, 9216\n" \
    "s_nop 0\n" \
    "global_load_lds_dwordx4 v200, s[80:81]\n" \
    "s_add_u32 s80, s80, 65536\n" \
    "s_addc_u32 s81, s81, 0\n" \
    "s_add_u32 m0, s66, 10240\n" \
    "s_nop 0\n" \
    "global_load_lds_dwordx4 v200, s[80:81]\n" \
    "s_add_u32 s80, s80, 65536\n" \
    "s_addc_u32 s81, s81, 0\n" \
    "s_add_u32 m0, s66, 11264\n" \
    "s_nop 0\n" \
    "global_load_lds_dwordx4 v200, s[80:81]\n" \
    "s_add_u32 s80, s80, 65536\n" \
    "s_addc_u32 s81, s81, 0\n" \
    "s_add_u32 m0, s66, 12288\n" \
    "s_nop 0\n" \
    "global_load_lds_dwordx4 v200, s[80:81]\n" \
    "s_add_u32 s80, s80, 65536\n" \
    "s_addc_u32 s81, s81, 0\n" \
    "s_add_u32 m0, s66, 13312\n" \
    "s_nop 0\n" \
    "global_load_lds_dwordx4 v200, s[80:81]\n" \
    "s_add_u32 s80, s80, 65536\n" \
    "s_addc_u32 s81, s81, 0\n" \
    "s_add_u32 m0, s66, 14336\n" \
    "s_nop 0\n" \
    "global_load_lds_dwordx4 v200, s[80:81]\n" \
    "s_add_u32 s80, s80, 65536\n" \
    "s_addc_u32 s81, s81, 0\n" \
    "s_add_u32 m0, s66, 15360\n" \
    "s_nop 0\n" \
    "global_load_lds_dwordx4 v200, s[80:81]\n" \
    "s_add_u32 s80, s80, 65536\n" \
    "s_addc_u32 s81, s81, 0\n" \
    "s_add_u32 s67, s41, 8\n" \
    "s_min_u32 s67, s67, 9\n" \
    "s_lshl_b32 s67, s67, 19\n" \
    "s_lshl_b32 s70, s98, 7\n" \
    "s_add_u32 s67, s67, s70\n" \
    "s_mov_b64 s[80:81], %1\n" \
    "s_add_u32 s80, s80, s67\n" \
    "s_addc_u32 s81, s81, 0\n" \
    "s_add_u32 m0, s66, 16384\n" \
    "s_nop 0\n" \
    "global_load_lds_dwordx4 v200, s[80:81]\n" \
    "s_add_u32 s80, s80, 65536\n" \
    "s_addc_u32 s81, s81, 0\n" \
    "s_add_u32 m0, s66, 17408\n" \
    "s_nop 0\n" \
    "global_load_lds_dwordx4 v200, s[80:81]\n" \
    "s_add_u32 s80, s80, 65536\n" \
    "s_addc_u32 s81, s81, 0\n" \
    "s_add_u32 m0, s66, 18432\n" \
    "s_nop 0\n" \
    "global_load_lds_dwordx4 v200, s[80:81]\n" \
    "s_add_u32 s80, s80, 65536\n" \
    "s_addc_u32 s81, s81, 0\n" \
    "s_add_u32 m0, s66, 19456\n" \
    "s_nop 0\n" \
    "global_load_lds_dwordx4 v200, s[80:81]\n" \
    "s_add_u32 s80, s80, 65536\n" \
    "s_addc_u32 s81, s81, 0\n" \
    "s_add_u32 m0, s66, 20480\n" \
    "s_nop 0\n" \
    "global_load_lds_dwordx4 v200, s[80:81]\n" \
    "s_add_u32 s80, s80, 65536\n" \
    "s_addc_u32 s81, s81, 0\n" \
    "s_add_u32 m0, s66, 21504\n" \
    "s_nop 0\n" \
    "global_load_lds_dwordx4 v200, s[80:81]\n" \
    "s_add_u32 s80, s80, 65536\n" \
    "s_addc_u32 s81, s81, 0\n" \
    "s_add_u32 m0, s66, 22528\n" \
    "s_nop 0\n" \
    "global_load_lds_dwordx4 v200, s[80:81]\n" \
    "s_add_u32 s80, s80, 65536\n" \
    "s_addc_u32 s81, s81, 0\n" \
    "s_add_u32 m0, s66, 23552\n" \
    "s_nop 0\n" \
    "global_load_lds_dwordx4 v200, s[80:81]\n" \
    "s_add_u32 s80, s80, 65536\n" \
    "s_addc_u32 s81, s81, 0\n" \
    "v_lshrrev_b32 v6, 1, v193\n" \
    "v_lshlrev_b32 v6, 4, v6\n" \
    "v_lshrrev_b32 v197, 1, v195\n" \
    "v_lshl_add_u32 v6, v197, 3, v6\n" \
    "v_and_b32 v198, 1, v193\n" \
    "v_lshl_add_u32 v6, v198, 2, v6\n" \
    "v_and_b32 v198, 1, v195\n" \
    "v_add_u32 v6, v6, v198\n" \
    "v_lshlrev_b32 v6, 7, v6\n" \
    "v_lshl_add_u32 v6, v196, 3, v6\n" \
    "v_add_u32 v201, s66, v6\n" \
    "s_waitcnt vmcnt(0)\n" \
    "ds_read_b64_tr_b16 a[0:1], v201 offset:0\n" \
    "ds_read_b64_tr_b16 a[2:3], v201 offset:256\n" \
    "ds_read_b64_tr_b16 a[4:5], v201 offset:4096\n" \
    "ds_read_b64_tr_b16 a[6:7], v201 offset:4352\n" \
    "ds_read_b64_tr_b16 a[8:9], v201 offset:32\n" \
    "ds_read_b64_tr_b16 a[10:11], v201 offset:288\n" \
    "ds_read_b64_tr_b16 a[12:13], v201 offset:4128\n" \
    "ds_read_b64_tr_b16 a[14:15], v201 offset:4384\n" \
    "s_waitcnt lgkmcnt(0)\n" \
    "ds_read_b64_tr_b16 a[16:17], v201 offset:64\n" \
    "ds_read_b64_tr_b16 a[18:19], v201 offset:320\n" \
    "ds_read_b64_tr_b16 a[20:21], v201 offset:4160\n" \
    "ds_read_b64_tr_b16 a[22:23], v201 offset:4416\n" \
    "ds_read_b64_tr_b16 a[24:25], v201 offset:96\n" \
    "ds_read_b64_tr_b16 a[26:27], v201 offset:352\n" \
    "ds_read_b64_tr_b16 a[28:29], v201 offset:4192\n" \
    "ds_read_b64_tr_b16 a[30:31], v201 offset:4448\n" \
    "s_waitcnt lgkmcnt(0)\n" \
    "ds_read_b64_tr_b16 a[32:33], v201 offset:8192\n" \
    "ds_read_b64_tr_b16 a[34:35], v201 offset:8448\n" \
    "ds_read_b64_tr_b16 a[36:37], v201 offset:12288\n" \
    "ds_read_b64_tr_b16 a[38:39], v201 offset:12544\n" \
    "ds_read_b64_tr_b16 a[40:41], v201 offset:8224\n" \
    "ds_read_b64_tr_b16 a[42:43], v201 offset:8480\n" \
    "ds_read_b64_tr_b16 a[44:45], v201 offset:12320\n" \
    "ds_read_b64_tr_b16 a[46:47], v201 offset:12576\n" \
    "s_waitcnt lgkmcnt(0)\n" \
    "ds_read_b64_tr_b16 a[48:49], v201 offset:8256\n" \
    "ds_read_b64_tr_b16 a[50:51], v201 offset:8512\n" \
    "ds_read_b64_tr_b16 a[52:53], v201 offset:12352\n" \
    "ds_read_b64_tr_b16 a[54:55], v201 offset:12608\n" \
    "ds_read_b64_tr_b16 a[56:57], v201 offset:8288\n" \
    "ds_read_b64_tr_b16 a[58:59], v201 offset:8544\n" \
    "ds_read_b64_tr_b16 a[60:61], v201 offset:12384\n" \
    "ds_read_b64_tr_b16 a[62:63], v201 offset:12640\n" \
    "s_waitcnt lgkmcnt(0)\n" \
    "ds_read_b64_tr_b16 a[64:65], v201 offset:16384\n" \
    "ds_read_b64_tr_b16 a[66:67], v201 offset:16640\n" \
    "ds_read_b64_tr_b16 a[68:69], v201 offset:20480\n" \
    "ds_read_b64_tr_b16 a[70:71], v201 offset:20736\n" \
    "ds_read_b64_tr_b16 a[72:73], v201 offset:16416\n" \
    "ds_read_b64_tr_b16 a[74:75], v201 offset:16672\n" \
    "ds_read_b64_tr_b16 a[76:77], v201 offset:20512\n" \
    "ds_read_b64_tr_b16 a[78:79], v201 offset:20768\n" \
    "s_waitcnt lgkmcnt(0)\n" \
    "ds_read_b64_tr_b16 a[80:81], v201 offset:16448\n" \
    "ds_read_b64_tr_b16 a[82:83], v201 offset:16704\n" \
    "ds_read_b64_tr_b16 a[84:85], v201 offset:20544\n" \
    "ds_read_b64_tr_b16 a[86:87], v201 offset:20800\n" \
    "ds_read_b64_tr_b16 a[88:89], v201 offset:16480\n" \
    "ds_read_b64_tr_b16 a[90:91], v201 offset:16736\n" \
    "ds_read_b64_tr_b16 a[92:93], v201 offset:20576\n" \
    "ds_read_b64_tr_b16 a[94:95], v201 offset:20832\n" \
    "s_waitcnt lgkmcnt(0)\n" \
    "RNOH%=:\n" \
    "s_mov_b32 s43, 0\n" \
    "s_memrealtime s[28:29]\n" \
    "s_waitcnt lgkmcnt(0)\n" \
    "s_lshl_b32 s73, s28, 5\n" \
    "s_mov_b32 s48, 0x55555555\n" \
    "s_mov_b32 s49, 0x55555555\n" \
    "s_sub_u32 s73, s73, 320000\n" \
    "s_mov_b32 s46, s73\n" \
    "RSTEP%=:\n" \
    "s_cmp_ge_u32 s98, 64\n" \
    "s_cbranch_scc1 RRED%=\n" \
    "s_and_b32 s66, s43, 3\n" \
    "s_lshl_b32 s67, s66, 16\n" \
    "s_lshl_b32 s70, s41, 10\n" \
    "s_add_u32 s67, s67, s70\n" \
    "s_add_u32 s67, s67, 542720\n" \
    "s_sub_u32 s67, s67, s99\n" \
    "s_add_u32 s58, s36, s67\n" \
    "s_addc_u32 s59, s37, 0\n" \
    "s_add_u32 s60, s58, 30720\n" \
    "s_addc_u32 s61, s59, 0\n" \
    "s_and_b32 s67, s43, 1\n" \
    "s_lshl_b32 s67, s67, 18\n" \
    "s_add_u32 s66, s67, s77\n" \
    "s_add_u32 s62, s36, s66\n" \
    "s_addc_u32 s63, s37, 0\n" \
    "s_xor_b32 s67, s67, 262144\n" \
    "s_add_u32 s66, s67, s77\n" \
    "s_add_u32 s64, s36, s66\n" \
    "s_addc_u32 s65, s37, 0\n" \
    "s_lshr_b32 s66, s43, 1\n" \
    "s_lshr_b32 s66, s42, s66\n" \
    "s_and_b32 s66, s66, 1\n" \
    "s_xor_b32 s66, s66, 1\n" \
    "s_lshl_b32 s66, s66, 13\n" \
    "s_lshl_b32 s67, s43, 20\n" \
    "s_add_u32 s66, s66, s67\n" \
    "s_lshr_b32 s67, s98, 1\n" \
    "s_lshl_b32 s67, s67, 15\n" \
    "s_add_u32 s66, s66, s67\n" \
    "s_add_u32 s66, s66, 18432\n" \
    "s_add_u32 s68, s38, s66\n" \
    "s_addc_u32 s69, s39, 0\n" \
    "v_mov_b64 v[208:209], 0\n" \
    "v_mov_b64 v[210:211], 0\n" \
    "v_mov_b64 v[212:213], 0\n" \
    "v_mov_b64 v[214:215], 0\n" \
    "v_mov_b64 v[216:217], 0\n" \
    "v_mov_b64 v[218:219], 0\n" \
    "v_mov_b64 v[220:221], 0\n" \
    "v_mov_b64 v[222:223], 0\n" \
    "s_mov_b64 s[86:87], -1\n" \
    "s_mov_b32 s79, 0\n" \
    "RWH%=:\n" \
    "s_sub_u32 s66, s73, 192\n" \
    "s_lshr_b32 s66, s66, 5\n" \
    "s_memrealtime s[28:29]\n" \
    "RWHL%=:\n" \
    "s_memrealtime s[30:31]\n" \
    "s_waitcnt lgkmcnt(1)\n" \
    "s_sub_u32 s67, s28, s66\n" \
    "s_bitcmp0_b32 s67, 26\n" \
    "s_cbranch_scc1 RWHL%=X\n" \
    "s_memrealtime s[28:29]\n" \
    "s_waitcnt lgkmcnt(1)\n" \
    "s_sub_u32 s67, s30, s66\n" \
    "s_bitcmp0_b32 s67, 26\n" \
    "s_cbranch_scc1 RWHL%=X\n" \
    "s_memrealtime s[30:31]\n" \
    "s_waitcnt lgkmcnt(1)\n" \
    "s_sub_u32 s67, s28, s66\n" \
    "s_bitcmp0_b32 s67, 26\n" \
    "s_cbranch_scc1 RWHL%=X\n" \
    "s_memrealtime s[28:29]\n" \
    "s_waitcnt lgkmcnt(1)\n" \
    "s_sub_u32 s67, s30, s66\n" \
    "s_bitcmp0_b32 s67, 26\n" \
    "s_cbranch_scc1 RWHL%=X\n" \
    "s_memrealtime s[30:31]\n" \
    "s_waitcnt lgkmcnt(1)\n" \
    "s_sub_u32 s67, s28, s66\n" \
    "s_bitcmp0_b32 s67, 26\n" \
    "s_cbranch_scc1 RWHL%=X\n" \
    "s_memrealtime s[28:29]\n" \
    "s_waitcnt lgkmcnt(1)\n" \
    "s_sub_u32 s67, s30, s66\n" \
    "s_bitcmp1_b32 s67, 26\n" \
    "s_cbranch_scc1 RWHL%=\n" \
    "RWHL%=X:\n" \
    "s_add_u32 s67, s67, s66\n" \
    "s_lshl_b32 s67, s67, 5\n" \
    "s_sub_u32 s67, s73, s67\n" \
    "s_sub_i32 s67, s67, 96\n" \
    "s_cmp_gt_i32 s67, -400\n" \
    "s_cbranch_scc1 RWHL%=Y\n" \
    "s_sleep 5\n" \
    "RWHL%=Y:\n" \
    "s_max_i32 s67, s67, 0\n" \
    "s_mul_i32 s67, s67, 3\n" \
    "s_lshr_b32 s67, s67, 2\n" \
    "s_and_b32 s67, s67, 0x7c\n" \
    "s_sub_u32 s67, 140, s67\n" \
    "s_waitcnt lgkmcnt(0)\n" \
    "s_getpc_b64 s[28:29]\n" \
    "s_add_u32 s28, s28, s67\n" \
    "s_addc_u32 s29, s29, 0\n" \
    "s_setpc_b64 s[28:29]\n" \
    "s_nop 0\n" \
    "s_nop 0\n" \
    "s_nop 0\n" \
    "s_nop 0\n" \
    "s_nop 0\n" \
    "s_nop 0\n" \
    "s_nop 0\n" \
    "s_nop 0\n" \
    "s_nop 0\n" \
    "s_nop 0\n" \
    "s_nop 0\n" \
    "s_nop 0\n" \
    "s_nop 0\n" \
    "s_nop 0\n" \
    "s_nop 0\n" \
    "s_nop 0\n" \
    "s_nop 0\n" \
    "s_nop 0\n" \
    "s_nop 0\n" \
    "s_nop 0\n" \
    "s_nop 0\n" \
    "s_nop 0\n" \
    "s_nop 0\n" \
    "s_nop 0\n" \
    "s_nop 0\n" \
    "s_nop 0\n" \
    "s_nop 0\n" \
    "s_nop 0\n" \
    "s_nop 0\n" \
    "s_nop 0\n" \
    "s_nop 0\n" \
    "s_nop 0\n" \
    "RHSHOT%=:\n" \
    "global_load_dwordx4 v[28:31], v0, s[58:59] offset:-2048 sc1\n" \
    "global_load_dwordx4 v[32:35], v0, s[58:59] offset:2048 sc1\n" \
    "s_cmp_ge_u32 s41, 2\n" \
    "s_cbranch_scc1 RHL1%=\n" \
    "global_load_dwordx4 v[36:39], v0, s[60:61] sc1\n" \
    "RHL1%=:\n" \
    "s_cbranch_scc1 RH2A%=\n" \
    "s_waitcnt vmcnt(2)\n" \
    "v_smfmac_f32_16x16x64_bf16 v[208:211], v[28:31], a[0:7], v1\n" \
    "v_smfmac_f32_16x16x64_bf16 v[212:215], v[28:31], a[8:15], v1\n" \
    "v_smfmac_f32_16x16x64_bf16 v[216:219], v[28:31], a[16:23], v1\n" \
    "v_smfmac_f32_16x16x64_bf16 v[220:223], v[28:31], a[24:31], v1\n" \
    "s_waitcnt vmcnt(1)\n" \
    "v_smfmac_f32_16x16x64_bf16 v[208:211], v[32:35], a[32:39], v1\n" \
    "v_smfmac_f32_16x16x64_bf16 v[212:215], v[32:35], a[40:47], v1\n" \
    "v_smfmac_f32_16x16x64_bf16 v[216:219], v[32:35], a[48:55], v1\n" \
    "v_smfmac_f32_16x16x64_bf16 v[220:223], v[32:35], a[56:63], v1\n" \
    "s_waitcnt vmcnt(0)\n" \
    "v_smfmac_f32_16x16x64_bf16 v[208:211], v[36:39], a[64:71], v1\n" \
    "v_smfmac_f32_16x16x64_bf16 v[212:215], v[36:39], a[72:79], v1\n" \
    "v_smfmac_f32_16x16x64_bf16 v[216:219], v[36:39], a[80:87], v1\n" \
    "v_smfmac_f32_16x16x64_bf16 v[220:223], v[36:39], a[88:95], v1\n" \
    "s_branch RH2%=\n" \
    "RH2A%=:\n" \
    "s_waitcnt vmcnt(1)\n" \
    "v_smfmac_f32_16x16x64_bf16 v[208:211], v[28:31], a[0:7], v1\n" \
    "v_smfmac_f32_16x16x64_bf16 v[212:215], v[28:31], a[8:15], v1\n" \
    "v_smfmac_f32_16x16x64_bf16 v[216:219], v[28:31], a[16:23], v1\n" \
    "v_smfmac_f32_16x16x64_bf16 v[220:223], v[28:31], a[24:31], v1\n" \
    "s_waitcnt vmcnt(0)\n" \
    "v_smfmac_f32_16x16x64_bf16 v[208:211], v[32:35], a[32:39], v1\n" \
    "v_smfmac_f32_16x16x64_bf16 v[212:215], v[32:35], a[40:47], v1\n" \
    "v_smfmac_f32_16x16x64_bf16 v[216:219], v[32:35], a[48:55], v1\n" \
    "v_smfmac_f32_16x16x64_bf16 v[220:223], v[32:35], a[56:63], v1\n" \
    "RH2%=:\n" \
    "s_nop 4\n" \
    "v_pk_add_f32 v[208:209], v[208:209], v[210:211]\n" \
    "v_pk_add_f32 v[212:213], v[212:213], v[214:215]\n" \
    "v_pk_add_f32 v[216:217], v[216:217], v[218:219]\n" \
    "v_pk_add_f32 v[220:221], v[220:221], v[222:223]\n" \
    "ds_write_b64 v8, v[208:209] offset:0\n" \
    "ds_write_b64 v8, v[212:213] offset:512\n" \
    "ds_write_b64 v8, v[216:217] offset:1024\n" \
    "ds_write_b64 v8, v[220:221] offset:1536\n" \
    "v_cmp_u_f32 vcc, v208, v209\n" \
    "s_cmp_ge_u32 s41, 2\n" \
    "s_and_b64 vcc, vcc, s[86:87]\n" \
    "s_cbranch_vccnz RHSLOW%=\n" \
    "s_cmp_ge_u32 s41, 2\n" \
    "s_waitcnt lgkmcnt(0)\n" \
    "s_barrier\n" \
    "s_cbranch_scc1 RH3%=\n" \
    "ds_read2st64_b64 v[28:31], v9 offset0:0 offset1:1\n" \
    "ds_read2st64_b64 v[32:35], v9 offset0:4 offset1:5\n" \
    "ds_read2st64_b64 v[36:39], v9 offset0:8 offset1:9\n" \
    "ds_read2st64_b64 v[40:43], v9 offset0:12 offset1:13\n" \
    "s_waitcnt lgkmcnt(2)\n" \
    "v_pk_add_f32 v[28:29], v[28:29], v[32:33]\n" \
    "v_pk_add_f32 v[30:31], v[30:31], v[34:35]\n" \
    "s_waitcnt lgkmcnt(0)\n" \
    "v_pk_add_f32 v[36:37], v[36:37], v[40:41]\n" \
    "v_pk_add_f32 v[38:39], v[38:39], v[42:43]\n" \
    "v_pk_add_f32 v[28:29], v[28:29], v[36:37]\n" \
    "v_pk_add_f32 v[30:31], v[30:31], v[38:39]\n" \
    "v_cvt_pk_bf16_f32 v24, v28, v29\n" \
    "v_cvt_pk_bf16_f32 v25, v30, v31\n" \
    "global_store_dwordx2 v10, v[24:25], s[62:63] sc1\n" \
    "global_store_dwordx2 v10, v[24:25], s[68:69] sc1\n" \
    "v_mov_b32 v26, s78\n" \
    "v_mov_b32 v27, s78\n" \
    "global_store_dwordx2 v10, v[26:27], s[64:65] sc1\n" \
    "RH3%=:\n" \
    "RRED%=:\n" \
    "s_cmp_ge_u32 s43, 63\n" \
    "s_cbranch_scc1 REND%=\n" \
    "s_and_b32 s66, s43, 3\n" \
    "s_lshl_b32 s67, s66, 17\n" \
    "s_add_u32 s50, s36, s67\n" \
    "s_addc_u32 s51, s37, 0\n" \
    "s_lshr_b32 s67, s43, 1\n" \
    "s_lshl_b32 s67, s67, 14\n" \
    "s_and_b32 s66, s43, 1\n" \
    "s_lshl_b32 s66, s66, 12\n" \
    "s_add_u32 s67, s67, s66\n" \
    "s_add_u32 s67, s67, 1572864\n" \
    "s_add_u32 s56, s36, s67\n" \
    "s_addc_u32 s57, s37, 0\n" \
    "s_add_u32 s66, s43, 1\n" \
    "s_and_b32 s66, s66, 3\n" \
    "s_lshl_b32 s67, s66, 16\n" \
    "s_add_u32 s67, s67, 540672\n" \
    "s_sub_u32 s67, s67, s99\n" \
    "s_add_u32 s52, s36, s67\n" \
    "s_addc_u32 s53, s37, 0\n" \
    "s_xor_b32 s67, s67, 131072\n" \
    "s_add_u32 s54, s36, s67\n" \
    "s_addc_u32 s55, s37, 0\n" \
    "v_mov_b64 v[12:13], 0\n" \
    "v_mov_b64 v[14:15], 0\n" \
    "v_mov_b64 v[16:17], 0\n" \
    "v_mov_b64 v[18:19], 0\n" \
    "s_mov_b64 s[86:87], -1\n" \
    "s_cmp_eq_u32 s43, 0\n" \
    "s_cbranch_scc1 RSL0%=\n" \
    "s_mov_b32 s70, 0\n" \
    "s_add_u32 s67, s43, 1\n" \
    "s_lshl_b32 s66, s43, 7\n" \
    "s_add_u32 s66, s66, 786432\n" \
    "v_mov_b32 v6, s66\n" \
    "RPW%=:\n" \
    "global_load_dwordx2 v[20:21], v6, s[36:37] sc1\n" \
    "s_waitcnt vmcnt(0)\n" \
    "v_readfirstlane_b32 s46, v20\n" \
    "v_readfirstlane_b32 s66, v21\n" \
    "s_lshr_b32 s47, s66, 8\n" \
    "s_and_b32 s66, s66, 255\n" \
    "s_cmp_eq_u32 s66, s67\n" \
    "s_cbranch_scc1 RPD%=\n" \
    "s_sleep 2\n" \
    "s_add_u32 s70, s70, 1\n" \
    "s_or_b32 s66, s70, s71\n" \
    "s_cmp_lt_u32 s66, 65536\n" \
    "s_cbranch_scc1 RPW%=\n" \
    "s_mov_b32 s71, 65536\n" \
    "s_mov_b32 s46, s73\n" \
    "s_mov_b32 s47, s45\n" \
    "RPD%=:\n" \
    "s_sub_u32 s44, s47, s45\n" \
    "s_add_u32 s47, s46, s45\n" \
    "s_cmp_ge_u32 s40, 80\n" \
    "s_cbranch_scc1 RNP%=\n" \
    "s_mov_b32 s79, 0\n" \
    "RW1%=:\n" \
    "s_sub_u32 s66, s47, 192\n" \
    "s_lshr_b32 s66, s66, 5\n" \
    "s_memrealtime s[28:29]\n" \
    "RW1L%=:\n" \
    "s_memrealtime s[30:31]\n" \
    "s_waitcnt lgkmcnt(1)\n" \
    "s_sub_u32 s67, s28, s66\n" \
    "s_bitcmp0_b32 s67, 26\n" \
    "s_cbranch_scc1 RW1L%=X\n" \
    "s_memrealtime s[28:29]\n" \
    "s_waitcnt lgkmcnt(1)\n" \
    "s_sub_u32 s67, s30, s66\n" \
    "s_bitcmp0_b32 s67, 26\n" \
    "s_cbranch_scc1 RW1L%=X\n" \
    "s_memrealtime s[30:31]\n" \
    "s_waitcnt lgkmcnt(1)\n" \
    "s_sub_u32 s67, s28, s66\n" \
    "s_bitcmp0_b32 s67, 26\n" \
    "s_cbranch_scc1 RW1L%=X\n" \
    "s_memrealtime s[28:29]\n" \
    "s_waitcnt lgkmcnt(1)\n" \
    "s_sub_u32 s67, s30, s66\n" \
    "s_bitcmp0_b32 s67, 26\n" \
    "s_cbranch_scc1 RW1L%=X\n" \
    "s_memrealtime s[30:31]\n" \
    "s_waitcnt lgkmcnt(1)\n" \
    "s_sub_u32 s67, s28, s66\n" \
    "s_bitcmp0_b32 s67, 26\n" \
    "s_cbranch_scc1 RW1L%=X\n" \
    "s_memrealtime s[28:29]\n" \
    "s_waitcnt lgkmcnt(1)\n" \
    "s_sub_u32 s67, s30, s66\n" \
    "s_bitcmp1_b32 s67, 26\n" \
    "s_cbranch_scc1 RW1L%=\n" \
    "RW1L%=X:\n" \
    "s_add_u32 s67, s67, s66\n" \
    "s_lshl_b32 s67, s67, 5\n" \
    "s_sub_u32 s67, s47, s67\n" \
    "s_sub_i32 s67, s67, 96\n" \
    "s_max_i32 s67, s67, 0\n" \
    "s_mul_i32 s67, s67, 3\n" \
    "s_lshr_b32 s67, s67, 2\n" \
    "s_and_b32 s67, s67, 0x7c\n" \
    "s_sub_u32 s67, 140, s67\n" \
    "s_waitcnt lgkmcnt(0)\n" \
    "s_getpc_b64 s[28:29]\n" \
    "s_add_u32 s28, s28, s67\n" \
    "s_addc_u32 s29, s29, 0\n" \
    "s_setpc_b64 s[28:29]\n" \
    "s_nop 0\n" \
    "s_nop 0\n" \
    "s_nop 0\n" \
    "s_nop 0\n" \
    "s_nop 0\n" \
    "s_nop 0\n" \
    "s_nop 0\n" \
    "s_nop 0\n" \
    "s_nop 0\n" \
    "s_nop 0\n" \
    "s_nop 0\n" \
    "s_nop 0\n" \
    "s_nop 0\n" \
    "s_nop 0\n" \
    "s_nop 0\n" \
    "s_nop 0\n" \
    "s_nop 0\n" \
    "s_nop 0\n" \
    "s_nop 0\n" \
    "s_nop 0\n" \
    "s_nop 0\n" \
    "s_nop 0\n" \
    "s_nop 0\n" \
    "s_nop 0\n" \
    "s_nop 0\n" \
    "s_nop 0\n" \
    "s_nop 0\n" \
    "s_nop 0\n" \
    "s_nop 0\n" \
    "s_nop 0\n" \
    "s_nop 0\n" \
    "s_nop 0\n" \
    "RSHOT%=:\n" \
    "global_load_dwordx4 v[28:31], v224, s[50:51] sc1\n" \
    "s_nop 15\n" \
    "global_load_dwordx4 v[32:35], v225, s[50:51] sc1\n" \
    "s_nop 15\n" \
    "global_load_dwordx4 v[36:39], v226, s[50:51] sc1\n" \
    "s_nop 15\n" \
    "global_load_dwordx4 v[40:43], v227, s[50:51] sc1\n" \
    "s_nop 15\n" \
    "global_load_dwordx4 v[44:47], v228, s[50:51] sc1\n" \
    "s_nop 15\n" \
    "global_load_dwordx4 v[48:51], v229, s[50:51] sc1\n" \
    "s_nop 15\n" \
    "global_load_dwordx4 v[52:55], v230, s[50:51] sc1\n" \
    "s_waitcnt vmcnt(6)\n" \
    "global_load_dwordx4 v[56:59], v231, s[50:51] sc1\n" \
    "v_smfmac_f32_16x16x64_bf16 v[12:15], v[28:31], v[64:71], v1\n" \
    "v_smfmac_f32_16x16x64_bf16 v[16:19], v[28:31], v[72:79], v1\n" \
    "s_waitcnt vmcnt(6)\n" \
    "v_smfmac_f32_16x16x64_bf16 v[12:15], v[32:35], v[80:87], v1\n" \
    "v_smfmac_f32_16x16x64_bf16 v[16:19], v[32:35], v[88:95], v1\n" \
    "s_waitcnt vmcnt(5)\n" \
    "v_smfmac_f32_16x16x64_bf16 v[12:15], v[36:39], v[96:103], v1\n" \
    "v_smfmac_f32_16x16x64_bf16 v[16:19], v[36:39], v[104:111], v1\n" \
    "s_waitcnt vmcnt(4)\n" \
    "v_smfmac_f32_16x16x64_bf16 v[12:15], v[40:43], v[112:119], v1\n" \
    "v_smfmac_f32_16x16x64_bf16 v[16:19], v[40:43], v[120:127], v1\n" \
    "s_waitcnt vmcnt(3)\n" \
    "v_smfmac_f32_16x16x64_bf16 v[12:15], v[44:47], v[128:135], v1\n" \
    "v_smfmac_f32_16x16x64_bf16 v[16:19], v[44:47], v[136:143], v1\n" \
    "s_waitcnt vmcnt(2)\n" \
    "v_smfmac_f32_16x16x64_bf16 v[12:15], v[48:51], v[144:151], v1\n" \
    "v_smfmac_f32_16x16x64_bf16 v[16:19], v[48:51], v[152:159], v1\n" \
    "s_waitcnt vmcnt(1)\n" \
    "v_smfmac_f32_16x16x64_bf16 v[12:15], v[52:55], v[160:167], v1\n" \
    "v_smfmac_f32_16x16x64_bf16 v[16:19], v[52:55], v[168:175], v1\n" \
    "s_waitcnt vmcnt(0)\n" \
    "v_smfmac_f32_16x16x64_bf16 v[12:15], v[56:59], v[176:183], v1\n" \
    "v_smfmac_f32_16x16x64_bf16 v[16:19], v[56:59], v[184:191], v1\n" \
    "s_nop 6\n" \
    "v_add_f32 v12, v12, v14\n" \
    "v_add_f32 v17, v17, v19\n" \
    "v_add_f32 v12, v12, v17\n" \
    "ds_write_b32 v3, v12\n" \
    "v_cmp_u_f32 vcc, v12, v12\n" \
    "s_and_b64 vcc, vcc, s[86:87]\n" \
    "s_cbranch_vccnz RSLOW%=\n" \
    "s_cmp_lg_u32 s41, 0\n" \
    "s_waitcnt lgkmcnt(0)\n" \
    "s_barrier\n" \
    "s_cbranch_scc1 RNP%=\n" \
    "ds_read_b128 v[28:31], v4\n" \
    "ds_read_b128 v[32:35], v4 offset:16\n" \
    "s_mov_b32 exec_hi, 0\n" \
    "s_waitcnt lgkmcnt(1)\n" \
    "v_pk_add_f32 v[28:29], v[28:29], v[30:31]\n" \
    "s_waitcnt lgkmcnt(0)\n" \
    "v_pk_add_f32 v[32:33], v[32:33], v[34:35]\n" \
    "v_add_f32 v28, v28, v29\n" \
    "v_add_f32 v32, v32, v33\n" \
    "v_cvt_pk_bf16_f32 v28, v28, v32\n" \
    "global_store_dword v5, v28, s[52:53] sc1\n" \
    "v_mov_b32 v29, s78\n" \
    "global_store_dword v5, v29, s[54:55] sc1\n" \
    "s_mov_b64 exec, -1\n" \
    "RNP%=:\n" \
    "s_add_u32 s73, s47, s44\n" \
    "s_add_u32 s43, s43, 1\n" \
    "s_branch RSTEP%=\n" \
    "RSLOW%=:\n" \
    "s_mov_b64 exec, -1\n" \
    "s_add_u32 s82, s82, 1\n" \
    "s_add_u32 s79, s79, 1\n" \
    "s_cmp_gt_u32 s79, 6\n" \
    "s_cbranch_scc1 RSLW2%=\n" \
    "s_waitcnt lgkmcnt(0)\n" \
    "v_mov_b64 v[12:13], 0\n" \
    "v_mov_b64 v[14:15], 0\n" \
    "v_mov_b64 v[16:17], 0\n" \
    "v_mov_b64 v[18:19], 0\n" \
    "s_branch RSL1%=\n" \
    "RSL0%=:\n" \
    "s_mov_b32 s79, 0\n" \
    "RSL1%=:\n" \
    "s_add_u32 s72, s46, 2304\n" \
    "s_lshr_b32 s72, s72, 5\n" \
    "RNW%=:\n" \
    "s_memrealtime s[28:29]\n" \
    "s_waitcnt lgkmcnt(0)\n" \
    "s_sub_u32 s66, s28, s72\n" \
    "s_bitcmp1_b32 s66, 26\n" \
    "s_cbranch_scc1 RNW%=\n" \
    "s_mov_b32 s70, 0\n" \
    "s_add_u32 s67, s43, 1\n" \
    "RT2L%=:\n" \
    "global_load_dwordx2 v[20:21], v2, s[56:57] sc1\n" \
    "s_waitcnt vmcnt(0)\n" \
    "RT2C%=:\n" \
    "v_cmp_ne_u32 vcc, s67, v21\n" \
    "v_subrev_u32 v20, s73, v20\n" \
    "s_cbranch_vccz RT2D%=\n" \
    "s_sleep 2\n" \
    "s_add_u32 s70, s70, 1\n" \
    "s_or_b32 s66, s70, s71\n" \
    "s_cmp_lt_u32 s66, 65536\n" \
    "s_cbranch_scc1 RT2L%=\n" \
    "s_mov_b32 s71, 65536\n" \
    "v_mov_b32 v20, 0\n" \
    "RT2D%=:\n" \
    "s_nop 1\n" \
    "v_max_i32_dpp v20, v20, v20 row_ror:1 row_mask:0xf bank_mask:0xf\n" \
    "s_nop 1\n" \
    "v_max_i32_dpp v20, v20, v20 row_ror:2 row_mask:0xf bank_mask:0xf\n" \
    "s_nop 1\n" \
    "v_max_i32_dpp v20, v20, v20 row_ror:4 row_mask:0xf bank_mask:0xf\n" \
    "s_nop 1\n" \
    "v_max_i32_dpp v20, v20, v20 row_ror:8 row_mask:0xf bank_mask:0xf\n" \
    "v_mov_b32 v21, v20\n" \
    "s_nop 1\n" \
    "v_permlane16_swap_b32 v21, v20\n" \
    "v_max_i32 v20, v21, v20\n" \
    "s_nop 1\n" \
    "v_readfirstlane_b32 s66, v20\n" \
    "s_add_u32 s66, s66, s73\n" \
    "s_sub_i32 s67, s66, s46\n" \
    "s_cmp_eq_u32 s43, 0\n" \
    "s_movk_i32 s70, 2240\n" \
    "s_cselect_b32 s70, 3840, s70\n" \
    "s_add_u32 s70, s70, s66\n" \
    "s_sub_u32 s70, s70, s46\n" \
    "s_max_i32 s70, s70, 3840\n" \
    "s_add_u32 s66, s46, s70\n" \
    "s_cmp_lt_i32 s67, 96\n" \
    "s_cselect_b32 s66, s46, s66\n" \
    "s_add_u32 s47, s66, s45\n" \
    "s_cmp_ge_u32 s40, 80\n" \
    "s_cbranch_scc1 RNP%=\n" \
    "s_branch RW1%=\n" \
    "RSLW2%=:\n" \
    "s_mov_b32 s70, 0\n" \
    "ROP%=:\n" \
    "global_load_dwordx4 v[28:31], v224, s[50:51] sc1\n" \
    "global_load_dwordx4 v[32:35], v225, s[50:51] sc1\n" \
    "global_load_dwordx4 v[36:39], v226, s[50:51] sc1\n" \
    "global_load_dwordx4 v[40:43], v227, s[50:51] sc1\n" \
    "global_load_dwordx4 v[44:47], v228, s[50:51] sc1\n" \
    "global_load_dwordx4 v[48:51], v229, s[50:51] sc1\n" \
    "global_load_dwordx4 v[52:55], v230, s[50:51] sc1\n" \
    "global_load_dwordx4 v[56:59], v231, s[50:51] sc1\n" \
    "s_waitcnt vmcnt(0)\n" \
    "v_mov_b32 v6, -1\n" \
    "v_xor_b32 v7, s78, v28\n" \
    "v_min_u32 v6, v6, v7\n" \
    "v_xor_b32 v7, s78, v29\n" \
    "v_min_u32 v6, v6, v7\n" \
    "v_xor_b32 v7, s78, v30\n" \
    "v_min_u32 v6, v6, v7\n" \
    "v_xor_b32 v7, s78, v31\n" \
    "v_min_u32 v6, v6, v7\n" \
    "v_xor_b32 v7, s78, v32\n" \
    "v_min_u32 v6, v6, v7\n" \
    "v_xor_b32 v7, s78, v33\n" \
    "v_min_u32 v6, v6, v7\n" \
    "v_xor_b32 v7, s78, v34\n" \
    "v_min_u32 v6, v6, v7\n" \
    "v_xor_b32 v7, s78, v35\n" \
    "v_min_u32 v6, v6, v7\n" \
    "v_xor_b32 v7, s78, v36\n" \
    "v_min_u32 v6, v6, v7\n" \
    "v_xor_b32 v7, s78, v37\n" \
    "v_min_u32 v6, v6, v7\n" \
    "v_xor_b32 v7, s78, v38\n" \
    "v_min_u32 v6, v6, v7\n" \
    "v_xor_b32 v7, s78, v39\n" \
    "v_min_u32 v6, v6, v7\n" \
    "v_xor_b32 v7, s78, v40\n" \
    "v_min_u32 v6, v6, v7\n" \
    "v_xor_b32 v7, s78, v41\n" \
    "v_min_u32 v6, v6, v7\n" \
    "v_xor_b32 v7, s78, v42\n" \
    "v_min_u32 v6, v6, v7\n" \
    "v_xor_b32 v7, s78, v43\n" \
    "v_min_u32 v6, v6, v7\n" \
    "v_xor_b32 v7, s78, v44\n" \
    "v_min_u32 v6, v6, v7\n" \
    "v_xor_b32 v7, s78, v45\n" \
    "v_min_u32 v6, v6, v7\n" \
    "v_xor_b32 v7, s78, v46\n" \
    "v_min_u32 v6, v6, v7\n" \
    "v_xor_b32 v7, s78, v47\n" \
    "v_min_u32 v6, v6, v7\n" \
    "v_xor_b32 v7, s78, v48\n" \
    "v_min_u32 v6, v6, v7\n" \
    "v_xor_b32 v7, s78, v49\n" \
    "v_min_u32 v6, v6, v7\n" \
    "v_xor_b32 v7, s78, v50\n" \
    "v_min_u32 v6, v6, v7\n" \
    "v_xor_b32 v7, s78, v51\n" \
    "v_min_u32 v6, v6, v7\n" \
    "v_xor_b32 v7, s78, v52\n" \
    "v_min_u32 v6, v6, v7\n" \
    "v_xor_b32 v7, s78, v53\n" \
    "v_min_u32 v6, v6, v7\n" \
    "v_xor_b32 v7, s78, v54\n" \
    "v_min_u32 v6, v6, v7\n" \
    "v_xor_b32 v7, s78, v55\n" \
    "v_min_u32 v6, v6, v7\n" \
    "v_xor_b32 v7, s78, v56\n" \
    "v_min_u32 v6, v6, v7\n" \
    "v_xor_b32 v7, s78, v57\n" \
    "v_min_u32 v6, v6, v7\n" \
    "v_xor_b32 v7, s78, v58\n" \
    "v_min_u32 v6, v6, v7\n" \
    "v_xor_b32 v7, s78, v59\n" \
    "v_min_u32 v6, v6, v7\n" \
    "v_cmp_eq_u32 vcc, 0, v6\n" \
    "s_cbranch_vccz ROK%=\n" \
    "s_sleep 2\n" \
    "s_add_u32 s70, s70, 1\n" \
    "s_or_b32 s66, s70, s71\n" \
    "s_cmp_lt_u32 s66, 65536\n" \
    "s_cbranch_scc1 ROP%=\n" \
    "s_mov_b32 s71, 65536\n" \
    "ROK%=:\n" \
    "s_waitcnt lgkmcnt(0)\n" \
    "v_mov_b64 v[12:13], 0\n" \
    "v_mov_b64 v[14:15], 0\n" \
    "v_mov_b64 v[16:17], 0\n" \
    "v_mov_b64 v[18:19], 0\n" \
    "s_mov_b64 s[86:87], 0\n" \
    "s_nop 1\n" \
    "s_branch RSHOT%=\n" \
    "RHSLOW%=:\n" \
    "s_add_u32 s83, s83, 1\n" \
    "s_add_u32 s79, s79, 1\n" \
    "s_cmp_gt_u32 s79, 6\n" \
    "s_cbranch_scc1 RHLW2%=\n" \
    "s_waitcnt lgkmcnt(0)\n" \
    "v_mov_b64 v[208:209], 0\n" \
    "v_mov_b64 v[210:211], 0\n" \
    "v_mov_b64 v[212:213], 0\n" \
    "v_mov_b64 v[214:215], 0\n" \
    "v_mov_b64 v[216:217], 0\n" \
    "v_mov_b64 v[218:219], 0\n" \
    "v_mov_b64 v[220:221], 0\n" \
    "v_mov_b64 v[222:223], 0\n" \
    "s_cmp_eq_u32 s43, 0\n" \
    "s_cbranch_scc1 RHSHOT%=\n" \
    "s_cmp_eq_u32 s79, 1\n" \
    "s_cbranch_scc1 RHSHOT%=\n" \
    "s_mov_b32 s70, 0\n" \
    "s_add_u32 s67, s43, 0\n" \
    "RT3L%=:\n" \
    "global_load_dwordx2 v[20:21], v2, s[56:57] sc1\n" \
    "s_waitcnt vmcnt(0)\n" \
    "RT3C%=:\n" \
    "v_cmp_ne_u32 vcc, s67, v21\n" \
    "v_subrev_u32 v20, s73, v20\n" \
    "s_cbranch_vccz RT3D%=\n" \
    "s_sleep 2\n" \
    "s_add_u32 s70, s70, 1\n" \
    "s_or_b32 s66, s70, s71\n" \
    "s_cmp_lt_u32 s66, 65536\n" \
    "s_cbranch_scc1 RT3L%=\n" \
    "s_mov_b32 s71, 65536\n" \
    "v_mov_b32 v20, 0\n" \
    "RT3D%=:\n" \
    "s_nop 1\n" \
    "v_max_i32_dpp v20, v20, v20 row_ror:1 row_mask:0xf bank_mask:0xf\n" \
    "s_nop 1\n" \
    "v_max_i32_dpp v20, v20, v20 row_ror:2 row_mask:0xf bank_mask:0xf\n" \
    "s_nop 1\n" \
    "v_max_i32_dpp v20, v20, v20 row_ror:4 row_mask:0xf bank_mask:0xf\n" \
    "s_nop 1\n" \
    "v_max_i32_dpp v20, v20, v20 row_ror:8 row_mask:0xf bank_mask:0xf\n" \
    "v_mov_b32 v21, v20\n" \
    "s_nop 1\n" \
    "v_permlane16_swap_b32 v21, v20\n" \
    "v_max_i32 v20, v21, v20\n" \
    "s_nop 1\n" \
    "v_readfirstlane_b32 s66, v20\n" \
    "s_add_u32 s66, s66, s73\n" \
    "s_sub_i32 s67, s66, s46\n" \
    "s_cmp_eq_u32 s43, 1\n" \
    "s_movk_i32 s70, 2240\n" \
    "s_cselect_b32 s70, 3840, s70\n" \
    "s_add_u32 s70, s70, s66\n" \
    "s_sub_u32 s70, s70, s46\n" \
    "s_max_i32 s70, s70, 3840\n" \
    "s_add_u32 s66, s46, s70\n" \
    "s_cmp_lt_i32 s67, 96\n" \
    "s_cselect_b32 s66, s46, s66\n" \
    "s_add_u32 s73, s66, s45\n" \
    "s_add_u32 s73, s73, s44\n" \
    "s_branch RWH%=\n" \
    "RHLW2%=:\n" \
    "v_mov_b32 v36, 0\n" \
    "v_mov_b32 v37, 0\n" \
    "v_mov_b32 v38, 0\n" \
    "v_mov_b32 v39, 0\n" \
    "s_mov_b32 s70, 0\n" \
    "RHP%=:\n" \
    "global_load_dwordx4 v[28:31], v0, s[58:59] offset:-2048 sc1\n" \
    "global_load_dwordx4 v[32:35], v0, s[58:59] offset:2048 sc1\n" \
    "s_cmp_ge_u32 s41, 2\n" \
    "s_cbranch_scc1 RHL2%=\n" \
    "global_load_dwordx4 v[36:39], v0, s[60:61] sc1\n" \
    "RHL2%=:\n" \
    "s_waitcnt vmcnt(0)\n" \
    "v_mov_b32 v6, -1\n" \
    "v_xor_b32 v7, s78, v28\n" \
    "v_min_u32 v6, v6, v7\n" \
    "v_xor_b32 v7, s78, v29\n" \
    "v_min_u32 v6, v6, v7\n" \
    "v_xor_b32 v7, s78, v30\n" \
    "v_min_u32 v6, v6, v7\n" \
    "v_xor_b32 v7, s78, v31\n" \
    "v_min_u32 v6, v6, v7\n" \
    "v_xor_b32 v7, s78, v32\n" \
    "v_min_u32 v6, v6, v7\n" \
    "v_xor_b32 v7, s78, v33\n" \
    "v_min_u32 v6, v6, v7\n" \
    "v_xor_b32 v7, s78, v34\n" \
    "v_min_u32 v6, v6, v7\n" \
    "v_xor_b32 v7, s78, v35\n" \
    "v_min_u32 v6, v6, v7\n" \
    "v_xor_b32 v7, s78, v36\n" \
    "v_min_u32 v6, v6, v7\n" \
    "v_xor_b32 v7, s78, v37\n" \
    "v_min_u32 v6, v6, v7\n" \
    "v_xor_b32 v7, s78, v38\n" \
    "v_min_u32 v6, v6, v7\n" \
    "v_xor_b32 v7, s78, v39\n" \
    "v_min_u32 v6, v6, v7\n" \
    "v_cmp_eq_u32 vcc, 0, v6\n" \
    "s_cbranch_vccz RHK%=\n" \
    "s_sleep 2\n" \
    "s_add_u32 s70, s70, 1\n" \
    "s_or_b32 s66, s70, s71\n" \
    "s_cmp_lt_u32 s66, 65536\n" \
    "s_cbranch_scc1 RHP%=\n" \
    "s_mov_b32 s71, 65536\n" \
    "RHK%=:\n" \
    "s_waitcnt lgkmcnt(0)\n" \
    "v_mov_b64 v[208:209], 0\n" \
    "v_mov_b64 v[210:211], 0\n" \
    "v_mov_b64 v[212:213], 0\n" \
    "v_mov_b64 v[214:215], 0\n" \
    "v_mov_b64 v[216:217], 0\n" \
    "v_mov_b64 v[218:219], 0\n" \
    "v_mov_b64 v[220:221], 0\n" \
    "v_mov_b64 v[222:223], 0\n" \
    "s_mov_b64 s[86:87], 0\n" \
    "s_nop 1\n" \
    "s_branch RHSHOT%=\n" \
    "REND%=:\n" \
    "s_waitcnt vmcnt(0)\n" \
    "s_cmp_ge_u32 s98, 64\n" \
    "s_cbranch_scc1 RFIN%=\n" \
    "s_cmp_ge_u32 s41, 2\n" \
    "s_cbranch_scc1 RFIN%=\n" \
    "s_lshr_b32 s98, s98, 1\n" \
    "s_mov_b32 s70, %11\n" \
    "s_lshl_b32 s66, s70, 5\n" \
    "s_add_u32 s66, s66, s98\n" \
    "s_lshl_b32 s66, s66, 7\n" \
    "s_add_u32 s66, s66, 67239936\n" \
    "v_mov_b32 v6, s66\n" \
    "v_mov_b32 v7, 1\n" \
    "s_mov_b64 exec, 1\n" \
    "global_atomic_add v6, v7, s[38:39] sc1\n" \
    "s_mov_b64 exec, -1\n" \
    "RFIN%=:\n" \
    ""
#define CLOB_V "v0", "v1", "v2", "v3", "v4", "v5", "v6", "v7", "v8", "v9", "v10", "v11", "v12", "v13", "v14", "v15", "v16", "v17", "v18", "v19", "v20", "v21", "v22", "v23", "v24", "v25", "v26", "v27", "v28", "v29", "v30", "v31", "v32", "v33", "v34", "v35", "v36", "v37", "v38", "v39", "v40", "v41", "v42", "v43", "v44", "v45", "v46", "v47", "v48", "v49", "v50", "v51", "v52", "v53", "v54", "v55", "v56", "v57", "v58", "v59", "v60", "v61", "v62", "v63", "v64", "v65", "v66", "v67", "v68", "v69", "v70", "v71", "v72", "v73", "v74", "v75", "v76", "v77", "v78", "v79", "v80", "v81", "v82", "v83", "v84", "v85", "v86", "v87", "v88", "v89", "v90", "v91", "v92", "v93", "v94", "v95", "v96", "v97", "v98", "v99", "v100", "v101", "v102", "v103", "v104", "v105", "v106", "v107", "v108", "v109", "v110", "v111", "v112", "v113", "v114", "v115", "v116", "v117", "v118", "v119", "v120", "v121", "v122", "v123", "v124", "v125", "v126", "v127", "v128", "v129", "v130", "v131", "v132", "v133", "v134", "v135", "v136", "v137", "v138", "v139", "v140", "v141", "v142", "v143", "v144", "v145", "v146", "v147", "v148", "v149", "v150", "v151", "v152", "v153", "v154", "v155", "v156", "v157", "v158", "v159", "v160", "v161", "v162", "v163", "v164", "v165", "v166", "v167", "v168", "v169", "v170", "v171", "v172", "v173", "v174", "v175", "v176", "v177", "v178", "v179", "v180", "v181", "v182", "v183", "v184", "v185", "v186", "v187", "v188", "v189", "v190", "v191", "v192", "v193", "v194", "v195", "v196", "v197", "v198", "v199", "v200", "v201", "v202", "v203", "v204", "v205", "v206", "v207", "v208", "v209", "v210", "v211", "v212", "v213", "v214", "v215", "v216", "v217", "v218", "v219", "v220", "v221", "v222", "v223", "v224", "v225", "v226", "v227", "v228", "v229", "v230", "v231", "v232", "v233", "v234", "v235", "v236", "v237", "v238", "v239", "v240", "v241", "v242", "v243", "v244", "v245", "v246", "v247"
#define CLOB_A "a0", "a1", "a2", "a3", "a4", "a5", "a6", "a7", "a8", "a9", "a10", "a11", "a12", "a13", "a14", "a15", "a16", "a17", "a18", "a19", "a20", "a21", "a22", "a23", "a24", "a25", "a26", "a27", "a28", "a29", "a30", "a31", "a32", "a33", "a34", "a35", "a36", "a37", "a38", "a39", "a40", "a41", "a42", "a43", "a44", "a45", "a46", "a47", "a48", "a49", "a50", "a51", "a52", "a53", "a54", "a55", "a56", "a57", "a58", "a59", "a60", "a61", "a62", "a63", "a64", "a65", "a66", "a67", "a68", "a69", "a70", "a71", "a72", "a73", "a74", "a75", "a76", "a77", "a78", "a79", "a80", "a81", "a82", "a83", "a84", "a85", "a86", "a87", "a88", "a89", "a90", "a91", "a92", "a93", "a94", "a95", "a96", "a97", "a98", "a99", "a100", "a101", "a102", "a103", "a104", "a105", "a106", "a107", "a108", "a109", "a110", "a111", "a112", "a113", "a114", "a115", "a116", "a117", "a118", "a119", "a120", "a121", "a122", "a123", "a124", "a125", "a126", "a127", "a128", "a129", "a130", "a131", "a132", "a133", "a134", "a135", "a136", "a137", "a138", "a139", "a140", "a141", "a142", "a143", "a144", "a145", "a146", "a147", "a148", "a149", "a150", "a151", "a152", "a153", "a154", "a155", "a156", "a157", "a158", "a159", "a160", "a161", "a162", "a163", "a164", "a165", "a166", "a167", "a168", "a169", "a170", "a171", "a172", "a173", "a174", "a175", "a176", "a177", "a178", "a179", "a180", "a181", "a182", "a183", "a184", "a185", "a186", "a187", "a188", "a189", "a190", "a191", "a192", "a193", "a194", "a195", "a196", "a197", "a198", "a199", "a200", "a201", "a202", "a203", "a204", "a205", "a206", "a207", "a208", "a209", "a210", "a211", "a212", "a213", "a214", "a215", "a216", "a217", "a218", "a219", "a220", "a221", "a222", "a223", "a224", "a225", "a226", "a227", "a228", "a229", "a230", "a231", "a232", "a233", "a234", "a235", "a236", "a237", "a238", "a239", "a240", "a241", "a242", "a243", "a244", "a245", "a246", "a247", "a248", "a249", "a250", "a251", "a252", "a253", "a254", "a255"
#define CLOB_S "s28", "s29", "s30", "s31", "s32", "s33", "s34", "s35", "s36", "s37", "s38", "s39", "s40", "s41", "s42", "s43", "s44", "s45", "s46", "s47", "s48", "s49", "s50", "s51", "s52", "s53", "s54", "s55", "s56", "s57", "s58", "s59", "s60", "s61", "s62", "s63", "s64", "s65", "s66", "s67", "s68", "s69", "s70", "s71", "s72", "s73", "s74", "s75", "s76", "s77", "s78", "s79", "s80", "s81", "s82", "s83", "s84", "s85", "s86", "s87", "s88", "s89", "s90", "s91", "s92", "s93", "s94", "s95", "s96", "s97", "s98", "s99", "s100", "s101"
#define QKV_ASM \
    "s_mov_b32 s40, %5\n" \
    "s_mov_b64 exec, -1\n" \
    "v_mbcnt_lo_u32_b32 v0, -1, 0\n" \
    "v_mbcnt_hi_u32_b32 v0, -1, v0\n" \
    "v_lshrrev_b32 v10, 4, v0\n" \
    "s_lshl_b32 s50, s40, 4\n" \
    "v_add_u32 v11, s50, v10\n" \
    "s_mov_b32 s50, 8192\n" \
    "v_mul_lo_u32 v1, v11, s50\n" \
    "v_lshlrev_b32 v10, 2, v10\n" \
    "v_and_b32 v11, 15, v0\n" \
    "v_xor_b32 v10, v10, v11\n" \
    "v_lshl_add_u32 v1, v10, 4, v1\n" \
    "v_lshrrev_b32 v10, 5, v0\n" \
    "v_lshlrev_b32 v10, 3, v10\n" \
    "v_bfe_u32 v11, v0, 2, 2\n" \
    "v_add_lshl_u32 v10, v10, v11, 8\n" \
    "v_bfe_u32 v12, v0, 4, 1\n" \
    "v_lshl_add_u32 v10, v12, 5, v10\n" \
    "v_and_b32 v12, 3, v0\n" \
    "v_lshl_add_u32 v10, v12, 3, v10\n" \
    "v_xor_b32 v12, 0, v11\n" \
    "v_lshl_add_u32 v12, v12, 6, v10\n" \
    "v_add_u32 v16, 0x10000, v12\n" \
    "v_xor_b32 v13, 1, v11\n" \
    "v_lshl_add_u32 v13, v13, 6, v10\n" \
    "v_add_u32 v17, 0x10000, v13\n" \
    "v_xor_b32 v14, 2, v11\n" \
    "v_lshl_add_u32 v14, v14, 6, v10\n" \
    "v_add_u32 v18, 0x10000, v14\n" \
    "v_xor_b32 v15, 3, v11\n" \
    "v_lshl_add_u32 v15, v15, 6, v10\n" \
    "v_add_u32 v19, 0x10000, v15\n" \
    "v_add_u32 v2, 32768, v1\n" \
    "v_add_u32 v3, 65536, v1\n" \
    "v_add_u32 v4, 98304, v1\n" \
    "v_add_u32 v20, 0x20000, v12\n" \
    "v_add_u32 v21, 0x20000, v13\n" \
    "v_add_u32 v22, 0x20000, v14\n" \
    "v_add_u32 v23, 0x20000, v15\n" \
    "v_lshlrev_b32 v6, 4, v0\n" \
    "v_lshrrev_b32 v10, 5, v0\n" \
    "v_lshlrev_b32 v10, 8, v10\n" \
    "v_bfe_u32 v11, v0, 3, 1\n" \
    "v_lshl_add_u32 v7, v11, 10, v10\n" \
    "v_bfe_u32 v11, v0, 4, 1\n" \
    "v_lshl_add_u32 v7, v11, 7, v7\n" \
    "v_lshl_add_u32 v8, v11, 10, v10\n" \
    "v_and_b32 v11, 7, v0\n" \
    "v_lshl_add_u32 v7, v11, 4, v7\n" \
    "v_and_b32 v11, 15, v0\n" \
    "v_lshl_add_u32 v8, v11, 4, v8\n" \
    "s_lshl_b32 s51, s40, 12\n" \
    "s_mov_b32 s52, %8\n" \
    "s_add_u32 s53, s52, %9\n" \
    "s_mov_b32 s54, 0\n" \
    "s_cmp_eq_u32 s52, 1\n" \
    "s_cselect_b64 s[42:43], %1, %0\n" \
    "s_cmp_eq_u32 s52, 2\n" \
    "s_cselect_b64 s[42:43], %2, s[42:43]\n" \
    "s_barrier\n" \
    "s_mov_b64 s[46:47], %3\n" \
    "s_add_u32 s46, s46, 4096\n" \
    "s_addc_u32 s47, s47, 0\n" \
    "s_mov_b64 s[48:49], s[42:43]\n" \
    "s_add_u32 s42, s42, 4194304\n" \
    "s_addc_u32 s43, s43, 0\n" \
    "global_load_dwordx4 a[0:3], v6, s[46:47] offset:-4096\n" \
    "global_load_dwordx4 a[4:7], v6, s[46:47] offset:-3072\n" \
    "global_load_dwordx4 a[8:11], v6, s[46:47] offset:-2048\n" \
    "global_load_dwordx4 a[12:15], v6, s[46:47] offset:-1024\n" \
    "s_add_u32 m0, s51, 0\n" \
    "s_nop 0\n" \
    "global_load_lds_dwordx4 v1, s[48:49]\n" \
    "s_add_u32 m0, s51, 1024\n" \
    "s_nop 0\n" \
    "global_load_lds_dwordx4 v2, s[48:49]\n" \
    "s_add_u32 m0, s51, 2048\n" \
    "s_nop 0\n" \
    "global_load_lds_dwordx4 v3, s[48:49]\n" \
    "s_add_u32 m0, s51, 3072\n" \
    "s_nop 0\n" \
    "global_load_lds_dwordx4 v4, s[48:49]\n" \
    "s_add_u32 s48, s48, 524288\n" \
    "s_addc_u32 s49, s49, 0\n" \
    "global_load_dwordx4 a[16:19], v6, s[46:47] offset:0\n" \
    "global_load_dwordx4 a[20:23], v6, s[46:47] offset:1024\n" \
    "global_load_dwordx4 a[24:27], v6, s[46:47] offset:2048\n" \
    "global_load_dwordx4 a[28:31], v6, s[46:47] offset:3072\n" \
    "s_add_u32 m0, s51, 16384\n" \
    "s_nop 0\n" \
    "global_load_lds_dwordx4 v1, s[48:49]\n" \
    "s_add_u32 m0, s51, 17408\n" \
    "s_nop 0\n" \
    "global_load_lds_dwordx4 v2, s[48:49]\n" \
    "s_add_u32 m0, s51, 18432\n" \
    "s_nop 0\n" \
    "global_load_lds_dwordx4 v3, s[48:49]\n" \
    "s_add_u32 m0, s51, 19456\n" \
    "s_nop 0\n" \
    "global_load_lds_dwordx4 v4, s[48:49]\n" \
    "s_add_u32 s48, s48, 524288\n" \
    "s_addc_u32 s49, s49, 0\n" \
    "s_waitcnt vmcnt(8)\n" \
    "s_barrier\n" \
    "ds_read_b64_tr_b16 v[24:25], v12 offset:0\n" \
    "ds_read_b64_tr_b16 v[26:27], v12 offset:1024\n" \
    "ds_read_b64_tr_b16 v[28:29], v13 offset:0\n" \
    "ds_read_b64_tr_b16 v[30:31], v13 offset:1024\n" \
    "ds_read_b64_tr_b16 v[32:33], v14 offset:0\n" \
    "ds_read_b64_tr_b16 v[34:35], v14 offset:1024\n" \
    "ds_read_b64_tr_b16 v[36:37], v15 offset:0\n" \
    "ds_read_b64_tr_b16 v[38:39], v15 offset:1024\n" \
    "QITEM%=:\n" \
    "s_add_u32 s50, s52, 1\n" \
    "s_cmp_ge_u32 s50, s53\n" \
    "s_cselect_b32 s55, 1, 0\n" \
    "s_cmp_eq_u32 s50, 1\n" \
    "s_cselect_b64 s[44:45], %1, %0\n" \
    "s_cmp_eq_u32 s50, 2\n" \
    "s_cselect_b64 s[44:45], %2, s[44:45]\n" \
    "s_cmp_eq_u32 s52, 1\n" \
    "s_cbranch_scc1 QV%=\n" \
    "QK%=:\n" \
    "s_cmp_lg_u32 s54, 0\n" \
    "s_cbranch_scc1 QKC0%=\n" \
    "s_add_u32 s46, s46, 8192\n" \
    "s_addc_u32 s47, s47, 0\n" \
    "global_load_dwordx4 a[32:35], v6, s[46:47] offset:-4096\n" \
    "global_load_dwordx4 a[36:39], v6, s[46:47] offset:-3072\n" \
    "global_load_dwordx4 a[40:43], v6, s[46:47] offset:-2048\n" \
    "global_load_dwordx4 a[44:47], v6, s[46:47] offset:-1024\n" \
    "s_add_u32 m0, s51, 32768\n" \
    "s_nop 0\n" \
    "global_load_lds_dwordx4 v1, s[48:49]\n" \
    "s_add_u32 m0, s51, 33792\n" \
    "s_nop 0\n" \
    "global_load_lds_dwordx4 v2, s[48:49]\n" \
    "s_add_u32 m0, s51, 34816\n" \
    "s_nop 0\n" \
    "global_load_lds_dwordx4 v3, s[48:49]\n" \
    "s_add_u32 m0, s51, 35840\n" \
    "s_nop 0\n" \
    "global_load_lds_dwordx4 v4, s[48:49]\n" \
    "s_add_u32 s48, s48, 524288\n" \
    "s_addc_u32 s49, s49, 0\n" \
    "QKC0%=:\n" \
    "s_cmp_eq_u32 s54, 0\n" \
    "s_cbranch_scc1 QKS%=\n" \
    "global_store_dwordx4 v9, v[128:131], s[46:47] offset:-4096 sc1\n" \
    "global_store_dwordx4 v9, v[132:135], s[46:47] offset:-3584 sc1\n" \
    "global_store_dwordx4 v9, v[136:139], s[46:47] offset:-2048 sc1\n" \
    "global_store_dwordx4 v9, v[140:143], s[46:47] offset:-1536 sc1\n" \
    "global_store_dwordx4 v9, v[144:147], s[46:47] offset:0 sc1\n" \
    "global_store_dwordx4 v9, v[148:151], s[46:47] offset:512 sc1\n" \
    "global_store_dwordx4 v9, v[152:155], s[46:47] offset:2048 sc1\n" \
    "global_store_dwordx4 v9, v[156:159], s[46:47] offset:2560 sc1\n" \
    "QKS%=:\n" \
    "s_waitcnt lgkmcnt(6)\n" \
    "v_mfma_f32_32x32x16_bf16 v[64:79], v[24:27], a[0:3], 0\n" \
    "ds_read_b64_tr_b16 v[40:41], v12 offset:4096\n" \
    "ds_read_b64_tr_b16 v[42:43], v12 offset:5120\n" \
    "s_waitcnt lgkmcnt(6)\n" \
    "v_mfma_f32_32x32x16_bf16 v[80:95], v[28:31], a[0:3], 0\n" \
    "ds_read_b64_tr_b16 v[44:45], v13 offset:4096\n" \
    "ds_read_b64_tr_b16 v[46:47], v13 offset:5120\n" \
    "s_waitcnt lgkmcnt(6)\n" \
    "v_mfma_f32_32x32x16_bf16 v[96:111], v[32:35], a[0:3], 0\n" \
    "ds_read_b64_tr_b16 v[48:49], v14 offset:4096\n" \
    "ds_read_b64_tr_b16 v[50:51], v14 offset:5120\n" \
    "s_waitcnt lgkmcnt(6)\n" \
    "v_mfma_f32_32x32x16_bf16 v[112:127], v[36:39], a[0:3], 0\n" \
    "ds_read_b64_tr_b16 v[52:53], v15 offset:4096\n" \
    "ds_read_b64_tr_b16 v[54:55], v15 offset:5120\n" \
    "s_waitcnt lgkmcnt(6)\n" \
    "v_mfma_f32_32x32x16_bf16 v[64:79], v[40:43], a[4:7], v[64:79]\n" \
    "ds_read_b64_tr_b16 v[24:25], v12 offset:8192\n" \
    "ds_read_b64_tr_b16 v[26:27], v12 offset:9216\n" \
    "s_waitcnt lgkmcnt(6)\n" \
    "v_mfma_f32_32x32x16_bf16 v[80:95], v[44:47], a[4:7], v[80:95]\n" \
    "ds_read_b64_tr_b16 v[28:29], v13 offset:8192\n" \
    "ds_read_b64_tr_b16 v[30:31], v13 offset:9216\n" \
    "s_waitcnt lgkmcnt(6)\n" \
    "v_mfma_f32_32x32x16_bf16 v[96:111], v[48:51], a[4:7], v[96:111]\n" \
    "ds_read_b64_tr_b16 v[32:33], v14 offset:8192\n" \
    "ds_read_b64_tr_b16 v[34:35], v14 offset:9216\n" \
    "s_waitcnt lgkmcnt(6)\n" \
    "v_mfma_f32_32x32x16_bf16 v[112:127], v[52:55], a[4:7], v[112:127]\n" \
    "ds_read_b64_tr_b16 v[36:37], v15 offset:8192\n" \
    "ds_read_b64_tr_b16 v[38:39], v15 offset:9216\n" \
    "s_cmp_eq_u32 s54, 0\n" \
    "s_cbranch_scc1 QKW0%=\n" \
    "s_waitcnt vmcnt(24)\n" \
    "s_cmp_eq_u32 s55, 0\n" \
    "s_cbranch_scc1 QKX0%=\n" \
    "s_waitcnt vmcnt(24)\n" \
    "s_branch QKX0%=\n" \
    "QKW0%=:\n" \
    "s_waitcnt vmcnt(12)\n" \
    "s_waitcnt vmcnt(8)\n" \
    "s_cmp_eq_u32 s55, 0\n" \
    "s_cbranch_scc1 QKX0%=\n" \
    "s_waitcnt vmcnt(12)\n" \
    "s_waitcnt vmcnt(8)\n" \
    "QKX0%=:\n" \
    "s_barrier\n" \
    "s_waitcnt lgkmcnt(6)\n" \
    "s_add_u32 m0, s51, 131072\n" \
    "v_mfma_f32_32x32x16_bf16 v[64:79], v[24:27], a[8:11], v[64:79]\n" \
    "global_load_lds_dwordx4 v1, s[42:43]\n" \
    "ds_read_b64_tr_b16 v[40:41], v12 offset:12288\n" \
    "ds_read_b64_tr_b16 v[42:43], v12 offset:13312\n" \
    "s_waitcnt lgkmcnt(6)\n" \
    "s_add_u32 m0, s51, 132096\n" \
    "v_mfma_f32_32x32x16_bf16 v[80:95], v[28:31], a[8:11], v[80:95]\n" \
    "global_load_lds_dwordx4 v2, s[42:43]\n" \
    "ds_read_b64_tr_b16 v[44:45], v13 offset:12288\n" \
    "ds_read_b64_tr_b16 v[46:47], v13 offset:13312\n" \
    "s_waitcnt lgkmcnt(6)\n" \
    "s_add_u32 m0, s51, 133120\n" \
    "v_mfma_f32_32x32x16_bf16 v[96:111], v[32:35], a[8:11], v[96:111]\n" \
    "global_load_lds_dwordx4 v3, s[42:43]\n" \
    "ds_read_b64_tr_b16 v[48:49], v14 offset:12288\n" \
    "ds_read_b64_tr_b16 v[50:51], v14 offset:13312\n" \
    "s_waitcnt lgkmcnt(6)\n" \
    "s_add_u32 m0, s51, 134144\n" \
    "v_mfma_f32_32x32x16_bf16 v[112:127], v[36:39], a[8:11], v[112:127]\n" \
    "global_load_lds_dwordx4 v4, s[42:43]\n" \
    "s_add_u32 s42, s42, 524288\n" \
    "s_addc_u32 s43, s43, 0\n" \
    "ds_read_b64_tr_b16 v[52:53], v15 offset:12288\n" \
    "ds_read_b64_tr_b16 v[54:55], v15 offset:13312\n" \
    "s_waitcnt lgkmcnt(6)\n" \
    "v_mfma_f32_32x32x16_bf16 v[64:79], v[40:43], a[12:15], v[64:79]\n" \
    "ds_read_b64_tr_b16 v[24:25], v12 offset:16384\n" \
    "ds_read_b64_tr_b16 v[26:27], v12 offset:17408\n" \
    "s_waitcnt lgkmcnt(6)\n" \
    "v_mfma_f32_32x32x16_bf16 v[80:95], v[44:47], a[12:15], v[80:95]\n" \
    "ds_read_b64_tr_b16 v[28:29], v13 offset:16384\n" \
    "ds_read_b64_tr_b16 v[30:31], v13 offset:17408\n" \
    "s_waitcnt lgkmcnt(6)\n" \
    "v_mfma_f32_32x32x16_bf16 v[96:111], v[48:51], a[12:15], v[96:111]\n" \
    "ds_read_b64_tr_b16 v[32:33], v14 offset:16384\n" \
    "ds_read_b64_tr_b16 v[34:35], v14 offset:17408\n" \
    "s_waitcnt lgkmcnt(6)\n" \
    "v_mfma_f32_32x32x16_bf16 v[112:127], v[52:55], a[12:15], v[112:127]\n" \
    "ds_read_b64_tr_b16 v[36:37], v15 offset:16384\n" \
    "ds_read_b64_tr_b16 v[38:39], v15 offset:17408\n" \
    "s_cmp_lg_u32 s54, 0\n" \
    "s_cbranch_scc1 QKC1%=\n" \
    "global_load_dwordx4 a[48:51], v6, s[46:47] offset:0\n" \
    "global_load_dwordx4 a[52:55], v6, s[46:47] offset:1024\n" \
    "global_load_dwordx4 a[56:59], v6, s[46:47] offset:2048\n" \
    "global_load_dwordx4 a[60:63], v6, s[46:47] offset:3072\n" \
    "s_add_u32 m0, s51, 49152\n" \
    "s_nop 0\n" \
    "global_load_lds_dwordx4 v1, s[48:49]\n" \
    "s_add_u32 m0, s51, 50176\n" \
    "s_nop 0\n" \
    "global_load_lds_dwordx4 v2, s[48:49]\n" \
    "s_add_u32 m0, s51, 51200\n" \
    "s_nop 0\n" \
    "global_load_lds_dwordx4 v3, s[48:49]\n" \
    "s_add_u32 m0, s51, 52224\n" \
    "s_nop 0\n" \
    "global_load_lds_dwordx4 v4, s[48:49]\n" \
    "s_add_u32 s48, s48, 524288\n" \
    "s_addc_u32 s49, s49, 0\n" \
    "QKC1%=:\n" \
    "s_waitcnt lgkmcnt(6)\n" \
    "v_mfma_f32_32x32x16_bf16 v[64:79], v[24:27], a[16:19], v[64:79]\n" \
    "ds_read_b64_tr_b16 v[40:41], v12 offset:20480\n" \
    "ds_read_b64_tr_b16 v[42:43], v12 offset:21504\n" \
    "s_waitcnt lgkmcnt(6)\n" \
    "v_mfma_f32_32x32x16_bf16 v[80:95], v[28:31], a[16:19], v[80:95]\n" \
    "ds_read_b64_tr_b16 v[44:45], v13 offset:20480\n" \
    "ds_read_b64_tr_b16 v[46:47], v13 offset:21504\n" \
    "s_waitcnt lgkmcnt(6)\n" \
    "v_mfma_f32_32x32x16_bf16 v[96:111], v[32:35], a[16:19], v[96:111]\n" \
    "ds_read_b64_tr_b16 v[48:49], v14 offset:20480\n" \
    "ds_read_b64_tr_b16 v[50:51], v14 offset:21504\n" \
    "s_waitcnt lgkmcnt(6)\n" \
    "v_mfma_f32_32x32x16_bf16 v[112:127], v[36:39], a[16:19], v[112:127]\n" \
    "ds_read_b64_tr_b16 v[52:53], v15 offset:20480\n" \
    "ds_read_b64_tr_b16 v[54:55], v15 offset:21504\n" \
    "s_waitcnt lgkmcnt(6)\n" \
    "v_mfma_f32_32x32x16_bf16 v[64:79], v[40:43], a[20:23], v[64:79]\n" \
    "ds_read_b64_tr_b16 v[24:25], v12 offset:24576\n" \
    "ds_read_b64_tr_b16 v[26:27], v12 offset:25600\n" \
    "s_waitcnt lgkmcnt(6)\n" \
    "v_mfma_f32_32x32x16_bf16 v[80:95], v[44:47], a[20:23], v[80:95]\n" \
    "ds_read_b64_tr_b16 v[28:29], v13 offset:24576\n" \
    "ds_read_b64_tr_b16 v[30:31], v13 offset:25600\n" \
    "s_waitcnt lgkmcnt(6)\n" \
    "v_mfma_f32_32x32x16_bf16 v[96:111], v[48:51], a[20:23], v[96:111]\n" \
    "ds_read_b64_tr_b16 v[32:33], v14 offset:24576\n" \
    "ds_read_b64_tr_b16 v[34:35], v14 offset:25600\n" \
    "s_waitcnt lgkmcnt(6)\n" \
    "v_mfma_f32_32x32x16_bf16 v[112:127], v[52:55], a[20:23], v[112:127]\n" \
    "ds_read_b64_tr_b16 v[36:37], v15 offset:24576\n" \
    "ds_read_b64_tr_b16 v[38:39], v15 offset:25600\n" \
    "s_cmp_eq_u32 s54, 0\n" \
    "s_cbranch_scc1 QKW1%=\n" \
    "s_waitcnt vmcnt(24)\n" \
    "s_cmp_eq_u32 s55, 0\n" \
    "s_cbranch_scc1 QKX1%=\n" \
    "s_waitcnt vmcnt(24)\n" \
    "s_branch QKX1%=\n" \
    "QKW1%=:\n" \
    "s_waitcnt vmcnt(16)\n" \
    "s_waitcnt vmcnt(12)\n" \
    "s_cmp_eq_u32 s55, 0\n" \
    "s_cbranch_scc1 QKX1%=\n" \
    "s_waitcnt vmcnt(16)\n" \
    "s_waitcnt vmcnt(12)\n" \
    "QKX1%=:\n" \
    "s_barrier\n" \
    "s_waitcnt lgkmcnt(6)\n" \
    "s_add_u32 m0, s51, 147456\n" \
    "v_mfma_f32_32x32x16_bf16 v[64:79], v[24:27], a[24:27], v[64:79]\n" \
    "global_load_lds_dwordx4 v1, s[42:43]\n" \
    "ds_read_b64_tr_b16 v[40:41], v12 offset:28672\n" \
    "ds_read_b64_tr_b16 v[42:43], v12 offset:29696\n" \
    "s_waitcnt lgkmcnt(6)\n" \
    "s_add_u32 m0, s51, 148480\n" \
    "v_mfma_f32_32x32x16_bf16 v[80:95], v[28:31], a[24:27], v[80:95]\n" \
    "global_load_lds_dwordx4 v2, s[42:43]\n" \
    "ds_read_b64_tr_b16 v[44:45], v13 offset:28672\n" \
    "ds_read_b64_tr_b16 v[46:47], v13 offset:29696\n" \
    "s_waitcnt lgkmcnt(6)\n" \
    "s_add_u32 m0, s51, 149504\n" \
    "v_mfma_f32_32x32x16_bf16 v[96:111], v[32:35], a[24:27], v[96:111]\n" \
    "global_load_lds_dwordx4 v3, s[42:43]\n" \
    "ds_read_b64_tr_b16 v[48:49], v14 offset:28672\n" \
    "ds_read_b64_tr_b16 v[50:51], v14 offset:29696\n" \
    "s_waitcnt lgkmcnt(6)\n" \
    "s_add_u32 m0, s51, 150528\n" \
    "v_mfma_f32_32x32x16_bf16 v[112:127], v[36:39], a[24:27], v[112:127]\n" \
    "global_load_lds_dwordx4 v4, s[42:43]\n" \
    "s_add_u32 s42, s42, 524288\n" \
    "s_addc_u32 s43, s43, 0\n" \
    "ds_read_b64_tr_b16 v[52:53], v15 offset:28672\n" \
    "ds_read_b64_tr_b16 v[54:55], v15 offset:29696\n" \
    "s_waitcnt lgkmcnt(6)\n" \
    "v_mfma_f32_32x32x16_bf16 v[64:79], v[40:43], a[28:31], v[64:79]\n" \
    "ds_read_b64_tr_b16 v[24:25], v12 offset:32768\n" \
    "ds_read_b64_tr_b16 v[26:27], v12 offset:33792\n" \
    "s_waitcnt lgkmcnt(6)\n" \
    "v_mfma_f32_32x32x16_bf16 v[80:95], v[44:47], a[28:31], v[80:95]\n" \
    "ds_read_b64_tr_b16 v[28:29], v13 offset:32768\n" \
    "ds_read_b64_tr_b16 v[30:31], v13 offset:33792\n" \
    "s_waitcnt lgkmcnt(6)\n" \
    "v_mfma_f32_32x32x16_bf16 v[96:111], v[48:51], a[28:31], v[96:111]\n" \
    "ds_read_b64_tr_b16 v[32:33], v14 offset:32768\n" \
    "ds_read_b64_tr_b16 v[34:35], v14 offset:33792\n" \
    "s_waitcnt lgkmcnt(6)\n" \
    "v_mfma_f32_32x32x16_bf16 v[112:127], v[52:55], a[28:31], v[112:127]\n" \
    "ds_read_b64_tr_b16 v[36:37], v15 offset:32768\n" \
    "ds_read_b64_tr_b16 v[38:39], v15 offset:33792\n" \
    "s_cmp_lg_u32 s54, 0\n" \
    "s_cbranch_scc1 QKC2%=\n" \
    "s_add_u32 s46, s46, 8192\n" \
    "s_addc_u32 s47, s47, 0\n" \
    "global_load_dwordx4 a[64:67], v6, s[46:47] offset:-4096\n" \
    "global_load_dwordx4 a[68:71], v6, s[46:47] offset:-3072\n" \
    "global_load_dwordx4 a[72:75], v6, s[46:47] offset:-2048\n" \
    "global_load_dwordx4 a[76:79], v6, s[46:47] offset:-1024\n" \
    "s_add_u32 m0, s51, 65536\n" \
    "s_nop 0\n" \
    "global_load_lds_dwordx4 v1, s[48:49]\n" \
    "s_add_u32 m0, s51, 66560\n" \
    "s_nop 0\n" \
    "global_load_lds_dwordx4 v2, s[48:49]\n" \
    "s_add_u32 m0, s51, 67584\n" \
    "s_nop 0\n" \
    "global_load_lds_dwordx4 v3, s[48:49]\n" \
    "s_add_u32 m0, s51, 68608\n" \
    "s_nop 0\n" \
    "global_load_lds_dwordx4 v4, s[48:49]\n" \
    "s_add_u32 s48, s48, 524288\n" \
    "s_addc_u32 s49, s49, 0\n" \
    "QKC2%=:\n" \
    "s_waitcnt lgkmcnt(6)\n" \
    "v_mfma_f32_32x32x16_bf16 v[64:79], v[24:27], a[32:35], v[64:79]\n" \
    "ds_read_b64_tr_b16 v[40:41], v12 offset:36864\n" \
    "ds_read_b64_tr_b16 v[42:43], v12 offset:37888\n" \
    "s_waitcnt lgkmcnt(6)\n" \
    "v_mfma_f32_32x32x16_bf16 v[80:95], v[28:31], a[32:35], v[80:95]\n" \
    "ds_read_b64_tr_b16 v[44:45], v13 offset:36864\n" \
    "ds_read_b64_tr_b16 v[46:47], v13 offset:37888\n" \
    "s_waitcnt lgkmcnt(6)\n" \
    "v_mfma_f32_32x32x16_bf16 v[96:111], v[32:35], a[32:35], v[96:111]\n" \
    "ds_read_b64_tr_b16 v[48:49], v14 offset:36864\n" \
    "ds_read_b64_tr_b16 v[50:51], v14 offset:37888\n" \
    "s_waitcnt lgkmcnt(6)\n" \
    "v_mfma_f32_32x32x16_bf16 v[112:127], v[36:39], a[32:35], v[112:127]\n" \
    "ds_read_b64_tr_b16 v[52:53], v15 offset:36864\n" \
    "ds_read_b64_tr_b16 v[54:55], v15 offset:37888\n" \
    "s_waitcnt lgkmcnt(6)\n" \
    "v_mfma_f32_32x32x16_bf16 v[64:79], v[40:43], a[36:39], v[64:79]\n" \
    "ds_read_b64_tr_b16 v[24:25], v12 offset:40960\n" \
    "ds_read_b64_tr_b16 v[26:27], v12 offset:41984\n" \
    "s_waitcnt lgkmcnt(6)\n" \
    "v_mfma_f32_32x32x16_bf16 v[80:95], v[44:47], a[36:39], v[80:95]\n" \
    "ds_read_b64_tr_b16 v[28:29], v13 offset:40960\n" \
    "ds_read_b64_tr_b16 v[30:31], v13 offset:41984\n" \
    "s_waitcnt lgkmcnt(6)\n" \
    "v_mfma_f32_32x32x16_bf16 v[96:111], v[48:51], a[36:39], v[96:111]\n" \
    "ds_read_b64_tr_b16 v[32:33], v14 offset:40960\n" \
    "ds_read_b64_tr_b16 v[34:35], v14 offset:41984\n" \
    "s_waitcnt lgkmcnt(6)\n" \
    "v_mfma_f32_32x32x16_bf16 v[112:127], v[52:55], a[36:39], v[112:127]\n" \
    "ds_read_b64_tr_b16 v[36:37], v15 offset:40960\n" \
    "ds_read_b64_tr_b16 v[38:39], v15 offset:41984\n" \
    "s_cmp_eq_u32 s54, 0\n" \
    "s_cbranch_scc1 QKW2%=\n" \
    "s_waitcnt vmcnt(24)\n" \
    "s_cmp_eq_u32 s55, 0\n" \
    "s_cbranch_scc1 QKX2%=\n" \
    "s_waitcnt vmcnt(24)\n" \
    "s_branch QKX2%=\n" \
    "QKW2%=:\n" \
    "s_waitcnt vmcnt(16)\n" \
    "s_waitcnt vmcnt(12)\n" \
    "s_cmp_eq_u32 s55, 0\n" \
    "s_cbranch_scc1 QKX2%=\n" \
    "s_waitcnt vmcnt(16)\n" \
    "s_waitcnt vmcnt(12)\n" \
    "QKX2%=:\n" \
    "s_barrier\n" \
    "s_waitcnt lgkmcnt(6)\n" \
    "s_mov_b64 s[42:43], s[44:45]\n" \
    "s_add_u32 m0, s51, 0\n" \
    "s_cmp_lg_u32 s55, 0\n" \
    "v_mfma_f32_32x32x16_bf16 v[64:79], v[24:27], a[40:43], v[64:79]\n" \
    "s_cbranch_scc1 QKG2_0%=\n" \
    "global_load_lds_dwordx4 v1, s[42:43]\n" \
    "QKG2_0%=:\n" \
    "ds_read_b64_tr_b16 v[40:41], v12 offset:45056\n" \
    "ds_read_b64_tr_b16 v[42:43], v12 offset:46080\n" \
    "s_waitcnt lgkmcnt(6)\n" \
    "s_add_u32 m0, s51, 1024\n" \
    "s_cmp_lg_u32 s55, 0\n" \
    "v_mfma_f32_32x32x16_bf16 v[80:95], v[28:31], a[40:43], v[80:95]\n" \
    "s_cbranch_scc1 QKG2_1%=\n" \
    "global_load_lds_dwordx4 v2, s[42:43]\n" \
    "QKG2_1%=:\n" \
    "ds_read_b64_tr_b16 v[44:45], v13 offset:45056\n" \
    "ds_read_b64_tr_b16 v[46:47], v13 offset:46080\n" \
    "s_waitcnt lgkmcnt(6)\n" \
    "s_add_u32 m0, s51, 2048\n" \
    "s_cmp_lg_u32 s55, 0\n" \
    "v_mfma_f32_32x32x16_bf16 v[96:111], v[32:35], a[40:43], v[96:111]\n" \
    "s_cbranch_scc1 QKG2_2%=\n" \
    "global_load_lds_dwordx4 v3, s[42:43]\n" \
    "QKG2_2%=:\n" \
    "ds_read_b64_tr_b16 v[48:49], v14 offset:45056\n" \
    "ds_read_b64_tr_b16 v[50:51], v14 offset:46080\n" \
    "s_waitcnt lgkmcnt(6)\n" \
    "s_add_u32 m0, s51, 3072\n" \
    "s_cmp_lg_u32 s55, 0\n" \
    "v_mfma_f32_32x32x16_bf16 v[112:127], v[36:39], a[40:43], v[112:127]\n" \
    "s_cbranch_scc1 QKG2_3%=\n" \
    "global_load_lds_dwordx4 v4, s[42:43]\n" \
    "s_add_u32 s42, s42, 524288\n" \
    "s_addc_u32 s43, s43, 0\n" \
    "QKG2_3%=:\n" \
    "ds_read_b64_tr_b16 v[52:53], v15 offset:45056\n" \
    "ds_read_b64_tr_b16 v[54:55], v15 offset:46080\n" \
    "s_waitcnt lgkmcnt(6)\n" \
    "v_mfma_f32_32x32x16_bf16 v[64:79], v[40:43], a[44:47], v[64:79]\n" \
    "ds_read_b64_tr_b16 v[24:25], v12 offset:49152\n" \
    "ds_read_b64_tr_b16 v[26:27], v12 offset:50176\n" \
    "s_waitcnt lgkmcnt(6)\n" \
    "v_mfma_f32_32x32x16_bf16 v[80:95], v[44:47], a[44:47], v[80:95]\n" \
    "ds_read_b64_tr_b16 v[28:29], v13 offset:49152\n" \
    "ds_read_b64_tr_b16 v[30:31], v13 offset:50176\n" \
    "s_waitcnt lgkmcnt(6)\n" \
    "v_mfma_f32_32x32x16_bf16 v[96:111], v[48:51], a[44:47], v[96:111]\n" \
    "ds_read_b64_tr_b16 v[32:33], v14 offset:49152\n" \
    "ds_read_b64_tr_b16 v[34:35], v14 offset:50176\n" \
    "s_waitcnt lgkmcnt(6)\n" \
    "v_mfma_f32_32x32x16_bf16 v[112:127], v[52:55], a[44:47], v[112:127]\n" \
    "ds_read_b64_tr_b16 v[36:37], v15 offset:49152\n" \
    "ds_read_b64_tr_b16 v[38:39], v15 offset:50176\n" \
    "s_cmp_lg_u32 s54, 0\n" \
    "s_cbranch_scc1 QKC3%=\n" \
    "global_load_dwordx4 a[80:83], v6, s[46:47] offset:0\n" \
    "global_load_dwordx4 a[84:87], v6, s[46:47] offset:1024\n" \
    "global_load_dwordx4 a[88:91], v6, s[46:47] offset:2048\n" \
    "global_load_dwordx4 a[92:95], v6, s[46:47] offset:3072\n" \
    "s_add_u32 m0, s51, 81920\n" \
    "s_nop 0\n" \
    "global_load_lds_dwordx4 v1, s[48:49]\n" \
    "s_add_u32 m0, s51, 82944\n" \
    "s_nop 0\n" \
    "global_load_lds_dwordx4 v2, s[48:49]\n" \
    "s_add_u32 m0, s51, 83968\n" \
    "s_nop 0\n" \
    "global_load_lds_dwordx4 v3, s[48:49]\n" \
    "s_add_u32 m0, s51, 84992\n" \
    "s_nop 0\n" \
    "global_load_lds_dwordx4 v4, s[48:49]\n" \
    "s_add_u32 s48, s48, 524288\n" \
    "s_addc_u32 s49, s49, 0\n" \
    "QKC3%=:\n" \
    "s_waitcnt lgkmcnt(6)\n" \
    "v_mfma_f32_32x32x16_bf16 v[64:79], v[24:27], a[48:51], v[64:79]\n" \
    "ds_read_b64_tr_b16 v[40:41], v12 offset:53248\n" \
    "ds_read_b64_tr_b16 v[42:43], v12 offset:54272\n" \
    "s_waitcnt lgkmcnt(6)\n" \
    "v_mfma_f32_32x32x16_bf16 v[80:95], v[28:31], a[48:51], v[80:95]\n" \
    "ds_read_b64_tr_b16 v[44:45], v13 offset:53248\n" \
    "ds_read_b64_tr_b16 v[46:47], v13 offset:54272\n" \
    "s_waitcnt lgkmcnt(6)\n" \
    "v_mfma_f32_32x32x16_bf16 v[96:111], v[32:35], a[48:51], v[96:111]\n" \
    "ds_read_b64_tr_b16 v[48:49], v14 offset:53248\n" \
    "ds_read_b64_tr_b16 v[50:51], v14 offset:54272\n" \
    "s_waitcnt lgkmcnt(6)\n" \
    "v_mfma_f32_32x32x16_bf16 v[112:127], v[36:39], a[48:51], v[112:127]\n" \
    "ds_read_b64_tr_b16 v[52:53], v15 offset:53248\n" \
    "ds_read_b64_tr_b16 v[54:55], v15 offset:54272\n" \
    "s_waitcnt lgkmcnt(6)\n" \
    "v_mfma_f32_32x32x16_bf16 v[64:79], v[40:43], a[52:55], v[64:79]\n" \
    "ds_read_b64_tr_b16 v[24:25], v12 offset:57344\n" \
    "ds_read_b64_tr_b16 v[26:27], v12 offset:58368\n" \
    "s_waitcnt lgkmcnt(6)\n" \
    "v_mfma_f32_32x32x16_bf16 v[80:95], v[44:47], a[52:55], v[80:95]\n" \
    "ds_read_b64_tr_b16 v[28:29], v13 offset:57344\n" \
    "ds_read_b64_tr_b16 v[30:31], v13 offset:58368\n" \
    "s_waitcnt lgkmcnt(6)\n" \
    "v_mfma_f32_32x32x16_bf16 v[96:111], v[48:51], a[52:55], v[96:111]\n" \
    "ds_read_b64_tr_b16 v[32:33], v14 offset:57344\n" \
    "ds_read_b64_tr_b16 v[34:35], v14 offset:58368\n" \
    "s_waitcnt lgkmcnt(6)\n" \
    "v_mfma_f32_32x32x16_bf16 v[112:127], v[52:55], a[52:55], v[112:127]\n" \
    "ds_read_b64_tr_b16 v[36:37], v15 offset:57344\n" \
    "ds_read_b64_tr_b16 v[38:39], v15 offset:58368\n" \
    "s_cmp_eq_u32 s54, 0\n" \
    "s_cbranch_scc1 QKW3%=\n" \
    "s_waitcnt vmcnt(24)\n" \
    "s_cmp_eq_u32 s55, 0\n" \
    "s_cbranch_scc1 QKX3%=\n" \
    "s_waitcnt vmcnt(20)\n" \
    "s_branch QKX3%=\n" \
    "QKW3%=:\n" \
    "s_waitcnt vmcnt(16)\n" \
    "s_waitcnt vmcnt(12)\n" \
    "s_cmp_eq_u32 s55, 0\n" \
    "s_cbranch_scc1 QKX3%=\n" \
    "s_waitcnt vmcnt(12)\n" \
    "s_waitcnt vmcnt(8)\n" \
    "QKX3%=:\n" \
    "s_barrier\n" \
    "s_waitcnt lgkmcnt(6)\n" \
    "s_add_u32 m0, s51, 16384\n" \
    "s_cmp_lg_u32 s55, 0\n" \
    "v_mfma_f32_32x32x16_bf16 v[64:79], v[24:27], a[56:59], v[64:79]\n" \
    "s_cbranch_scc1 QKG3_0%=\n" \
    "global_load_lds_dwordx4 v1, s[42:43]\n" \
    "QKG3_0%=:\n" \
    "ds_read_b64_tr_b16 v[40:41], v12 offset:61440\n" \
    "ds_read_b64_tr_b16 v[42:43], v12 offset:62464\n" \
    "s_waitcnt lgkmcnt(6)\n" \
    "s_add_u32 m0, s51, 17408\n" \
    "s_cmp_lg_u32 s55, 0\n" \
    "v_mfma_f32_32x32x16_bf16 v[80:95], v[28:31], a[56:59], v[80:95]\n" \
    "s_cbranch_scc1 QKG3_1%=\n" \
    "global_load_lds_dwordx4 v2, s[42:43]\n" \
    "QKG3_1%=:\n" \
    "ds_read_b64_tr_b16 v[44:45], v13 offset:61440\n" \
    "ds_read_b64_tr_b16 v[46:47], v13 offset:62464\n" \
    "s_waitcnt lgkmcnt(6)\n" \
    "s_add_u32 m0, s51, 18432\n" \
    "s_cmp_lg_u32 s55, 0\n" \
    "v_mfma_f32_32x32x16_bf16 v[96:111], v[32:35], a[56:59], v[96:111]\n" \
    "s_cbranch_scc1 QKG3_2%=\n" \
    "global_load_lds_dwordx4 v3, s[42:43]\n" \
    "QKG3_2%=:\n" \
    "ds_read_b64_tr_b16 v[48:49], v14 offset:61440\n" \
    "ds_read_b64_tr_b16 v[50:51], v14 offset:62464\n" \
    "s_waitcnt lgkmcnt(6)\n" \
    "s_add_u32 m0, s51, 19456\n" \
    "s_cmp_lg_u32 s55, 0\n" \
    "v_mfma_f32_32x32x16_bf16 v[112:127], v[36:39], a[56:59], v[112:127]\n" \
    "s_cbranch_scc1 QKG3_3%=\n" \
    "global_load_lds_dwordx4 v4, s[42:43]\n" \
    "s_add_u32 s42, s42, 524288\n" \
    "s_addc_u32 s43, s43, 0\n" \
    "QKG3_3%=:\n" \
    "ds_read_b64_tr_b16 v[52:53], v15 offset:61440\n" \
    "ds_read_b64_tr_b16 v[54:55], v15 offset:62464\n" \
    "s_waitcnt lgkmcnt(6)\n" \
    "v_mfma_f32_32x32x16_bf16 v[64:79], v[40:43], a[60:63], v[64:79]\n" \
    "ds_read_b64_tr_b16 v[24:25], v16 offset:0\n" \
    "ds_read_b64_tr_b16 v[26:27], v16 offset:1024\n" \
    "s_waitcnt lgkmcnt(6)\n" \
    "v_mfma_f32_32x32x16_bf16 v[80:95], v[44:47], a[60:63], v[80:95]\n" \
    "ds_read_b64_tr_b16 v[28:29], v17 offset:0\n" \
    "ds_read_b64_tr_b16 v[30:31], v17 offset:1024\n" \
    "s_waitcnt lgkmcnt(6)\n" \
    "v_mfma_f32_32x32x16_bf16 v[96:111], v[48:51], a[60:63], v[96:111]\n" \
    "ds_read_b64_tr_b16 v[32:33], v18 offset:0\n" \
    "ds_read_b64_tr_b16 v[34:35], v18 offset:1024\n" \
    "s_waitcnt lgkmcnt(6)\n" \
    "v_mfma_f32_32x32x16_bf16 v[112:127], v[52:55], a[60:63], v[112:127]\n" \
    "ds_read_b64_tr_b16 v[36:37], v19 offset:0\n" \
    "ds_read_b64_tr_b16 v[38:39], v19 offset:1024\n" \
    "s_cmp_lg_u32 s54, 0\n" \
    "s_cbranch_scc1 QKC4%=\n" \
    "s_add_u32 s46, s46, 8192\n" \
    "s_addc_u32 s47, s47, 0\n" \
    "global_load_dwordx4 a[96:99], v6, s[46:47] offset:-4096\n" \
    "global_load_dwordx4 a[100:103], v6, s[46:47] offset:-3072\n" \
    "global_load_dwordx4 a[104:107], v6, s[46:47] offset:-2048\n" \
    "global_load_dwordx4 a[108:111], v6, s[46:47] offset:-1024\n" \
    "s_add_u32 m0, s51, 98304\n" \
    "s_nop 0\n" \
    "global_load_lds_dwordx4 v1, s[48:49]\n" \
    "s_add_u32 m0, s51, 99328\n" \
    "s_nop 0\n" \
    "global_load_lds_dwordx4 v2, s[48:49]\n" \
    "s_add_u32 m0, s51, 100352\n" \
    "s_nop 0\n" \
    "global_load_lds_dwordx4 v3, s[48:49]\n" \
    "s_add_u32 m0, s51, 101376\n" \
    "s_nop 0\n" \
    "global_load_lds_dwordx4 v4, s[48:49]\n" \
    "s_add_u32 s48, s48, 524288\n" \
    "s_addc_u32 s49, s49, 0\n" \
    "QKC4%=:\n" \
    "s_waitcnt lgkmcnt(6)\n" \
    "v_mfma_f32_32x32x16_bf16 v[64:79], v[24:27], a[64:67], v[64:79]\n" \
    "ds_read_b64_tr_b16 v[40:41], v16 offset:4096\n" \
    "ds_read_b64_tr_b16 v[42:43], v16 offset:5120\n" \
    "s_waitcnt lgkmcnt(6)\n" \
    "v_mfma_f32_32x32x16_bf16 v[80:95], v[28:31], a[64:67], v[80:95]\n" \
    "ds_read_b64_tr_b16 v[44:45], v17 offset:4096\n" \
    "ds_read_b64_tr_b16 v[46:47], v17 offset:5120\n" \
    "s_waitcnt lgkmcnt(6)\n" \
    "v_mfma_f32_32x32x16_bf16 v[96:111], v[32:35], a[64:67], v[96:111]\n" \
    "ds_read_b64_tr_b16 v[48:49], v18 offset:4096\n" \
    "ds_read_b64_tr_b16 v[50:51], v18 offset:5120\n" \
    "s_waitcnt lgkmcnt(6)\n" \
    "v_mfma_f32_32x32x16_bf16 v[112:127], v[36:39], a[64:67], v[112:127]\n" \
    "ds_read_b64_tr_b16 v[52:53], v19 offset:4096\n" \
    "ds_read_b64_tr_b16 v[54:55], v19 offset:5120\n" \
    "s_cmp_eq_u32 s54, 0\n" \
    "s_cbranch_scc1 QKF%=\n" \
    "s_waitcnt vmcnt(8)\n" \
    "v_mov_b32 v10, 0\n" \
    "v_mov_b32 v11, %7\n" \
    "s_mov_b64 exec, 1\n" \
    "global_store_dword v10, v11, s[48:49] sc1\n" \
    "s_mov_b64 exec, -1\n" \
    "QKF%=:\n" \
    "s_waitcnt lgkmcnt(6)\n" \
    "v_mfma_f32_32x32x16_bf16 v[64:79], v[40:43], a[68:71], v[64:79]\n" \
    "ds_read_b64_tr_b16 v[24:25], v16 offset:8192\n" \
    "ds_read_b64_tr_b16 v[26:27], v16 offset:9216\n" \
    "s_waitcnt lgkmcnt(6)\n" \
    "v_mfma_f32_32x32x16_bf16 v[80:95], v[44:47], a[68:71], v[80:95]\n" \
    "ds_read_b64_tr_b16 v[28:29], v17 offset:8192\n" \
    "ds_read_b64_tr_b16 v[30:31], v17 offset:9216\n" \
    "s_waitcnt lgkmcnt(6)\n" \
    "v_mfma_f32_32x32x16_bf16 v[96:111], v[48:51], a[68:71], v[96:111]\n" \
    "ds_read_b64_tr_b16 v[32:33], v18 offset:8192\n" \
    "ds_read_b64_tr_b16 v[34:35], v18 offset:9216\n" \
    "s_waitcnt lgkmcnt(6)\n" \
    "v_mfma_f32_32x32x16_bf16 v[112:127], v[52:55], a[68:71], v[112:127]\n" \
    "ds_read_b64_tr_b16 v[36:37], v19 offset:8192\n" \
    "ds_read_b64_tr_b16 v[38:39], v19 offset:9216\n" \
    "s_cmp_eq_u32 s54, 0\n" \
    "s_cbranch_scc1 QKW4%=\n" \
    "s_waitcnt vmcnt(24)\n" \
    "s_cmp_eq_u32 s55, 0\n" \
    "s_cbranch_scc1 QKX4%=\n" \
    "s_waitcnt vmcnt(16)\n" \
    "s_branch QKX4%=\n" \
    "QKW4%=:\n" \
    "s_waitcnt vmcnt(16)\n" \
    "s_waitcnt vmcnt(12)\n" \
    "s_cmp_eq_u32 s55, 0\n" \
    "s_cbranch_scc1 QKX4%=\n" \
    "s_waitcnt vmcnt(12)\n" \
    "s_waitcnt vmcnt(8)\n" \
    "QKX4%=:\n" \
    "s_barrier\n" \
    "s_waitcnt lgkmcnt(6)\n" \
    "s_add_u32 m0, s51, 32768\n" \
    "s_cmp_lg_u32 s55, 0\n" \
    "v_mfma_f32_32x32x16_bf16 v[64:79], v[24:27], a[72:75], v[64:79]\n" \
    "s_cbranch_scc1 QKG4_0%=\n" \
    "global_load_lds_dwordx4 v1, s[42:43]\n" \
    "QKG4_0%=:\n" \
    "ds_read_b64_tr_b16 v[40:41], v16 offset:12288\n" \
    "ds_read_b64_tr_b16 v[42:43], v16 offset:13312\n" \
    "s_waitcnt lgkmcnt(6)\n" \
    "s_add_u32 m0, s51, 33792\n" \
    "s_cmp_lg_u32 s55, 0\n" \
    "v_mfma_f32_32x32x16_bf16 v[80:95], v[28:31], a[72:75], v[80:95]\n" \
    "s_cbranch_scc1 QKG4_1%=\n" \
    "global_load_lds_dwordx4 v2, s[42:43]\n" \
    "QKG4_1%=:\n" \
    "ds_read_b64_tr_b16 v[44:45], v17 offset:12288\n" \
    "ds_read_b64_tr_b16 v[46:47], v17 offset:13312\n" \
    "s_waitcnt lgkmcnt(6)\n" \
    "s_add_u32 m0, s51, 34816\n" \
    "s_cmp_lg_u32 s55, 0\n" \
    "v_mfma_f32_32x32x16_bf16 v[96:111], v[32:35], a[72:75], v[96:111]\n" \
    "s_cbranch_scc1 QKG4_2%=\n" \
    "global_load_lds_dwordx4 v3, s[42:43]\n" \
    "QKG4_2%=:\n" \
    "ds_read_b64_tr_b16 v[48:49], v18 offset:12288\n" \
    "ds_read_b64_tr_b16 v[50:51], v18 offset:13312\n" \
    "s_waitcnt lgkmcnt(6)\n" \
    "s_add_u32 m0, s51, 35840\n" \
    "s_cmp_lg_u32 s55, 0\n" \
    "v_mfma_f32_32x32x16_bf16 v[112:127], v[36:39], a[72:75], v[112:127]\n" \
    "s_cbranch_scc1 QKG4_3%=\n" \
    "global_load_lds_dwordx4 v4, s[42:43]\n" \
    "s_add_u32 s42, s42, 524288\n" \
    "s_addc_u32 s43, s43, 0\n" \
    "QKG4_3%=:\n" \
    "ds_read_b64_tr_b16 v[52:53], v19 offset:12288\n" \
    "ds_read_b64_tr_b16 v[54:55], v19 offset:13312\n" \
    "s_waitcnt lgkmcnt(6)\n" \
    "v_mfma_f32_32x32x16_bf16 v[64:79], v[40:43], a[76:79], v[64:79]\n" \
    "ds_read_b64_tr_b16 v[24:25], v16 offset:16384\n" \
    "ds_read_b64_tr_b16 v[26:27], v16 offset:17408\n" \
    "s_waitcnt lgkmcnt(6)\n" \
    "v_mfma_f32_32x32x16_bf16 v[80:95], v[44:47], a[76:79], v[80:95]\n" \
    "ds_read_b64_tr_b16 v[28:29], v17 offset:16384\n" \
    "ds_read_b64_tr_b16 v[30:31], v17 offset:17408\n" \
    "s_waitcnt lgkmcnt(6)\n" \
    "v_mfma_f32_32x32x16_bf16 v[96:111], v[48:51], a[76:79], v[96:111]\n" \
    "ds_read_b64_tr_b16 v[32:33], v18 offset:16384\n" \
    "ds_read_b64_tr_b16 v[34:35], v18 offset:17408\n" \
    "s_waitcnt lgkmcnt(6)\n" \
    "v_mfma_f32_32x32x16_bf16 v[112:127], v[52:55], a[76:79], v[112:127]\n" \
    "ds_read_b64_tr_b16 v[36:37], v19 offset:16384\n" \
    "ds_read_b64_tr_b16 v[38:39], v19 offset:17408\n" \
    "s_cmp_lg_u32 s54, 0\n" \
    "s_cbranch_scc1 QKC5%=\n" \
    "global_load_dwordx4 a[112:115], v6, s[46:47] offset:0\n" \
    "global_load_dwordx4 a[116:119], v6, s[46:47] offset:1024\n" \
    "global_load_dwordx4 a[120:123], v6, s[46:47] offset:2048\n" \
    "global_load_dwordx4 a[124:127], v6, s[46:47] offset:3072\n" \
    "s_add_u32 m0, s51, 114688\n" \
    "s_nop 0\n" \
    "global_load_lds_dwordx4 v1, s[48:49]\n" \
    "s_add_u32 m0, s51, 115712\n" \
    "s_nop 0\n" \
    "global_load_lds_dwordx4 v2, s[48:49]\n" \
    "s_add_u32 m0, s51, 116736\n" \
    "s_nop 0\n" \
    "global_load_lds_dwordx4 v3, s[48:49]\n" \
    "s_add_u32 m0, s51, 117760\n" \
    "s_nop 0\n" \
    "global_load_lds_dwordx4 v4, s[48:49]\n" \
    "s_add_u32 s48, s48, 524288\n" \
    "s_addc_u32 s49, s49, 0\n" \
    "QKC5%=:\n" \
    "s_waitcnt lgkmcnt(6)\n" \
    "v_mfma_f32_32x32x16_bf16 v[64:79], v[24:27], a[80:83], v[64:79]\n" \
    "ds_read_b64_tr_b16 v[40:41], v16 offset:20480\n" \
    "ds_read_b64_tr_b16 v[42:43], v16 offset:21504\n" \
    "s_waitcnt lgkmcnt(6)\n" \
    "v_mfma_f32_32x32x16_bf16 v[80:95], v[28:31], a[80:83], v[80:95]\n" \
    "ds_read_b64_tr_b16 v[44:45], v17 offset:20480\n" \
    "ds_read_b64_tr_b16 v[46:47], v17 offset:21504\n" \
    "s_waitcnt lgkmcnt(6)\n" \
    "v_mfma_f32_32x32x16_bf16 v[96:111], v[32:35], a[80:83], v[96:111]\n" \
    "ds_read_b64_tr_b16 v[48:49], v18 offset:20480\n" \
    "ds_read_b64_tr_b16 v[50:51], v18 offset:21504\n" \
    "s_waitcnt lgkmcnt(6)\n" \
    "v_mfma_f32_32x32x16_bf16 v[112:127], v[36:39], a[80:83], v[112:127]\n" \
    "ds_read_b64_tr_b16 v[52:53], v19 offset:20480\n" \
    "ds_read_b64_tr_b16 v[54:55], v19 offset:21504\n" \
    "s_waitcnt lgkmcnt(6)\n" \
    "v_mfma_f32_32x32x16_bf16 v[64:79], v[40:43], a[84:87], v[64:79]\n" \
    "ds_read_b64_tr_b16 v[24:25], v16 offset:24576\n" \
    "ds_read_b64_tr_b16 v[26:27], v16 offset:25600\n" \
    "s_waitcnt lgkmcnt(6)\n" \
    "v_mfma_f32_32x32x16_bf16 v[80:95], v[44:47], a[84:87], v[80:95]\n" \
    "ds_read_b64_tr_b16 v[28:29], v17 offset:24576\n" \
    "ds_read_b64_tr_b16 v[30:31], v17 offset:25600\n" \
    "s_waitcnt lgkmcnt(6)\n" \
    "v_mfma_f32_32x32x16_bf16 v[96:111], v[48:51], a[84:87], v[96:111]\n" \
    "ds_read_b64_tr_b16 v[32:33], v18 offset:24576\n" \
    "ds_read_b64_tr_b16 v[34:35], v18 offset:25600\n" \
    "s_waitcnt lgkmcnt(6)\n" \
    "v_mfma_f32_32x32x16_bf16 v[112:127], v[52:55], a[84:87], v[112:127]\n" \
    "ds_read_b64_tr_b16 v[36:37], v19 offset:24576\n" \
    "ds_read_b64_tr_b16 v[38:39], v19 offset:25600\n" \
    "s_cmp_eq_u32 s54, 0\n" \
    "s_cbranch_scc1 QKW5%=\n" \
    "s_waitcnt vmcnt(24)\n" \
    "s_cmp_eq_u32 s55, 0\n" \
    "s_cbranch_scc1 QKX5%=\n" \
    "s_waitcnt vmcnt(12)\n" \
    "s_branch QKX5%=\n" \
    "QKW5%=:\n" \
    "s_waitcnt vmcnt(16)\n" \
    "s_waitcnt vmcnt(12)\n" \
    "s_cmp_eq_u32 s55, 0\n" \
    "s_cbranch_scc1 QKX5%=\n" \
    "s_waitcnt vmcnt(12)\n" \
    "s_waitcnt vmcnt(8)\n" \
    "QKX5%=:\n" \
    "s_barrier\n" \
    "s_waitcnt lgkmcnt(6)\n" \
    "s_add_u32 m0, s51, 49152\n" \
    "s_cmp_lg_u32 s55, 0\n" \
    "v_mfma_f32_32x32x16_bf16 v[64:79], v[24:27], a[88:91], v[64:79]\n" \
    "s_cbranch_scc1 QKG5_0%=\n" \
    "global_load_lds_dwordx4 v1, s[42:43]\n" \
    "QKG5_0%=:\n" \
    "ds_read_b64_tr_b16 v[40:41], v16 offset:28672\n" \
    "ds_read_b64_tr_b16 v[42:43], v16 offset:29696\n" \
    "s_waitcnt lgkmcnt(6)\n" \
    "s_add_u32 m0, s51, 50176\n" \
    "s_cmp_lg_u32 s55, 0\n" \
    "v_mfma_f32_32x32x16_bf16 v[80:95], v[28:31], a[88:91], v[80:95]\n" \
    "s_cbranch_scc1 QKG5_1%=\n" \
    "global_load_lds_dwordx4 v2, s[42:43]\n" \
    "QKG5_1%=:\n" \
    "ds_read_b64_tr_b16 v[44:45], v17 offset:28672\n" \
    "ds_read_b64_tr_b16 v[46:47], v17 offset:29696\n" \
    "s_waitcnt lgkmcnt(6)\n" \
    "s_add_u32 m0, s51, 51200\n" \
    "s_cmp_lg_u32 s55, 0\n" \
    "v_mfma_f32_32x32x16_bf16 v[96:111], v[32:35], a[88:91], v[96:111]\n" \
    "s_cbranch_scc1 QKG5_2%=\n" \
    "global_load_lds_dwordx4 v3, s[42:43]\n" \
    "QKG5_2%=:\n" \
    "ds_read_b64_tr_b16 v[48:49], v18 offset:28672\n" \
    "ds_read_b64_tr_b16 v[50:51], v18 offset:29696\n" \
    "s_waitcnt lgkmcnt(6)\n" \
    "s_add_u32 m0, s51, 52224\n" \
    "s_cmp_lg_u32 s55, 0\n" \
    "v_mfma_f32_32x32x16_bf16 v[112:127], v[36:39], a[88:91], v[112:127]\n" \
    "s_cbranch_scc1 QKG5_3%=\n" \
    "global_load_lds_dwordx4 v4, s[42:43]\n" \
    "s_add_u32 s42, s42, 524288\n" \
    "s_addc_u32 s43, s43, 0\n" \
    "QKG5_3%=:\n" \
    "ds_read_b64_tr_b16 v[52:53], v19 offset:28672\n" \
    "ds_read_b64_tr_b16 v[54:55], v19 offset:29696\n" \
    "s_waitcnt lgkmcnt(6)\n" \
    "v_mfma_f32_32x32x16_bf16 v[64:79], v[40:43], a[92:95], v[64:79]\n" \
    "ds_read_b64_tr_b16 v[24:25], v16 offset:32768\n" \
    "ds_read_b64_tr_b16 v[26:27], v16 offset:33792\n" \
    "s_waitcnt lgkmcnt(6)\n" \
    "v_mfma_f32_32x32x16_bf16 v[80:95], v[44:47], a[92:95], v[80:95]\n" \
    "ds_read_b64_tr_b16 v[28:29], v17 offset:32768\n" \
    "ds_read_b64_tr_b16 v[30:31], v17 offset:33792\n" \
    "s_waitcnt lgkmcnt(6)\n" \
    "v_mfma_f32_32x32x16_bf16 v[96:111], v[48:51], a[92:95], v[96:111]\n" \
    "ds_read_b64_tr_b16 v[32:33], v18 offset:32768\n" \
    "ds_read_b64_tr_b16 v[34:35], v18 offset:33792\n" \
    "s_waitcnt lgkmcnt(6)\n" \
    "v_mfma_f32_32x32x16_bf16 v[112:127], v[52:55], a[92:95], v[112:127]\n" \
    "ds_read_b64_tr_b16 v[36:37], v19 offset:32768\n" \
    "ds_read_b64_tr_b16 v[38:39], v19 offset:33792\n" \
    "s_cmp_lg_u32 s54, 0\n" \
    "s_cbranch_scc1 QKC6%=\n" \
    "s_add_u32 s46, s46, 8192\n" \
    "s_addc_u32 s47, s47, 0\n" \
    "global_load_dwordx4 a[128:131], v6, s[46:47] offset:-4096\n" \
    "global_load_dwordx4 a[132:135], v6, s[46:47] offset:-3072\n" \
    "global_load_dwordx4 a[136:139], v6, s[46:47] offset:-2048\n" \
    "global_load_dwordx4 a[140:143], v6, s[46:47] offset:-1024\n" \
    "QKC6%=:\n" \
    "s_waitcnt lgkmcnt(6)\n" \
    "v_mfma_f32_32x32x16_bf16 v[64:79], v[24:27], a[96:99], v[64:79]\n" \
    "ds_read_b64_tr_b16 v[40:41], v16 offset:36864\n" \
    "ds_read_b64_tr_b16 v[42:43], v16 offset:37888\n" \
    "s_waitcnt lgkmcnt(6)\n" \
    "v_mfma_f32_32x32x16_bf16 v[80:95], v[28:31], a[96:99], v[80:95]\n" \
    "ds_read_b64_tr_b16 v[44:45], v17 offset:36864\n" \
    "ds_read_b64_tr_b16 v[46:47], v17 offset:37888\n" \
    "s_waitcnt lgkmcnt(6)\n" \
    "v_mfma_f32_32x32x16_bf16 v[96:111], v[32:35], a[96:99], v[96:111]\n" \
    "ds_read_b64_tr_b16 v[48:49], v18 offset:36864\n" \
    "ds_read_b64_tr_b16 v[50:51], v18 offset:37888\n" \
    "s_waitcnt lgkmcnt(6)\n" \
    "v_mfma_f32_32x32x16_bf16 v[112:127], v[36:39], a[96:99], v[112:127]\n" \
    "ds_read_b64_tr_b16 v[52:53], v19 offset:36864\n" \
    "ds_read_b64_tr_b16 v[54:55], v19 offset:37888\n" \
    "s_waitcnt lgkmcnt(6)\n" \
    "v_mfma_f32_32x32x16_bf16 v[64:79], v[40:43], a[100:103], v[64:79]\n" \
    "ds_read_b64_tr_b16 v[24:25], v16 offset:40960\n" \
    "ds_read_b64_tr_b16 v[26:27], v16 offset:41984\n" \
    "s_waitcnt lgkmcnt(6)\n" \
    "v_mfma_f32_32x32x16_bf16 v[80:95], v[44:47], a[100:103], v[80:95]\n" \
    "ds_read_b64_tr_b16 v[28:29], v17 offset:40960\n" \
    "ds_read_b64_tr_b16 v[30:31], v17 offset:41984\n" \
    "s_waitcnt lgkmcnt(6)\n" \
    "v_mfma_f32_32x32x16_bf16 v[96:111], v[48:51], a[100:103], v[96:111]\n" \
    "ds_read_b64_tr_b16 v[32:33], v18 offset:40960\n" \
    "ds_read_b64_tr_b16 v[34:35], v18 offset:41984\n" \
    "s_waitcnt lgkmcnt(6)\n" \
    "v_mfma_f32_32x32x16_bf16 v[112:127], v[52:55], a[100:103], v[112:127]\n" \
    "ds_read_b64_tr_b16 v[36:37], v19 offset:40960\n" \
    "ds_read_b64_tr_b16 v[38:39], v19 offset:41984\n" \
    "s_cmp_eq_u32 s54, 0\n" \
    "s_cbranch_scc1 QKW6%=\n" \
    "s_waitcnt vmcnt(24)\n" \
    "s_cmp_eq_u32 s55, 0\n" \
    "s_cbranch_scc1 QKX6%=\n" \
    "s_waitcnt vmcnt(8)\n" \
    "s_branch QKX6%=\n" \
    "QKW6%=:\n" \
    "s_waitcnt vmcnt(12)\n" \
    "s_waitcnt vmcnt(8)\n" \
    "s_cmp_eq_u32 s55, 0\n" \
    "s_cbranch_scc1 QKX6%=\n" \
    "s_waitcnt vmcnt(8)\n" \
    "s_waitcnt vmcnt(4)\n" \
    "QKX6%=:\n" \
    "s_barrier\n" \
    "s_waitcnt lgkmcnt(6)\n" \
    "s_add_u32 m0, s51, 65536\n" \
    "s_cmp_lg_u32 s55, 0\n" \
    "v_mfma_f32_32x32x16_bf16 v[64:79], v[24:27], a[104:107], v[64:79]\n" \
    "s_cbranch_scc1 QKG6_0%=\n" \
    "global_load_lds_dwordx4 v1, s[42:43]\n" \
    "QKG6_0%=:\n" \
    "ds_read_b64_tr_b16 v[40:41], v16 offset:45056\n" \
    "ds_read_b64_tr_b16 v[42:43], v16 offset:46080\n" \
    "s_waitcnt lgkmcnt(6)\n" \
    "s_add_u32 m0, s51, 66560\n" \
    "s_cmp_lg_u32 s55, 0\n" \
    "v_mfma_f32_32x32x16_bf16 v[80:95], v[28:31], a[104:107], v[80:95]\n" \
    "s_cbranch_scc1 QKG6_1%=\n" \
    "global_load_lds_dwordx4 v2, s[42:43]\n" \
    "QKG6_1%=:\n" \
    "ds_read_b64_tr_b16 v[44:45], v17 offset:45056\n" \
    "ds_read_b64_tr_b16 v[46:47], v17 offset:46080\n" \
    "s_waitcnt lgkmcnt(6)\n" \
    "s_add_u32 m0, s51, 67584\n" \
    "s_cmp_lg_u32 s55, 0\n" \
    "v_mfma_f32_32x32x16_bf16 v[96:111], v[32:35], a[104:107], v[96:111]\n" \
    "s_cbranch_scc1 QKG6_2%=\n" \
    "global_load_lds_dwordx4 v3, s[42:43]\n" \
    "QKG6_2%=:\n" \
    "ds_read_b64_tr_b16 v[48:49], v18 offset:45056\n" \
    "ds_read_b64_tr_b16 v[50:51], v18 offset:46080\n" \
    "s_waitcnt lgkmcnt(6)\n" \
    "s_add_u32 m0, s51, 68608\n" \
    "s_cmp_lg_u32 s55, 0\n" \
    "v_mfma_f32_32x32x16_bf16 v[112:127], v[36:39], a[104:107], v[112:127]\n" \
    "s_cbranch_scc1 QKG6_3%=\n" \
    "global_load_lds_dwordx4 v4, s[42:43]\n" \
    "s_add_u32 s42, s42, 524288\n" \
    "s_addc_u32 s43, s43, 0\n" \
    "QKG6_3%=:\n" \
    "ds_read_b64_tr_b16 v[52:53], v19 offset:45056\n" \
    "ds_read_b64_tr_b16 v[54:55], v19 offset:46080\n" \
    "s_waitcnt lgkmcnt(6)\n" \
    "v_mfma_f32_32x32x16_bf16 v[64:79], v[40:43], a[108:111], v[64:79]\n" \
    "ds_read_b64_tr_b16 v[24:25], v16 offset:49152\n" \
    "ds_read_b64_tr_b16 v[26:27], v16 offset:50176\n" \
    "s_waitcnt lgkmcnt(6)\n" \
    "v_mfma_f32_32x32x16_bf16 v[80:95], v[44:47], a[108:111], v[80:95]\n" \
    "ds_read_b64_tr_b16 v[28:29], v17 offset:49152\n" \
    "ds_read_b64_tr_b16 v[30:31], v17 offset:50176\n" \
    "s_waitcnt lgkmcnt(6)\n" \
    "v_mfma_f32_32x32x16_bf16 v[96:111], v[48:51], a[108:111], v[96:111]\n" \
    "ds_read_b64_tr_b16 v[32:33], v18 offset:49152\n" \
    "ds_read_b64_tr_b16 v[34:35], v18 offset:50176\n" \
    "s_waitcnt lgkmcnt(6)\n" \
    "v_mfma_f32_32x32x16_bf16 v[112:127], v[52:55], a[108:111], v[112:127]\n" \
    "ds_read_b64_tr_b16 v[36:37], v19 offset:49152\n" \
    "ds_read_b64_tr_b16 v[38:39], v19 offset:50176\n" \
    "s_cmp_lg_u32 s54, 0\n" \
    "s_cbranch_scc1 QKC7%=\n" \
    "global_load_dwordx4 a[144:147], v6, s[46:47] offset:0\n" \
    "global_load_dwordx4 a[148:151], v6, s[46:47] offset:1024\n" \
    "global_load_dwordx4 a[152:155], v6, s[46:47] offset:2048\n" \
    "global_load_dwordx4 a[156:159], v6, s[46:47] offset:3072\n" \
    "QKC7%=:\n" \
    "s_waitcnt lgkmcnt(6)\n" \
    "v_mfma_f32_32x32x16_bf16 v[64:79], v[24:27], a[112:115], v[64:79]\n" \
    "ds_read_b64_tr_b16 v[40:41], v16 offset:53248\n" \
    "ds_read_b64_tr_b16 v[42:43], v16 offset:54272\n" \
    "s_waitcnt lgkmcnt(6)\n" \
    "v_mfma_f32_32x32x16_bf16 v[80:95], v[28:31], a[112:115], v[80:95]\n" \
    "ds_read_b64_tr_b16 v[44:45], v17 offset:53248\n" \
    "ds_read_b64_tr_b16 v[46:47], v17 offset:54272\n" \
    "s_waitcnt lgkmcnt(6)\n" \
    "v_mfma_f32_32x32x16_bf16 v[96:111], v[32:35], a[112:115], v[96:111]\n" \
    "ds_read_b64_tr_b16 v[48:49], v18 offset:53248\n" \
    "ds_read_b64_tr_b16 v[50:51], v18 offset:54272\n" \
    "s_waitcnt lgkmcnt(6)\n" \
    "v_mfma_f32_32x32x16_bf16 v[112:127], v[36:39], a[112:115], v[112:127]\n" \
    "ds_read_b64_tr_b16 v[52:53], v19 offset:53248\n" \
    "ds_read_b64_tr_b16 v[54:55], v19 offset:54272\n" \
    "s_waitcnt lgkmcnt(6)\n" \
    "v_mfma_f32_32x32x16_bf16 v[64:79], v[40:43], a[116:119], v[64:79]\n" \
    "ds_read_b64_tr_b16 v[24:25], v16 offset:57344\n" \
    "ds_read_b64_tr_b16 v[26:27], v16 offset:58368\n" \
    "s_waitcnt lgkmcnt(6)\n" \
    "v_mfma_f32_32x32x16_bf16 v[80:95], v[44:47], a[116:119], v[80:95]\n" \
    "ds_read_b64_tr_b16 v[28:29], v17 offset:57344\n" \
    "ds_read_b64_tr_b16 v[30:31], v17 offset:58368\n" \
    "s_waitcnt lgkmcnt(6)\n" \
    "v_mfma_f32_32x32x16_bf16 v[96:111], v[48:51], a[116:119], v[96:111]\n" \
    "ds_read_b64_tr_b16 v[32:33], v18 offset:57344\n" \
    "ds_read_b64_tr_b16 v[34:35], v18 offset:58368\n" \
    "s_waitcnt lgkmcnt(6)\n" \
    "v_mfma_f32_32x32x16_bf16 v[112:127], v[52:55], a[116:119], v[112:127]\n" \
    "ds_read_b64_tr_b16 v[36:37], v19 offset:57344\n" \
    "ds_read_b64_tr_b16 v[38:39], v19 offset:58368\n" \
    "s_cmp_eq_u32 s54, 0\n" \
    "s_cbranch_scc1 QKW7%=\n" \
    "s_waitcnt vmcnt(24)\n" \
    "s_cmp_eq_u32 s55, 0\n" \
    "s_cbranch_scc1 QKX7%=\n" \
    "s_waitcnt vmcnt(4)\n" \
    "s_branch QKX7%=\n" \
    "QKW7%=:\n" \
    "s_waitcnt vmcnt(8)\n" \
    "s_cmp_eq_u32 s55, 0\n" \
    "s_cbranch_scc1 QKX7%=\n" \
    "s_waitcnt vmcnt(4)\n" \
    "QKX7%=:\n" \
    "s_barrier\n" \
    "s_waitcnt lgkmcnt(6)\n" \
    "s_add_u32 m0, s51, 81920\n" \
    "s_cmp_lg_u32 s55, 0\n" \
    "v_mfma_f32_32x32x16_bf16 v[64:79], v[24:27], a[120:123], v[64:79]\n" \
    "s_cbranch_scc1 QKG7_0%=\n" \
    "global_load_lds_dwordx4 v1, s[42:43]\n" \
    "QKG7_0%=:\n" \
    "ds_read_b64_tr_b16 v[40:41], v16 offset:61440\n" \
    "ds_read_b64_tr_b16 v[42:43], v16 offset:62464\n" \
    "s_waitcnt lgkmcnt(6)\n" \
    "s_add_u32 m0, s51, 82944\n" \
    "s_cmp_lg_u32 s55, 0\n" \
    "v_mfma_f32_32x32x16_bf16 v[80:95], v[28:31], a[120:123], v[80:95]\n" \
    "s_cbranch_scc1 QKG7_1%=\n" \
    "global_load_lds_dwordx4 v2, s[42:43]\n" \
    "QKG7_1%=:\n" \
    "ds_read_b64_tr_b16 v[44:45], v17 offset:61440\n" \
    "ds_read_b64_tr_b16 v[46:47], v17 offset:62464\n" \
    "s_waitcnt lgkmcnt(6)\n" \
    "s_add_u32 m0, s51, 83968\n" \
    "s_cmp_lg_u32 s55, 0\n" \
    "v_mfma_f32_32x32x16_bf16 v[96:111], v[32:35], a[120:123], v[96:111]\n" \
    "s_cbranch_scc1 QKG7_2%=\n" \
    "global_load_lds_dwordx4 v3, s[42:43]\n" \
    "QKG7_2%=:\n" \
    "ds_read_b64_tr_b16 v[48:49], v18 offset:61440\n" \
    "ds_read_b64_tr_b16 v[50:51], v18 offset:62464\n" \
    "s_waitcnt lgkmcnt(6)\n" \
    "s_add_u32 m0, s51, 84992\n" \
    "s_cmp_lg_u32 s55, 0\n" \
    "v_mfma_f32_32x32x16_bf16 v[112:127], v[36:39], a[120:123], v[112:127]\n" \
    "s_cbranch_scc1 QKG7_3%=\n" \
    "global_load_lds_dwordx4 v4, s[42:43]\n" \
    "s_add_u32 s42, s42, 524288\n" \
    "s_addc_u32 s43, s43, 0\n" \
    "QKG7_3%=:\n" \
    "ds_read_b64_tr_b16 v[52:53], v19 offset:61440\n" \
    "ds_read_b64_tr_b16 v[54:55], v19 offset:62464\n" \
    "s_waitcnt lgkmcnt(6)\n" \
    "v_mfma_f32_32x32x16_bf16 v[64:79], v[40:43], a[124:127], v[64:79]\n" \
    "ds_read_b64_tr_b16 v[24:25], v20 offset:0\n" \
    "ds_read_b64_tr_b16 v[26:27], v20 offset:1024\n" \
    "s_waitcnt lgkmcnt(6)\n" \
    "v_mfma_f32_32x32x16_bf16 v[80:95], v[44:47], a[124:127], v[80:95]\n" \
    "ds_read_b64_tr_b16 v[28:29], v21 offset:0\n" \
    "ds_read_b64_tr_b16 v[30:31], v21 offset:1024\n" \
    "s_waitcnt lgkmcnt(6)\n" \
    "v_mfma_f32_32x32x16_bf16 v[96:111], v[48:51], a[124:127], v[96:111]\n" \
    "ds_read_b64_tr_b16 v[32:33], v22 offset:0\n" \
    "ds_read_b64_tr_b16 v[34:35], v22 offset:1024\n" \
    "s_waitcnt lgkmcnt(6)\n" \
    "v_mfma_f32_32x32x16_bf16 v[112:127], v[52:55], a[124:127], v[112:127]\n" \
    "ds_read_b64_tr_b16 v[36:37], v23 offset:0\n" \
    "ds_read_b64_tr_b16 v[38:39], v23 offset:1024\n" \
    "s_waitcnt lgkmcnt(6)\n" \
    "v_mfma_f32_32x32x16_bf16 v[64:79], v[24:27], a[128:131], v[64:79]\n" \
    "ds_read_b64_tr_b16 v[40:41], v20 offset:4096\n" \
    "ds_read_b64_tr_b16 v[42:43], v20 offset:5120\n" \
    "s_waitcnt lgkmcnt(6)\n" \
    "v_mfma_f32_32x32x16_bf16 v[80:95], v[28:31], a[128:131], v[80:95]\n" \
    "ds_read_b64_tr_b16 v[44:45], v21 offset:4096\n" \
    "ds_read_b64_tr_b16 v[46:47], v21 offset:5120\n" \
    "s_waitcnt lgkmcnt(6)\n" \
    "v_mfma_f32_32x32x16_bf16 v[96:111], v[32:35], a[128:131], v[96:111]\n" \
    "ds_read_b64_tr_b16 v[48:49], v22 offset:4096\n" \
    "ds_read_b64_tr_b16 v[50:51], v22 offset:5120\n" \
    "s_waitcnt lgkmcnt(6)\n" \
    "v_mfma_f32_32x32x16_bf16 v[112:127], v[36:39], a[128:131], v[112:127]\n" \
    "ds_read_b64_tr_b16 v[52:53], v23 offset:4096\n" \
    "ds_read_b64_tr_b16 v[54:55], v23 offset:5120\n" \
    "s_waitcnt lgkmcnt(6)\n" \
    "v_mfma_f32_32x32x16_bf16 v[64:79], v[40:43], a[132:135], v[64:79]\n" \
    "ds_read_b64_tr_b16 v[24:25], v20 offset:8192\n" \
    "ds_read_b64_tr_b16 v[26:27], v20 offset:9216\n" \
    "s_waitcnt lgkmcnt(6)\n" \
    "v_mfma_f32_32x32x16_bf16 v[80:95], v[44:47], a[132:135], v[80:95]\n" \
    "ds_read_b64_tr_b16 v[28:29], v21 offset:8192\n" \
    "ds_read_b64_tr_b16 v[30:31], v21 offset:9216\n" \
    "s_waitcnt lgkmcnt(6)\n" \
    "v_mfma_f32_32x32x16_bf16 v[96:111], v[48:51], a[132:135], v[96:111]\n" \
    "ds_read_b64_tr_b16 v[32:33], v22 offset:8192\n" \
    "ds_read_b64_tr_b16 v[34:35], v22 offset:9216\n" \
    "s_waitcnt lgkmcnt(6)\n" \
    "v_mfma_f32_32x32x16_bf16 v[112:127], v[52:55], a[132:135], v[112:127]\n" \
    "ds_read_b64_tr_b16 v[36:37], v23 offset:8192\n" \
    "ds_read_b64_tr_b16 v[38:39], v23 offset:9216\n" \
    "s_cmp_eq_u32 s54, 0\n" \
    "s_cbranch_scc1 QKW8%=\n" \
    "s_waitcnt vmcnt(24)\n" \
    "s_cmp_eq_u32 s55, 0\n" \
    "s_cbranch_scc1 QKX8%=\n" \
    "s_waitcnt vmcnt(0)\n" \
    "s_branch QKX8%=\n" \
    "QKW8%=:\n" \
    "s_waitcnt vmcnt(4)\n" \
    "s_cmp_eq_u32 s55, 0\n" \
    "s_cbranch_scc1 QKX8%=\n" \
    "s_waitcnt vmcnt(0)\n" \
    "QKX8%=:\n" \
    "s_barrier\n" \
    "s_waitcnt lgkmcnt(6)\n" \
    "s_add_u32 m0, s51, 98304\n" \
    "s_cmp_lg_u32 s55, 0\n" \
    "v_mfma_f32_32x32x16_bf16 v[64:79], v[24:27], a[136:139], v[64:79]\n" \
    "s_cbranch_scc1 QKG8_0%=\n" \
    "global_load_lds_dwordx4 v1, s[42:43]\n" \
    "QKG8_0%=:\n" \
    "ds_read_b64_tr_b16 v[40:41], v20 offset:12288\n" \
    "ds_read_b64_tr_b16 v[42:43], v20 offset:13312\n" \
    "s_waitcnt lgkmcnt(6)\n" \
    "s_add_u32 m0, s51, 99328\n" \
    "s_cmp_lg_u32 s55, 0\n" \
    "v_mfma_f32_32x32x16_bf16 v[80:95], v[28:31], a[136:139], v[80:95]\n" \
    "s_cbranch_scc1 QKG8_1%=\n" \
    "global_load_lds_dwordx4 v2, s[42:43]\n" \
    "QKG8_1%=:\n" \
    "ds_read_b64_tr_b16 v[44:45], v21 offset:12288\n" \
    "ds_read_b64_tr_b16 v[46:47], v21 offset:13312\n" \
    "s_waitcnt lgkmcnt(6)\n" \
    "s_add_u32 m0, s51, 100352\n" \
    "s_cmp_lg_u32 s55, 0\n" \
    "v_mfma_f32_32x32x16_bf16 v[96:111], v[32:35], a[136:139], v[96:111]\n" \
    "s_cbranch_scc1 QKG8_2%=\n" \
    "global_load_lds_dwordx4 v3, s[42:43]\n" \
    "QKG8_2%=:\n" \
    "ds_read_b64_tr_b16 v[48:49], v22 offset:12288\n" \
    "ds_read_b64_tr_b16 v[50:51], v22 offset:13312\n" \
    "s_waitcnt lgkmcnt(6)\n" \
    "s_add_u32 m0, s51, 101376\n" \
    "s_cmp_lg_u32 s55, 0\n" \
    "v_mfma_f32_32x32x16_bf16 v[112:127], v[36:39], a[136:139], v[112:127]\n" \
    "s_cbranch_scc1 QKG8_3%=\n" \
    "global_load_lds_dwordx4 v4, s[42:43]\n" \
    "s_add_u32 s42, s42, 524288\n" \
    "s_addc_u32 s43, s43, 0\n" \
    "QKG8_3%=:\n" \
    "ds_read_b64_tr_b16 v[52:53], v23 offset:12288\n" \
    "ds_read_b64_tr_b16 v[54:55], v23 offset:13312\n" \
    "s_waitcnt lgkmcnt(6)\n" \
    "v_mfma_f32_32x32x16_bf16 v[64:79], v[40:43], a[140:143], v[64:79]\n" \
    "ds_read_b64_tr_b16 v[24:25], v20 offset:16384\n" \
    "ds_read_b64_tr_b16 v[26:27], v20 offset:17408\n" \
    "s_waitcnt lgkmcnt(6)\n" \
    "v_mfma_f32_32x32x16_bf16 v[80:95], v[44:47], a[140:143], v[80:95]\n" \
    "ds_read_b64_tr_b16 v[28:29], v21 offset:16384\n" \
    "ds_read_b64_tr_b16 v[30:31], v21 offset:17408\n" \
    "s_waitcnt lgkmcnt(6)\n" \
    "v_mfma_f32_32x32x16_bf16 v[96:111], v[48:51], a[140:143], v[96:111]\n" \
    "ds_read_b64_tr_b16 v[32:33], v22 offset:16384\n" \
    "ds_read_b64_tr_b16 v[34:35], v22 offset:17408\n" \
    "s_waitcnt lgkmcnt(6)\n" \
    "v_mfma_f32_32x32x16_bf16 v[112:127], v[52:55], a[140:143], v[112:127]\n" \
    "ds_read_b64_tr_b16 v[36:37], v23 offset:16384\n" \
    "ds_read_b64_tr_b16 v[38:39], v23 offset:17408\n" \
    "s_waitcnt lgkmcnt(6)\n" \
    "v_mfma_f32_32x32x16_bf16 v[64:79], v[24:27], a[144:147], v[64:79]\n" \
    "ds_read_b64_tr_b16 v[40:41], v20 offset:20480\n" \
    "ds_read_b64_tr_b16 v[42:43], v20 offset:21504\n" \
    "s_waitcnt lgkmcnt(6)\n" \
    "v_mfma_f32_32x32x16_bf16 v[80:95], v[28:31], a[144:147], v[80:95]\n" \
    "ds_read_b64_tr_b16 v[44:45], v21 offset:20480\n" \
    "ds_read_b64_tr_b16 v[46:47], v21 offset:21504\n" \
    "s_waitcnt lgkmcnt(6)\n" \
    "v_mfma_f32_32x32x16_bf16 v[96:111], v[32:35], a[144:147], v[96:111]\n" \
    "ds_read_b64_tr_b16 v[48:49], v22 offset:20480\n" \
    "ds_read_b64_tr_b16 v[50:51], v22 offset:21504\n" \
    "s_waitcnt lgkmcnt(6)\n" \
    "v_mfma_f32_32x32x16_bf16 v[112:127], v[36:39], a[144:147], v[112:127]\n" \
    "ds_read_b64_tr_b16 v[52:53], v23 offset:20480\n" \
    "ds_read_b64_tr_b16 v[54:55], v23 offset:21504\n" \
    "s_waitcnt lgkmcnt(6)\n" \
    "v_mfma_f32_32x32x16_bf16 v[64:79], v[40:43], a[148:151], v[64:79]\n" \
    "ds_read_b64_tr_b16 v[24:25], v20 offset:24576\n" \
    "ds_read_b64_tr_b16 v[26:27], v20 offset:25600\n" \
    "s_waitcnt lgkmcnt(6)\n" \
    "v_mfma_f32_32x32x16_bf16 v[80:95], v[44:47], a[148:151], v[80:95]\n" \
    "ds_read_b64_tr_b16 v[28:29], v21 offset:24576\n" \
    "ds_read_b64_tr_b16 v[30:31], v21 offset:25600\n" \
    "s_waitcnt lgkmcnt(6)\n" \
    "v_mfma_f32_32x32x16_bf16 v[96:111], v[48:51], a[148:151], v[96:111]\n" \
    "ds_read_b64_tr_b16 v[32:33], v22 offset:24576\n" \
    "ds_read_b64_tr_b16 v[34:35], v22 offset:25600\n" \
    "s_waitcnt lgkmcnt(6)\n" \
    "v_mfma_f32_32x32x16_bf16 v[112:127], v[52:55], a[148:151], v[112:127]\n" \
    "ds_read_b64_tr_b16 v[36:37], v23 offset:24576\n" \
    "ds_read_b64_tr_b16 v[38:39], v23 offset:25600\n" \
    "s_cmp_eq_u32 s54, 0\n" \
    "s_cbranch_scc1 QKW9%=\n" \
    "s_waitcnt vmcnt(24)\n" \
    "s_cmp_eq_u32 s55, 0\n" \
    "s_cbranch_scc1 QKX9%=\n" \
    "s_branch QKX9%=\n" \
    "QKW9%=:\n" \
    "s_cmp_eq_u32 s55, 0\n" \
    "s_cbranch_scc1 QKX9%=\n" \
    "QKX9%=:\n" \
    "s_barrier\n" \
    "s_waitcnt lgkmcnt(6)\n" \
    "s_add_u32 m0, s51, 114688\n" \
    "s_cmp_lg_u32 s55, 0\n" \
    "v_mfma_f32_32x32x16_bf16 v[64:79], v[24:27], a[152:155], v[64:79]\n" \
    "s_cbranch_scc1 QKG9_0%=\n" \
    "global_load_lds_dwordx4 v1, s[42:43]\n" \
    "QKG9_0%=:\n" \
    "ds_read_b64_tr_b16 v[40:41], v20 offset:28672\n" \
    "ds_read_b64_tr_b16 v[42:43], v20 offset:29696\n" \
    "s_waitcnt lgkmcnt(6)\n" \
    "s_add_u32 m0, s51, 115712\n" \
    "s_cmp_lg_u32 s55, 0\n" \
    "v_mfma_f32_32x32x16_bf16 v[80:95], v[28:31], a[152:155], v[80:95]\n" \
    "s_cbranch_scc1 QKG9_1%=\n" \
    "global_load_lds_dwordx4 v2, s[42:43]\n" \
    "QKG9_1%=:\n" \
    "ds_read_b64_tr_b16 v[44:45], v21 offset:28672\n" \
    "ds_read_b64_tr_b16 v[46:47], v21 offset:29696\n" \
    "s_waitcnt lgkmcnt(6)\n" \
    "s_add_u32 m0, s51, 116736\n" \
    "s_cmp_lg_u32 s55, 0\n" \
    "v_mfma_f32_32x32x16_bf16 v[96:111], v[32:35], a[152:155], v[96:111]\n" \
    "s_cbranch_scc1 QKG9_2%=\n" \
    "global_load_lds_dwordx4 v3, s[42:43]\n" \
    "QKG9_2%=:\n" \
    "ds_read_b64_tr_b16 v[48:49], v22 offset:28672\n" \
    "ds_read_b64_tr_b16 v[50:51], v22 offset:29696\n" \
    "s_waitcnt lgkmcnt(6)\n" \
    "s_add_u32 m0, s51, 117760\n" \
    "s_cmp_lg_u32 s55, 0\n" \
    "v_mfma_f32_32x32x16_bf16 v[112:127], v[36:39], a[152:155], v[112:127]\n" \
    "s_cbranch_scc1 QKG9_3%=\n" \
    "global_load_lds_dwordx4 v4, s[42:43]\n" \
    "s_add_u32 s42, s42, 524288\n" \
    "s_addc_u32 s43, s43, 0\n" \
    "QKG9_3%=:\n" \
    "ds_read_b64_tr_b16 v[52:53], v23 offset:28672\n" \
    "ds_read_b64_tr_b16 v[54:55], v23 offset:29696\n" \
    "s_waitcnt lgkmcnt(6)\n" \
    "v_mfma_f32_32x32x16_bf16 v[64:79], v[40:43], a[156:159], v[64:79]\n" \
    "ds_read_b64_tr_b16 v[24:25], v12 offset:0\n" \
    "ds_read_b64_tr_b16 v[26:27], v12 offset:1024\n" \
    "s_waitcnt lgkmcnt(6)\n" \
    "v_mfma_f32_32x32x16_bf16 v[80:95], v[44:47], a[156:159], v[80:95]\n" \
    "ds_read_b64_tr_b16 v[28:29], v13 offset:0\n" \
    "ds_read_b64_tr_b16 v[30:31], v13 offset:1024\n" \
    "s_waitcnt lgkmcnt(6)\n" \
    "v_mfma_f32_32x32x16_bf16 v[96:111], v[48:51], a[156:159], v[96:111]\n" \
    "ds_read_b64_tr_b16 v[32:33], v14 offset:0\n" \
    "ds_read_b64_tr_b16 v[34:35], v14 offset:1024\n" \
    "s_waitcnt lgkmcnt(6)\n" \
    "v_mfma_f32_32x32x16_bf16 v[112:127], v[52:55], a[156:159], v[112:127]\n" \
    "ds_read_b64_tr_b16 v[36:37], v15 offset:0\n" \
    "ds_read_b64_tr_b16 v[38:39], v15 offset:1024\n" \
    "s_branch QE%=\n" \
    "QV%=:\n" \
    "s_cmp_lg_u32 s54, 0\n" \
    "s_cbranch_scc1 QVC0%=\n" \
    "s_add_u32 s46, s46, 8192\n" \
    "s_addc_u32 s47, s47, 0\n" \
    "global_load_dwordx4 a[32:35], v6, s[46:47] offset:-4096\n" \
    "global_load_dwordx4 a[36:39], v6, s[46:47] offset:-3072\n" \
    "global_load_dwordx4 a[40:43], v6, s[46:47] offset:-2048\n" \
    "global_load_dwordx4 a[44:47], v6, s[46:47] offset:-1024\n" \
    "s_add_u32 m0, s51, 32768\n" \
    "s_nop 0\n" \
    "global_load_lds_dwordx4 v1, s[48:49]\n" \
    "s_add_u32 m0, s51, 33792\n" \
    "s_nop 0\n" \
    "global_load_lds_dwordx4 v2, s[48:49]\n" \
    "s_add_u32 m0, s51, 34816\n" \
    "s_nop 0\n" \
    "global_load_lds_dwordx4 v3, s[48:49]\n" \
    "s_add_u32 m0, s51, 35840\n" \
    "s_nop 0\n" \
    "global_load_lds_dwordx4 v4, s[48:49]\n" \
    "s_add_u32 s48, s48, 524288\n" \
    "s_addc_u32 s49, s49, 0\n" \
    "QVC0%=:\n" \
    "s_cmp_eq_u32 s54, 0\n" \
    "s_cbranch_scc1 QVS%=\n" \
    "global_store_dwordx4 v9, v[128:131], s[46:47] offset:-4096 sc1\n" \
    "global_store_dwordx4 v9, v[132:135], s[46:47] offset:-3584 sc1\n" \
    "global_store_dwordx4 v9, v[136:139], s[46:47] offset:-2048 sc1\n" \
    "global_store_dwordx4 v9, v[140:143], s[46:47] offset:-1536 sc1\n" \
    "global_store_dwordx4 v9, v[144:147], s[46:47] offset:0 sc1\n" \
    "global_store_dwordx4 v9, v[148:151], s[46:47] offset:512 sc1\n" \
    "global_store_dwordx4 v9, v[152:155], s[46:47] offset:2048 sc1\n" \
    "global_store_dwordx4 v9, v[156:159], s[46:47] offset:2560 sc1\n" \
    "QVS%=:\n" \
    "s_waitcnt lgkmcnt(6)\n" \
    "v_mfma_f32_32x32x16_bf16 v[64:79], a[0:3], v[24:27], 0\n" \
    "ds_read_b64_tr_b16 v[40:41], v12 offset:4096\n" \
    "ds_read_b64_tr_b16 v[42:43], v12 offset:5120\n" \
    "s_waitcnt lgkmcnt(6)\n" \
    "v_mfma_f32_32x32x16_bf16 v[80:95], a[0:3], v[28:31], 0\n" \
    "ds_read_b64_tr_b16 v[44:45], v13 offset:4096\n" \
    "ds_read_b64_tr_b16 v[46:47], v13 offset:5120\n" \
    "s_waitcnt lgkmcnt(6)\n" \
    "v_mfma_f32_32x32x16_bf16 v[96:111], a[0:3], v[32:35], 0\n" \
    "ds_read_b64_tr_b16 v[48:49], v14 offset:4096\n" \
    "ds_read_b64_tr_b16 v[50:51], v14 offset:5120\n" \
    "s_waitcnt lgkmcnt(6)\n" \
    "v_mfma_f32_32x32x16_bf16 v[112:127], a[0:3], v[36:39], 0\n" \
    "ds_read_b64_tr_b16 v[52:53], v15 offset:4096\n" \
    "ds_read_b64_tr_b16 v[54:55], v15 offset:5120\n" \
    "s_waitcnt lgkmcnt(6)\n" \
    "v_mfma_f32_32x32x16_bf16 v[64:79], a[4:7], v[40:43], v[64:79]\n" \
    "ds_read_b64_tr_b16 v[24:25], v12 offset:8192\n" \
    "ds_read_b64_tr_b16 v[26:27], v12 offset:9216\n" \
    "s_waitcnt lgkmcnt(6)\n" \
    "v_mfma_f32_32x32x16_bf16 v[80:95], a[4:7], v[44:47], v[80:95]\n" \
    "ds_read_b64_tr_b16 v[28:29], v13 offset:8192\n" \
    "ds_read_b64_tr_b16 v[30:31], v13 offset:9216\n" \
    "s_waitcnt lgkmcnt(6)\n" \
    "v_mfma_f32_32x32x16_bf16 v[96:111], a[4:7], v[48:51], v[96:111]\n" \
    "ds_read_b64_tr_b16 v[32:33], v14 offset:8192\n" \
    "ds_read_b64_tr_b16 v[34:35], v14 offset:9216\n" \
    "s_waitcnt lgkmcnt(6)\n" \
    "v_mfma_f32_32x32x16_bf16 v[112:127], a[4:7], v[52:55], v[112:127]\n" \
    "ds_read_b64_tr_b16 v[36:37], v15 offset:8192\n" \
    "ds_read_b64_tr_b16 v[38:39], v15 offset:9216\n" \
    "s_cmp_eq_u32 s54, 0\n" \
    "s_cbranch_scc1 QVW0%=\n" \
    "s_waitcnt vmcnt(24)\n" \
    "s_cmp_eq_u32 s55, 0\n" \
    "s_cbranch_scc1 QVX0%=\n" \
    "s_waitcnt vmcnt(24)\n" \
    "s_branch QVX0%=\n" \
    "QVW0%=:\n" \
    "s_waitcnt vmcnt(12)\n" \
    "s_waitcnt vmcnt(8)\n" \
    "s_cmp_eq_u32 s55, 0\n" \
    "s_cbranch_scc1 QVX0%=\n" \
    "s_waitcnt vmcnt(12)\n" \
    "s_waitcnt vmcnt(8)\n" \
    "QVX0%=:\n" \
    "s_barrier\n" \
    "s_waitcnt lgkmcnt(6)\n" \
    "s_add_u32 m0, s51, 131072\n" \
    "v_mfma_f32_32x32x16_bf16 v[64:79], a[8:11], v[24:27], v[64:79]\n" \
    "global_load_lds_dwordx4 v1, s[42:43]\n" \
    "ds_read_b64_tr_b16 v[40:41], v12 offset:12288\n" \
    "ds_read_b64_tr_b16 v[42:43], v12 offset:13312\n" \
    "s_waitcnt lgkmcnt(6)\n" \
    "s_add_u32 m0, s51, 132096\n" \
    "v_mfma_f32_32x32x16_bf16 v[80:95], a[8:11], v[28:31], v[80:95]\n" \
    "global_load_lds_dwordx4 v2, s[42:43]\n" \
    "ds_read_b64_tr_b16 v[44:45], v13 offset:12288\n" \
    "ds_read_b64_tr_b16 v[46:47], v13 offset:13312\n" \
    "s_waitcnt lgkmcnt(6)\n" \
    "s_add_u32 m0, s51, 133120\n" \
    "v_mfma_f32_32x32x16_bf16 v[96:111], a[8:11], v[32:35], v[96:111]\n" \
    "global_load_lds_dwordx4 v3, s[42:43]\n" \
    "ds_read_b64_tr_b16 v[48:49], v14 offset:12288\n" \
    "ds_read_b64_tr_b16 v[50:51], v14 offset:13312\n" \
    "s_waitcnt lgkmcnt(6)\n" \
    "s_add_u32 m0, s51, 134144\n" \
    "v_mfma_f32_32x32x16_bf16 v[112:127], a[8:11], v[36:39], v[112:127]\n" \
    "global_load_lds_dwordx4 v4, s[42:43]\n" \
    "s_add_u32 s42, s42, 524288\n" \
    "s_addc_u32 s43, s43, 0\n" \
    "ds_read_b64_tr_b16 v[52:53], v15 offset:12288\n" \
    "ds_read_b64_tr_b16 v[54:55], v15 offset:13312\n" \
    "s_waitcnt lgkmcnt(6)\n" \
    "v_mfma_f32_32x32x16_bf16 v[64:79], a[12:15], v[40:43], v[64:79]\n" \
    "ds_read_b64_tr_b16 v[24:25], v12 offset:16384\n" \
    "ds_read_b64_tr_b16 v[26:27], v12 offset:17408\n" \
    "s_waitcnt lgkmcnt(6)\n" \
    "v_mfma_f32_32x32x16_bf16 v[80:95], a[12:15], v[44:47], v[80:95]\n" \
    "ds_read_b64_tr_b16 v[28:29], v13 offset:16384\n" \
    "ds_read_b64_tr_b16 v[30:31], v13 offset:17408\n" \
    "s_waitcnt lgkmcnt(6)\n" \
    "v_mfma_f32_32x32x16_bf16 v[96:111], a[12:15], v[48:51], v[96:111]\n" \
    "ds_read_b64_tr_b16 v[32:33], v14 offset:16384\n" \
    "ds_read_b64_tr_b16 v[34:35], v14 offset:17408\n" \
    "s_waitcnt lgkmcnt(6)\n" \
    "v_mfma_f32_32x32x16_bf16 v[112:127], a[12:15], v[52:55], v[112:127]\n" \
    "ds_read_b64_tr_b16 v[36:37], v15 offset:16384\n" \
    "ds_read_b64_tr_b16 v[38:39], v15 offset:17408\n" \
    "s_cmp_lg_u32 s54, 0\n" \
    "s_cbranch_scc1 QVC1%=\n" \
    "global_load_dwordx4 a[48:51], v6, s[46:47] offset:0\n" \
    "global_load_dwordx4 a[52:55], v6, s[46:47] offset:1024\n" \
    "global_load_dwordx4 a[56:59], v6, s[46:47] offset:2048\n" \
    "global_load_dwordx4 a[60:63], v6, s[46:47] offset:3072\n" \
    "s_add_u32 m0, s51, 49152\n" \
    "s_nop 0\n" \
    "global_load_lds_dwordx4 v1, s[48:49]\n" \
    "s_add_u32 m0, s51, 50176\n" \
    "s_nop 0\n" \
    "global_load_lds_dwordx4 v2, s[48:49]\n" \
    "s_add_u32 m0, s51, 51200\n" \
    "s_nop 0\n" \
    "global_load_lds_dwordx4 v3, s[48:49]\n" \
    "s_add_u32 m0, s51, 52224\n" \
    "s_nop 0\n" \
    "global_load_lds_dwordx4 v4, s[48:49]\n" \
    "s_add_u32 s48, s48, 524288\n" \
    "s_addc_u32 s49, s49, 0\n" \
    "QVC1%=:\n" \
    "s_waitcnt lgkmcnt(6)\n" \
    "v_mfma_f32_32x32x16_bf16 v[64:79], a[16:19], v[24:27], v[64:79]\n" \
    "ds_read_b64_tr_b16 v[40:41], v12 offset:20480\n" \
    "ds_read_b64_tr_b16 v[42:43], v12 offset:21504\n" \
    "s_waitcnt lgkmcnt(6)\n" \
    "v_mfma_f32_32x32x16_bf16 v[80:95], a[16:19], v[28:31], v[80:95]\n" \
    "ds_read_b64_tr_b16 v[44:45], v13 offset:20480\n" \
    "ds_read_b64_tr_b16 v[46:47], v13 offset:21504\n" \
    "s_waitcnt lgkmcnt(6)\n" \
    "v_mfma_f32_32x32x16_bf16 v[96:111], a[16:19], v[32:35], v[96:111]\n" \
    "ds_read_b64_tr_b16 v[48:49], v14 offset:20480\n" \
    "ds_read_b64_tr_b16 v[50:51], v14 offset:21504\n" \
    "s_waitcnt lgkmcnt(6)\n" \
    "v_mfma_f32_32x32x16_bf16 v[112:127], a[16:19], v[36:39], v[112:127]\n" \
    "ds_read_b64_tr_b16 v[52:53], v15 offset:20480\n" \
    "ds_read_b64_tr_b16 v[54:55], v15 offset:21504\n" \
    "s_waitcnt lgkmcnt(6)\n" \
    "v_mfma_f32_32x32x16_bf16 v[64:79], a[20:23], v[40:43], v[64:79]\n" \
    "ds_read_b64_tr_b16 v[24:25], v12 offset:24576\n" \
    "ds_read_b64_tr_b16 v[26:27], v12 offset:25600\n" \
    "s_waitcnt lgkmcnt(6)\n" \
    "v_mfma_f32_32x32x16_bf16 v[80:95], a[20:23], v[44:47], v[80:95]\n" \
    "ds_read_b64_tr_b16 v[28:29], v13 offset:24576\n" \
    "ds_read_b64_tr_b16 v[30:31], v13 offset:25600\n" \
    "s_waitcnt lgkmcnt(6)\n" \
    "v_mfma_f32_32x32x16_bf16 v[96:111], a[20:23], v[48:51], v[96:111]\n" \
    "ds_read_b64_tr_b16 v[32:33], v14 offset:24576\n" \
    "ds_read_b64_tr_b16 v[34:35], v14 offset:25600\n" \
    "s_waitcnt lgkmcnt(6)\n" \
    "v_mfma_f32_32x32x16_bf16 v[112:127], a[20:23], v[52:55], v[112:127]\n" \
    "ds_read_b64_tr_b16 v[36:37], v15 offset:24576\n" \
    "ds_read_b64_tr_b16 v[38:39], v15 offset:25600\n" \
    "s_cmp_eq_u32 s54, 0\n" \
    "s_cbranch_scc1 QVW1%=\n" \
    "s_waitcnt vmcnt(24)\n" \
    "s_cmp_eq_u32 s55, 0\n" \
    "s_cbranch_scc1 QVX1%=\n" \
    "s_waitcnt vmcnt(24)\n" \
    "s_branch QVX1%=\n" \
    "QVW1%=:\n" \
    "s_waitcnt vmcnt(16)\n" \
    "s_waitcnt vmcnt(12)\n" \
    "s_cmp_eq_u32 s55, 0\n" \
    "s_cbranch_scc1 QVX1%=\n" \
    "s_waitcnt vmcnt(16)\n" \
    "s_waitcnt vmcnt(12)\n" \
    "QVX1%=:\n" \
    "s_barrier\n" \
    "s_waitcnt lgkmcnt(6)\n" \
    "s_add_u32 m0, s51, 147456\n" \
    "v_mfma_f32_32x32x16_bf16 v[64:79], a[24:27], v[24:27], v[64:79]\n" \
    "global_load_lds_dwordx4 v1, s[42:43]\n" \
    "ds_read_b64_tr_b16 v[40:41], v12 offset:28672\n" \
    "ds_read_b64_tr_b16 v[42:43], v12 offset:29696\n" \
    "s_waitcnt lgkmcnt(6)\n" \
    "s_add_u32 m0, s51, 148480\n" \
    "v_mfma_f32_32x32x16_bf16 v[80:95], a[24:27], v[28:31], v[80:95]\n" \
    "global_load_lds_dwordx4 v2, s[42:43]\n" \
    "ds_read_b64_tr_b16 v[44:45], v13 offset:28672\n" \
    "ds_read_b64_tr_b16 v[46:47], v13 offset:29696\n" \
    "s_waitcnt lgkmcnt(6)\n" \
    "s_add_u32 m0, s51, 149504\n" \
    "v_mfma_f32_32x32x16_bf16 v[96:111], a[24:27], v[32:35], v[96:111]\n" \
    "global_load_lds_dwordx4 v3, s[42:43]\n" \
    "ds_read_b64_tr_b16 v[48:49], v14 offset:28672\n" \
    "ds_read_b64_tr_b16 v[50:51], v14 offset:29696\n" \
    "s_waitcnt lgkmcnt(6)\n" \
    "s_add_u32 m0, s51, 150528\n" \
    "v_mfma_f32_32x32x16_bf16 v[112:127], a[24:27], v[36:39], v[112:127]\n" \
    "global_load_lds_dwordx4 v4, s[42:43]\n" \
    "s_add_u32 s42, s42, 524288\n" \
    "s_addc_u32 s43, s43, 0\n" \
    "ds_read_b64_tr_b16 v[52:53], v15 offset:28672\n" \
    "ds_read_b64_tr_b16 v[54:55], v15 offset:29696\n" \
    "s_waitcnt lgkmcnt(6)\n" \
    "v_mfma_f32_32x32x16_bf16 v[64:79], a[28:31], v[40:43], v[64:79]\n" \
    "ds_read_b64_tr_b16 v[24:25], v12 offset:32768\n" \
    "ds_read_b64_tr_b16 v[26:27], v12 offset:33792\n" \
    "s_waitcnt lgkmcnt(6)\n" \
    "v_mfma_f32_32x32x16_bf16 v[80:95], a[28:31], v[44:47], v[80:95]\n" \
    "ds_read_b64_tr_b16 v[28:29], v13 offset:32768\n" \
    "ds_read_b64_tr_b16 v[30:31], v13 offset:33792\n" \
    "s_waitcnt lgkmcnt(6)\n" \
    "v_mfma_f32_32x32x16_bf16 v[96:111], a[28:31], v[48:51], v[96:111]\n" \
    "ds_read_b64_tr_b16 v[32:33], v14 offset:32768\n" \
    "ds_read_b64_tr_b16 v[34:35], v14 offset:33792\n" \
    "s_waitcnt lgkmcnt(6)\n" \
    "v_mfma_f32_32x32x16_bf16 v[112:127], a[28:31], v[52:55], v[112:127]\n" \
    "ds_read_b64_tr_b16 v[36:37], v15 offset:32768\n" \
    "ds_read_b64_tr_b16 v[38:39], v15 offset:33792\n" \
    "s_cmp_lg_u32 s54, 0\n" \
    "s_cbranch_scc1 QVC2%=\n" \
    "s_add_u32 s46, s46, 8192\n" \
    "s_addc_u32 s47, s47, 0\n" \
    "global_load_dwordx4 a[64:67], v6, s[46:47] offset:-4096\n" \
    "global_load_dwordx4 a[68:71], v6, s[46:47] offset:-3072\n" \
    "global_load_dwordx4 a[72:75], v6, s[46:47] offset:-2048\n" \
    "global_load_dwordx4 a[76:79], v6, s[46:47] offset:-1024\n" \
    "s_add_u32 m0, s51, 65536\n" \
    "s_nop 0\n" \
    "global_load_lds_dwordx4 v1, s[48:49]\n" \
    "s_add_u32 m0, s51, 66560\n" \
    "s_nop 0\n" \
    "global_load_lds_dwordx4 v2, s[48:49]\n" \
    "s_add_u32 m0, s51, 67584\n" \
    "s_nop 0\n" \
    "global_load_lds_dwordx4 v3, s[48:49]\n" \
    "s_add_u32 m0, s51, 68608\n" \
    "s_nop 0\n" \
    "global_load_lds_dwordx4 v4, s[48:49]\n" \
    "s_add_u32 s48, s48, 524288\n" \
    "s_addc_u32 s49, s49, 0\n" \
    "QVC2%=:\n" \
    "s_waitcnt lgkmcnt(6)\n" \
    "v_mfma_f32_32x32x16_bf16 v[64:79], a[32:35], v[24:27], v[64:79]\n" \
    "ds_read_b64_tr_b16 v[40:41], v12 offset:36864\n" \
    "ds_read_b64_tr_b16 v[42:43], v12 offset:37888\n" \
    "s_waitcnt lgkmcnt(6)\n" \
    "v_mfma_f32_32x32x16_bf16 v[80:95], a[32:35], v[28:31], v[80:95]\n" \
    "ds_read_b64_tr_b16 v[44:45], v13 offset:36864\n" \
    "ds_read_b64_tr_b16 v[46:47], v13 offset:37888\n" \
    "s_waitcnt lgkmcnt(6)\n" \
    "v_mfma_f32_32x32x16_bf16 v[96:111], a[32:35], v[32:35], v[96:111]\n" \
    "ds_read_b64_tr_b16 v[48:49], v14 offset:36864\n" \
    "ds_read_b64_tr_b16 v[50:51], v14 offset:37888\n" \
    "s_waitcnt lgkmcnt(6)\n" \
    "v_mfma_f32_32x32x16_bf16 v[112:127], a[32:35], v[36:39], v[112:127]\n" \
    "ds_read_b64_tr_b16 v[52:53], v15 offset:36864\n" \
    "ds_read_b64_tr_b16 v[54:55], v15 offset:37888\n" \
    "s_waitcnt lgkmcnt(6)\n" \
    "v_mfma_f32_32x32x16_bf16 v[64:79], a[36:39], v[40:43], v[64:79]\n" \
    "ds_read_b64_tr_b16 v[24:25], v12 offset:40960\n" \
    "ds_read_b64_tr_b16 v[26:27], v12 offset:41984\n" \
    "s_waitcnt lgkmcnt(6)\n" \
    "v_mfma_f32_32x32x16_bf16 v[80:95], a[36:39], v[44:47], v[80:95]\n" \
    "ds_read_b64_tr_b16 v[28:29], v13 offset:40960\n" \
    "ds_read_b64_tr_b16 v[30:31], v13 offset:41984\n" \
    "s_waitcnt lgkmcnt(6)\n" \
    "v_mfma_f32_32x32x16_bf16 v[96:111], a[36:39], v[48:51], v[96:111]\n" \
    "ds_read_b64_tr_b16 v[32:33], v14 offset:40960\n" \
    "ds_read_b64_tr_b16 v[34:35], v14 offset:41984\n" \
    "s_waitcnt lgkmcnt(6)\n" \
    "v_mfma_f32_32x32x16_bf16 v[112:127], a[36:39], v[52:55], v[112:127]\n" \
    "ds_read_b64_tr_b16 v[36:37], v15 offset:40960\n" \
    "ds_read_b64_tr_b16 v[38:39], v15 offset:41984\n" \
    "s_cmp_eq_u32 s54, 0\n" \
    "s_cbranch_scc1 QVW2%=\n" \
    "s_waitcnt vmcnt(24)\n" \
    "s_cmp_eq_u32 s55, 0\n" \
    "s_cbranch_scc1 QVX2%=\n" \
    "s_waitcnt vmcnt(24)\n" \
    "s_branch QVX2%=\n" \
    "QVW2%=:\n" \
    "s_waitcnt vmcnt(16)\n" \
    "s_waitcnt vmcnt(12)\n" \
    "s_cmp_eq_u32 s55, 0\n" \
    "s_cbranch_scc1 QVX2%=\n" \
    "s_waitcnt vmcnt(16)\n" \
    "s_waitcnt vmcnt(12)\n" \
    "QVX2%=:\n" \
    "s_barrier\n" \
    "s_waitcnt lgkmcnt(6)\n" \
    "s_mov_b64 s[42:43], s[44:45]\n" \
    "s_add_u32 m0, s51, 0\n" \
    "s_cmp_lg_u32 s55, 0\n" \
    "v_mfma_f32_32x32x16_bf16 v[64:79], a[40:43], v[24:27], v[64:79]\n" \
    "s_cbranch_scc1 QVG2_0%=\n" \
    "global_load_lds_dwordx4 v1, s[42:43]\n" \
    "QVG2_0%=:\n" \
    "ds_read_b64_tr_b16 v[40:41], v12 offset:45056\n" \
    "ds_read_b64_tr_b16 v[42:43], v12 offset:46080\n" \
    "s_waitcnt lgkmcnt(6)\n" \
    "s_add_u32 m0, s51, 1024\n" \
    "s_cmp_lg_u32 s55, 0\n" \
    "v_mfma_f32_32x32x16_bf16 v[80:95], a[40:43], v[28:31], v[80:95]\n" \
    "s_cbranch_scc1 QVG2_1%=\n" \
    "global_load_lds_dwordx4 v2, s[42:43]\n" \
    "QVG2_1%=:\n" \
    "ds_read_b64_tr_b16 v[44:45], v13 offset:45056\n" \
    "ds_read_b64_tr_b16 v[46:47], v13 offset:46080\n" \
    "s_waitcnt lgkmcnt(6)\n" \
    "s_add_u32 m0, s51, 2048\n" \
    "s_cmp_lg_u32 s55, 0\n" \
    "v_mfma_f32_32x32x16_bf16 v[96:111], a[40:43], v[32:35], v[96:111]\n" \
    "s_cbranch_scc1 QVG2_2%=\n" \
    "global_load_lds_dwordx4 v3, s[42:43]\n" \
    "QVG2_2%=:\n" \
    "ds_read_b64_tr_b16 v[48:49], v14 offset:45056\n" \
    "ds_read_b64_tr_b16 v[50:51], v14 offset:46080\n" \
    "s_waitcnt lgkmcnt(6)\n" \
    "s_add_u32 m0, s51, 3072\n" \
    "s_cmp_lg_u32 s55, 0\n" \
    "v_mfma_f32_32x32x16_bf16 v[112:127], a[40:43], v[36:39], v[112:127]\n" \
    "s_cbranch_scc1 QVG2_3%=\n" \
    "global_load_lds_dwordx4 v4, s[42:43]\n" \
    "s_add_u32 s42, s42, 524288\n" \
    "s_addc_u32 s43, s43, 0\n" \
    "QVG2_3%=:\n" \
    "ds_read_b64_tr_b16 v[52:53], v15 offset:45056\n" \
    "ds_read_b64_tr_b16 v[54:55], v15 offset:46080\n" \
    "s_waitcnt lgkmcnt(6)\n" \
    "v_mfma_f32_32x32x16_bf16 v[64:79], a[44:47], v[40:43], v[64:79]\n" \
    "ds_read_b64_tr_b16 v[24:25], v12 offset:49152\n" \
    "ds_read_b64_tr_b16 v[26:27], v12 offset:50176\n" \
    "s_waitcnt lgkmcnt(6)\n" \
    "v_mfma_f32_32x32x16_bf16 v[80:95], a[44:47], v[44:47], v[80:95]\n" \
    "ds_read_b64_tr_b16 v[28:29], v13 offset:49152\n" \
    "ds_read_b64_tr_b16 v[30:31], v13 offset:50176\n" \
    "s_waitcnt lgkmcnt(6)\n" \
    "v_mfma_f32_32x32x16_bf16 v[96:111], a[44:47], v[48:51], v[96:111]\n" \
    "ds_read_b64_tr_b16 v[32:33], v14 offset:49152\n" \
    "ds_read_b64_tr_b16 v[34:35], v14 offset:50176\n" \
    "s_waitcnt lgkmcnt(6)\n" \
    "v_mfma_f32_32x32x16_bf16 v[112:127], a[44:47], v[52:55], v[112:127]\n" \
    "ds_read_b64_tr_b16 v[36:37], v15 offset:49152\n" \
    "ds_read_b64_tr_b16 v[38:39], v15 offset:50176\n" \
    "s_cmp_lg_u32 s54, 0\n" \
    "s_cbranch_scc1 QVC3%=\n" \
    "global_load_dwordx4 a[80:83], v6, s[46:47] offset:0\n" \
    "global_load_dwordx4 a[84:87], v6, s[46:47] offset:1024\n" \
    "global_load_dwordx4 a[88:91], v6, s[46:47] offset:2048\n" \
    "global_load_dwordx4 a[92:95], v6, s[46:47] offset:3072\n" \
    "s_add_u32 m0, s51, 81920\n" \
    "s_nop 0\n" \
    "global_load_lds_dwordx4 v1, s[48:49]\n" \
    "s_add_u32 m0, s51, 82944\n" \
    "s_nop 0\n" \
    "global_load_lds_dwordx4 v2, s[48:49]\n" \
    "s_add_u32 m0, s51, 83968\n" \
    "s_nop 0\n" \
    "global_load_lds_dwordx4 v3, s[48:49]\n" \
    "s_add_u32 m0, s51, 84992\n" \
    "s_nop 0\n" \
    "global_load_lds_dwordx4 v4, s[48:49]\n" \
    "s_add_u32 s48, s48, 524288\n" \
    "s_addc_u32 s49, s49, 0\n" \
    "QVC3%=:\n" \
    "s_waitcnt lgkmcnt(6)\n" \
    "v_mfma_f32_32x32x16_bf16 v[64:79], a[48:51], v[24:27], v[64:79]\n" \
    "ds_read_b64_tr_b16 v[40:41], v12 offset:53248\n" \
    "ds_read_b64_tr_b16 v[42:43], v12 offset:54272\n" \
    "s_waitcnt lgkmcnt(6)\n" \
    "v_mfma_f32_32x32x16_bf16 v[80:95], a[48:51], v[28:31], v[80:95]\n" \
    "ds_read_b64_tr_b16 v[44:45], v13 offset:53248\n" \
    "ds_read_b64_tr_b16 v[46:47], v13 offset:54272\n" \
    "s_waitcnt lgkmcnt(6)\n" \
    "v_mfma_f32_32x32x16_bf16 v[96:111], a[48:51], v[32:35], v[96:111]\n" \
    "ds_read_b64_tr_b16 v[48:49], v14 offset:53248\n" \
    "ds_read_b64_tr_b16 v[50:51], v14 offset:54272\n" \
    "s_waitcnt lgkmcnt(6)\n" \
    "v_mfma_f32_32x32x16_bf16 v[112:127], a[48:51], v[36:39], v[112:127]\n" \
    "ds_read_b64_tr_b16 v[52:53], v15 offset:53248\n" \
    "ds_read_b64_tr_b16 v[54:55], v15 offset:54272\n" \
    "s_waitcnt lgkmcnt(6)\n" \
    "v_mfma_f32_32x32x16_bf16 v[64:79], a[52:55], v[40:43], v[64:79]\n" \
    "ds_read_b64_tr_b16 v[24:25], v12 offset:57344\n" \
    "ds_read_b64_tr_b16 v[26:27], v12 offset:58368\n" \
    "s_waitcnt lgkmcnt(6)\n" \
    "v_mfma_f32_32x32x16_bf16 v[80:95], a[52:55], v[44:47], v[80:95]\n" \
    "ds_read_b64_tr_b16 v[28:29], v13 offset:57344\n" \
    "ds_read_b64_tr_b16 v[30:31], v13 offset:58368\n" \
    "s_waitcnt lgkmcnt(6)\n" \
    "v_mfma_f32_32x32x16_bf16 v[96:111], a[52:55], v[48:51], v[96:111]\n" \
    "ds_read_b64_tr_b16 v[32:33], v14 offset:57344\n" \
    "ds_read_b64_tr_b16 v[34:35], v14 offset:58368\n" \
    "s_waitcnt lgkmcnt(6)\n" \
    "v_mfma_f32_32x32x16_bf16 v[112:127], a[52:55], v[52:55], v[112:127]\n" \
    "ds_read_b64_tr_b16 v[36:37], v15 offset:57344\n" \
    "ds_read_b64_tr_b16 v[38:39], v15 offset:58368\n" \
    "s_cmp_eq_u32 s54, 0\n" \
    "s_cbranch_scc1 QVW3%=\n" \
    "s_waitcnt vmcnt(24)\n" \
    "s_cmp_eq_u32 s55, 0\n" \
    "s_cbranch_scc1 QVX3%=\n" \
    "s_waitcnt vmcnt(20)\n" \
    "s_branch QVX3%=\n" \
    "QVW3%=:\n" \
    "s_waitcnt vmcnt(16)\n" \
    "s_waitcnt vmcnt(12)\n" \
    "s_cmp_eq_u32 s55, 0\n" \
    "s_cbranch_scc1 QVX3%=\n" \
    "s_waitcnt vmcnt(12)\n" \
    "s_waitcnt vmcnt(8)\n" \
    "QVX3%=:\n" \
    "s_barrier\n" \
    "s_waitcnt lgkmcnt(6)\n" \
    "s_add_u32 m0, s51, 16384\n" \
    "s_cmp_lg_u32 s55, 0\n" \
    "v_mfma_f32_32x32x16_bf16 v[64:79], a[56:59], v[24:27], v[64:79]\n" \
    "s_cbranch_scc1 QVG3_0%=\n" \
    "global_load_lds_dwordx4 v1, s[42:43]\n" \
    "QVG3_0%=:\n" \
    "ds_read_b64_tr_b16 v[40:41], v12 offset:61440\n" \
    "ds_read_b64_tr_b16 v[42:43], v12 offset:62464\n" \
    "s_waitcnt lgkmcnt(6)\n" \
    "s_add_u32 m0, s51, 17408\n" \
    "s_cmp_lg_u32 s55, 0\n" \
    "v_mfma_f32_32x32x16_bf16 v[80:95], a[56:59], v[28:31], v[80:95]\n" \
    "s_cbranch_scc1 QVG3_1%=\n" \
    "global_load_lds_dwordx4 v2, s[42:43]\n" \
    "QVG3_1%=:\n" \
    "ds_read_b64_tr_b16 v[44:45], v13 offset:61440\n" \
    "ds_read_b64_tr_b16 v[46:47], v13 offset:62464\n" \
    "s_waitcnt lgkmcnt(6)\n" \
    "s_add_u32 m0, s51, 18432\n" \
    "s_cmp_lg_u32 s55, 0\n" \
    "v_mfma_f32_32x32x16_bf16 v[96:111], a[56:59], v[32:35], v[96:111]\n" \
    "s_cbranch_scc1 QVG3_2%=\n" \
    "global_load_lds_dwordx4 v3, s[42:43]\n" \
    "QVG3_2%=:\n" \
    "ds_read_b64_tr_b16 v[48:49], v14 offset:61440\n" \
    "ds_read_b64_tr_b16 v[50:51], v14 offset:62464\n" \
    "s_waitcnt lgkmcnt(6)\n" \
    "s_add_u32 m0, s51, 19456\n" \
    "s_cmp_lg_u32 s55, 0\n" \
    "v_mfma_f32_32x32x16_bf16 v[112:127], a[56:59], v[36:39], v[112:127]\n" \
    "s_cbranch_scc1 QVG3_3%=\n" \
    "global_load_lds_dwordx4 v4, s[42:43]\n" \
    "s_add_u32 s42, s42, 524288\n" \
    "s_addc_u32 s43, s43, 0\n" \
    "QVG3_3%=:\n" \
    "ds_read_b64_tr_b16 v[52:53], v15 offset:61440\n" \
    "ds_read_b64_tr_b16 v[54:55], v15 offset:62464\n" \
    "s_waitcnt lgkmcnt(6)\n" \
    "v_mfma_f32_32x32x16_bf16 v[64:79], a[60:63], v[40:43], v[64:79]\n" \
    "ds_read_b64_tr_b16 v[24:25], v16 offset:0\n" \
    "ds_read_b64_tr_b16 v[26:27], v16 offset:1024\n" \
    "s_waitcnt lgkmcnt(6)\n" \
    "v_mfma_f32_32x32x16_bf16 v[80:95], a[60:63], v[44:47], v[80:95]\n" \
    "ds_read_b64_tr_b16 v[28:29], v17 offset:0\n" \
    "ds_read_b64_tr_b16 v[30:31], v17 offset:1024\n" \
    "s_waitcnt lgkmcnt(6)\n" \
    "v_mfma_f32_32x32x16_bf16 v[96:111], a[60:63], v[48:51], v[96:111]\n" \
    "ds_read_b64_tr_b16 v[32:33], v18 offset:0\n" \
    "ds_read_b64_tr_b16 v[34:35], v18 offset:1024\n" \
    "s_waitcnt lgkmcnt(6)\n" \
    "v_mfma_f32_32x32x16_bf16 v[112:127], a[60:63], v[52:55], v[112:127]\n" \
    "ds_read_b64_tr_b16 v[36:37], v19 offset:0\n" \
    "ds_read_b64_tr_b16 v[38:39], v19 offset:1024\n" \
    "s_cmp_lg_u32 s54, 0\n" \
    "s_cbranch_scc1 QVC4%=\n" \
    "s_add_u32 s46, s46, 8192\n" \
    "s_addc_u32 s47, s47, 0\n" \
    "global_load_dwordx4 a[96:99], v6, s[46:47] offset:-4096\n" \
    "global_load_dwordx4 a[100:103], v6, s[46:47] offset:-3072\n" \
    "global_load_dwordx4 a[104:107], v6, s[46:47] offset:-2048\n" \
    "global_load_dwordx4 a[108:111], v6, s[46:47] offset:-1024\n" \
    "s_add_u32 m0, s51, 98304\n" \
    "s_nop 0\n" \
    "global_load_lds_dwordx4 v1, s[48:49]\n" \
    "s_add_u32 m0, s51, 99328\n" \
    "s_nop 0\n" \
    "global_load_lds_dwordx4 v2, s[48:49]\n" \
    "s_add_u32 m0, s51, 100352\n" \
    "s_nop 0\n" \
    "global_load_lds_dwordx4 v3, s[48:49]\n" \
    "s_add_u32 m0, s51, 101376\n" \
    "s_nop 0\n" \
    "global_load_lds_dwordx4 v4, s[48:49]\n" \
    "s_add_u32 s48, s48, 524288\n" \
    "s_addc_u32 s49, s49, 0\n" \
    "QVC4%=:\n" \
    "s_waitcnt lgkmcnt(6)\n" \
    "v_mfma_f32_32x32x16_bf16 v[64:79], a[64:67], v[24:27], v[64:79]\n" \
    "ds_read_b64_tr_b16 v[40:41], v16 offset:4096\n" \
    "ds_read_b64_tr_b16 v[42:43], v16 offset:5120\n" \
    "s_waitcnt lgkmcnt(6)\n" \
    "v_mfma_f32_32x32x16_bf16 v[80:95], a[64:67], v[28:31], v[80:95]\n" \
    "ds_read_b64_tr_b16 v[44:45], v17 offset:4096\n" \
    "ds_read_b64_tr_b16 v[46:47], v17 offset:5120\n" \
    "s_waitcnt lgkmcnt(6)\n" \
    "v_mfma_f32_32x32x16_bf16 v[96:111], a[64:67], v[32:35], v[96:111]\n" \
    "ds_read_b64_tr_b16 v[48:49], v18 offset:4096\n" \
    "ds_read_b64_tr_b16 v[50:51], v18 offset:5120\n" \
    "s_waitcnt lgkmcnt(6)\n" \
    "v_mfma_f32_32x32x16_bf16 v[112:127], a[64:67], v[36:39], v[112:127]\n" \
    "ds_read_b64_tr_b16 v[52:53], v19 offset:4096\n" \
    "ds_read_b64_tr_b16 v[54:55], v19 offset:5120\n" \
    "s_cmp_eq_u32 s54, 0\n" \
    "s_cbranch_scc1 QVF%=\n" \
    "s_waitcnt vmcnt(8)\n" \
    "v_mov_b32 v10, 0\n" \
    "v_mov_b32 v11, %7\n" \
    "s_mov_b64 exec, 1\n" \
    "global_store_dword v10, v11, s[48:49] sc1\n" \
    "s_mov_b64 exec, -1\n" \
    "QVF%=:\n" \
    "s_waitcnt lgkmcnt(6)\n" \
    "v_mfma_f32_32x32x16_bf16 v[64:79], a[68:71], v[40:43], v[64:79]\n" \
    "ds_read_b64_tr_b16 v[24:25], v16 offset:8192\n" \
    "ds_read_b64_tr_b16 v[26:27], v16 offset:9216\n" \
    "s_waitcnt lgkmcnt(6)\n" \
    "v_mfma_f32_32x32x16_bf16 v[80:95], a[68:71], v[44:47], v[80:95]\n" \
    "ds_read_b64_tr_b16 v[28:29], v17 offset:8192\n" \
    "ds_read_b64_tr_b16 v[30:31], v17 offset:9216\n" \
    "s_waitcnt lgkmcnt(6)\n" \
    "v_mfma_f32_32x32x16_bf16 v[96:111], a[68:71], v[48:51], v[96:111]\n" \
    "ds_read_b64_tr_b16 v[32:33], v18 offset:8192\n" \
    "ds_read_b64_tr_b16 v[34:35], v18 offset:9216\n" \
    "s_waitcnt lgkmcnt(6)\n" \
    "v_mfma_f32_32x32x16_bf16 v[112:127], a[68:71], v[52:55], v[112:127]\n" \
    "ds_read_b64_tr_b16 v[36:37], v19 offset:8192\n" \
    "ds_read_b64_tr_b16 v[38:39], v19 offset:9216\n" \
    "s_cmp_eq_u32 s54, 0\n" \
    "s_cbranch_scc1 QVW4%=\n" \
    "s_waitcnt vmcnt(24)\n" \
    "s_cmp_eq_u32 s55, 0\n" \
    "s_cbranch_scc1 QVX4%=\n" \
    "s_waitcnt vmcnt(16)\n" \
    "s_branch QVX4%=\n" \
    "QVW4%=:\n" \
    "s_waitcnt vmcnt(16)\n" \
    "s_waitcnt vmcnt(12)\n" \
    "s_cmp_eq_u32 s55, 0\n" \
    "s_cbranch_scc1 QVX4%=\n" \
    "s_waitcnt vmcnt(12)\n" \
    "s_waitcnt vmcnt(8)\n" \
    "QVX4%=:\n" \
    "s_barrier\n" \
    "s_waitcnt lgkmcnt(6)\n" \
    "s_add_u32 m0, s51, 32768\n" \
    "s_cmp_lg_u32 s55, 0\n" \
    "v_mfma_f32_32x32x16_bf16 v[64:79], a[72:75], v[24:27], v[64:79]\n" \
    "s_cbranch_scc1 QVG4_0%=\n" \
    "global_load_lds_dwordx4 v1, s[42:43]\n" \
    "QVG4_0%=:\n" \
    "ds_read_b64_tr_b16 v[40:41], v16 offset:12288\n" \
    "ds_read_b64_tr_b16 v[42:43], v16 offset:13312\n" \
    "s_waitcnt lgkmcnt(6)\n" \
    "s_add_u32 m0, s51, 33792\n" \
    "s_cmp_lg_u32 s55, 0\n" \
    "v_mfma_f32_32x32x16_bf16 v[80:95], a[72:75], v[28:31], v[80:95]\n" \
    "s_cbranch_scc1 QVG4_1%=\n" \
    "global_load_lds_dwordx4 v2, s[42:43]\n" \
    "QVG4_1%=:\n" \
    "ds_read_b64_tr_b16 v[44:45], v17 offset:12288\n" \
    "ds_read_b64_tr_b16 v[46:47], v17 offset:13312\n" \
    "s_waitcnt lgkmcnt(6)\n" \
    "s_add_u32 m0, s51, 34816\n" \
    "s_cmp_lg_u32 s55, 0\n" \
    "v_mfma_f32_32x32x16_bf16 v[96:111], a[72:75], v[32:35], v[96:111]\n" \
    "s_cbranch_scc1 QVG4_2%=\n" \
    "global_load_lds_dwordx4 v3, s[42:43]\n" \
    "QVG4_2%=:\n" \
    "ds_read_b64_tr_b16 v[48:49], v18 offset:12288\n" \
    "ds_read_b64_tr_b16 v[50:51], v18 offset:13312\n" \
    "s_waitcnt lgkmcnt(6)\n" \
    "s_add_u32 m0, s51, 35840\n" \
    "s_cmp_lg_u32 s55, 0\n" \
    "v_mfma_f32_32x32x16_bf16 v[112:127], a[72:75], v[36:39], v[112:127]\n" \
    "s_cbranch_scc1 QVG4_3%=\n" \
    "global_load_lds_dwordx4 v4, s[42:43]\n" \
    "s_add_u32 s42, s42, 524288\n" \
    "s_addc_u32 s43, s43, 0\n" \
    "QVG4_3%=:\n" \
    "ds_read_b64_tr_b16 v[52:53], v19 offset:12288\n" \
    "ds_read_b64_tr_b16 v[54:55], v19 offset:13312\n" \
    "s_waitcnt lgkmcnt(6)\n" \
    "v_mfma_f32_32x32x16_bf16 v[64:79], a[76:79], v[40:43], v[64:79]\n" \
    "ds_read_b64_tr_b16 v[24:25], v16 offset:16384\n" \
    "ds_read_b64_tr_b16 v[26:27], v16 offset:17408\n" \
    "s_waitcnt lgkmcnt(6)\n" \
    "v_mfma_f32_32x32x16_bf16 v[80:95], a[76:79], v[44:47], v[80:95]\n" \
    "ds_read_b64_tr_b16 v[28:29], v17 offset:16384\n" \
    "ds_read_b64_tr_b16 v[30:31], v17 offset:17408\n" \
    "s_waitcnt lgkmcnt(6)\n" \
    "v_mfma_f32_32x32x16_bf16 v[96:111], a[76:79], v[48:51], v[96:111]\n" \
    "ds_read_b64_tr_b16 v[32:33], v18 offset:16384\n" \
    "ds_read_b64_tr_b16 v[34:35], v18 offset:17408\n" \
    "s_waitcnt lgkmcnt(6)\n" \
    "v_mfma_f32_32x32x16_bf16 v[112:127], a[76:79], v[52:55], v[112:127]\n" \
    "ds_read_b64_tr_b16 v[36:37], v19 offset:16384\n" \
    "ds_read_b64_tr_b16 v[38:39], v19 offset:17408\n" \
    "s_cmp_lg_u32 s54, 0\n" \
    "s_cbranch_scc1 QVC5%=\n" \
    "global_load_dwordx4 a[112:115], v6, s[46:47] offset:0\n" \
    "global_load_dwordx4 a[116:119], v6, s[46:47] offset:1024\n" \
    "global_load_dwordx4 a[120:123], v6, s[46:47] offset:2048\n" \
    "global_load_dwordx4 a[124:127], v6, s[46:47] offset:3072\n" \
    "s_add_u32 m0, s51, 114688\n" \
    "s_nop 0\n" \
    "global_load_lds_dwordx4 v1, s[48:49]\n" \
    "s_add_u32 m0, s51, 115712\n" \
    "s_nop 0\n" \
    "global_load_lds_dwordx4 v2, s[48:49]\n" \
    "s_add_u32 m0, s51, 116736\n" \
    "s_nop 0\n" \
    "global_load_lds_dwordx4 v3, s[48:49]\n" \
    "s_add_u32 m0, s51, 117760\n" \
    "s_nop 0\n" \
    "global_load_lds_dwordx4 v4, s[48:49]\n" \
    "s_add_u32 s48, s48, 524288\n" \
    "s_addc_u32 s49, s49, 0\n" \
    "QVC5%=:\n" \
    "s_waitcnt lgkmcnt(6)\n" \
    "v_mfma_f32_32x32x16_bf16 v[64:79], a[80:83], v[24:27], v[64:79]\n" \
    "ds_read_b64_tr_b16 v[40:41], v16 offset:20480\n" \
    "ds_read_b64_tr_b16 v[42:43], v16 offset:21504\n" \
    "s_waitcnt lgkmcnt(6)\n" \
    "v_mfma_f32_32x32x16_bf16 v[80:95], a[80:83], v[28:31], v[80:95]\n" \
    "ds_read_b64_tr_b16 v[44:45], v17 offset:20480\n" \
    "ds_read_b64_tr_b16 v[46:47], v17 offset:21504\n" \
    "s_waitcnt lgkmcnt(6)\n" \
    "v_mfma_f32_32x32x16_bf16 v[96:111], a[80:83], v[32:35], v[96:111]\n" \
    "ds_read_b64_tr_b16 v[48:49], v18 offset:20480\n" \
    "ds_read_b64_tr_b16 v[50:51], v18 offset:21504\n" \
    "s_waitcnt lgkmcnt(6)\n" \
    "v_mfma_f32_32x32x16_bf16 v[112:127], a[80:83], v[36:39], v[112:127]\n" \
    "ds_read_b64_tr_b16 v[52:53], v19 offset:20480\n" \
    "ds_read_b64_tr_b16 v[54:55], v19 offset:21504\n" \
    "s_waitcnt lgkmcnt(6)\n" \
    "v_mfma_f32_32x32x16_bf16 v[64:79], a[84:87], v[40:43], v[64:79]\n" \
    "ds_read_b64_tr_b16 v[24:25], v16 offset:24576\n" \
    "ds_read_b64_tr_b16 v[26:27], v16 offset:25600\n" \
    "s_waitcnt lgkmcnt(6)\n" \
    "v_mfma_f32_32x32x16_bf16 v[80:95], a[84:87], v[44:47], v[80:95]\n" \
    "ds_read_b64_tr_b16 v[28:29], v17 offset:24576\n" \
    "ds_read_b64_tr_b16 v[30:31], v17 offset:25600\n" \
    "s_waitcnt lgkmcnt(6)\n" \
    "v_mfma_f32_32x32x16_bf16 v[96:111], a[84:87], v[48:51], v[96:111]\n" \
    "ds_read_b64_tr_b16 v[32:33], v18 offset:24576\n" \
    "ds_read_b64_tr_b16 v[34:35], v18 offset:25600\n" \
    "s_waitcnt lgkmcnt(6)\n" \
    "v_mfma_f32_32x32x16_bf16 v[112:127], a[84:87], v[52:55], v[112:127]\n" \
    "ds_read_b64_tr_b16 v[36:37], v19 offset:24576\n" \
    "ds_read_b64_tr_b16 v[38:39], v19 offset:25600\n" \
    "s_cmp_eq_u32 s54, 0\n" \
    "s_cbranch_scc1 QVW5%=\n" \
    "s_waitcnt vmcnt(24)\n" \
    "s_cmp_eq_u32 s55, 0\n" \
    "s_cbranch_scc1 QVX5%=\n" \
    "s_waitcnt vmcnt(12)\n" \
    "s_branch QVX5%=\n" \
    "QVW5%=:\n" \
    "s_waitcnt vmcnt(16)\n" \
    "s_waitcnt vmcnt(12)\n" \
    "s_cmp_eq_u32 s55, 0\n" \
    "s_cbranch_scc1 QVX5%=\n" \
    "s_waitcnt vmcnt(12)\n" \
    "s_waitcnt vmcnt(8)\n" \
    "QVX5%=:\n" \
    "s_barrier\n" \
    "s_waitcnt lgkmcnt(6)\n" \
    "s_add_u32 m0, s51, 49152\n" \
    "s_cmp_lg_u32 s55, 0\n" \
    "v_mfma_f32_32x32x16_bf16 v[64:79], a[88:91], v[24:27], v[64:79]\n" \
    "s_cbranch_scc1 QVG5_0%=\n" \
    "global_load_lds_dwordx4 v1, s[42:43]\n" \
    "QVG5_0%=:\n" \
    "ds_read_b64_tr_b16 v[40:41], v16 offset:28672\n" \
    "ds_read_b64_tr_b16 v[42:43], v16 offset:29696\n" \
    "s_waitcnt lgkmcnt(6)\n" \
    "s_add_u32 m0, s51, 50176\n" \
    "s_cmp_lg_u32 s55, 0\n" \
    "v_mfma_f32_32x32x16_bf16 v[80:95], a[88:91], v[28:31], v[80:95]\n" \
    "s_cbranch_scc1 QVG5_1%=\n" \
    "global_load_lds_dwordx4 v2, s[42:43]\n" \
    "QVG5_1%=:\n" \
    "ds_read_b64_tr_b16 v[44:45], v17 offset:28672\n" \
    "ds_read_b64_tr_b16 v[46:47], v17 offset:29696\n" \
    "s_waitcnt lgkmcnt(6)\n" \
    "s_add_u32 m0, s51, 51200\n" \
    "s_cmp_lg_u32 s55, 0\n" \
    "v_mfma_f32_32x32x16_bf16 v[96:111], a[88:91], v[32:35], v[96:111]\n" \
    "s_cbranch_scc1 QVG5_2%=\n" \
    "global_load_lds_dwordx4 v3, s[42:43]\n" \
    "QVG5_2%=:\n" \
    "ds_read_b64_tr_b16 v[48:49], v18 offset:28672\n" \
    "ds_read_b64_tr_b16 v[50:51], v18 offset:29696\n" \
    "s_waitcnt lgkmcnt(6)\n" \
    "s_add_u32 m0, s51, 52224\n" \
    "s_cmp_lg_u32 s55, 0\n" \
    "v_mfma_f32_32x32x16_bf16 v[112:127], a[88:91], v[36:39], v[112:127]\n" \
    "s_cbranch_scc1 QVG5_3%=\n" \
    "global_load_lds_dwordx4 v4, s[42:43]\n" \
    "s_add_u32 s42, s42, 524288\n" \
    "s_addc_u32 s43, s43, 0\n" \
    "QVG5_3%=:\n" \
    "ds_read_b64_tr_b16 v[52:53], v19 offset:28672\n" \
    "ds_read_b64_tr_b16 v[54:55], v19 offset:29696\n" \
    "s_waitcnt lgkmcnt(6)\n" \
    "v_mfma_f32_32x32x16_bf16 v[64:79], a[92:95], v[40:43], v[64:79]\n" \
    "ds_read_b64_tr_b16 v[24:25], v16 offset:32768\n" \
    "ds_read_b64_tr_b16 v[26:27], v16 offset:33792\n" \
    "s_waitcnt lgkmcnt(6)\n" \
    "v_mfma_f32_32x32x16_bf16 v[80:95], a[92:95], v[44:47], v[80:95]\n" \
    "ds_read_b64_tr_b16 v[28:29], v17 offset:32768\n" \
    "ds_read_b64_tr_b16 v[30:31], v17 offset:33792\n" \
    "s_waitcnt lgkmcnt(6)\n" \
    "v_mfma_f32_32x32x16_bf16 v[96:111], a[92:95], v[48:51], v[96:111]\n" \
    "ds_read_b64_tr_b16 v[32:33], v18 offset:32768\n" \
    "ds_read_b64_tr_b16 v[34:35], v18 offset:33792\n" \
    "s_waitcnt lgkmcnt(6)\n" \
    "v_mfma_f32_32x32x16_bf16 v[112:127], a[92:95], v[52:55], v[112:127]\n" \
    "ds_read_b64_tr_b16 v[36:37], v19 offset:32768\n" \
    "ds_read_b64_tr_b16 v[38:39], v19 offset:33792\n" \
    "s_cmp_lg_u32 s54, 0\n" \
    "s_cbranch_scc1 QVC6%=\n" \
    "s_add_u32 s46, s46, 8192\n" \
    "s_addc_u32 s47, s47, 0\n" \
    "global_load_dwordx4 a[128:131], v6, s[46:47] offset:-4096\n" \
    "global_load_dwordx4 a[132:135], v6, s[46:47] offset:-3072\n" \
    "global_load_dwordx4 a[136:139], v6, s[46:47] offset:-2048\n" \
    "global_load_dwordx4 a[140:143], v6, s[46:47] offset:-1024\n" \
    "QVC6%=:\n" \
    "s_waitcnt lgkmcnt(6)\n" \
    "v_mfma_f32_32x32x16_bf16 v[64:79], a[96:99], v[24:27], v[64:79]\n" \
    "ds_read_b64_tr_b16 v[40:41], v16 offset:36864\n" \
    "ds_read_b64_tr_b16 v[42:43], v16 offset:37888\n" \
    "s_waitcnt lgkmcnt(6)\n" \
    "v_mfma_f32_32x32x16_bf16 v[80:95], a[96:99], v[28:31], v[80:95]\n" \
    "ds_read_b64_tr_b16 v[44:45], v17 offset:36864\n" \
    "ds_read_b64_tr_b16 v[46:47], v17 offset:37888\n" \
    "s_waitcnt lgkmcnt(6)\n" \
    "v_mfma_f32_32x32x16_bf16 v[96:111], a[96:99], v[32:35], v[96:111]\n" \
    "ds_read_b64_tr_b16 v[48:49], v18 offset:36864\n" \
    "ds_read_b64_tr_b16 v[50:51], v18 offset:37888\n" \
    "s_waitcnt lgkmcnt(6)\n" \
    "v_mfma_f32_32x32x16_bf16 v[112:127], a[96:99], v[36:39], v[112:127]\n" \
    "ds_read_b64_tr_b16 v[52:53], v19 offset:36864\n" \
    "ds_read_b64_tr_b16 v[54:55], v19 offset:37888\n" \
    "s_waitcnt lgkmcnt(6)\n" \
    "v_mfma_f32_32x32x16_bf16 v[64:79], a[100:103], v[40:43], v[64:79]\n" \
    "ds_read_b64_tr_b16 v[24:25], v16 offset:40960\n" \
    "ds_read_b64_tr_b16 v[26:27], v16 offset:41984\n" \
    "s_waitcnt lgkmcnt(6)\n" \
    "v_mfma_f32_32x32x16_bf16 v[80:95], a[100:103], v[44:47], v[80:95]\n" \
    "ds_read_b64_tr_b16 v[28:29], v17 offset:40960\n" \
    "ds_read_b64_tr_b16 v[30:31], v17 offset:41984\n" \
    "s_waitcnt lgkmcnt(6)\n" \
    "v_mfma_f32_32x32x16_bf16 v[96:111], a[100:103], v[48:51], v[96:111]\n" \
    "ds_read_b64_tr_b16 v[32:33], v18 offset:40960\n" \
    "ds_read_b64_tr_b16 v[34:35], v18 offset:41984\n" \
    "s_waitcnt lgkmcnt(6)\n" \
    "v_mfma_f32_32x32x16_bf16 v[112:127], a[100:103], v[52:55], v[112:127]\n" \
    "ds_read_b64_tr_b16 v[36:37], v19 offset:40960\n" \
    "ds_read_b64_tr_b16 v[38:39], v19 offset:41984\n" \
    "s_cmp_eq_u32 s54, 0\n" \
    "s_cbranch_scc1 QVW6%=\n" \
    "s_waitcnt vmcnt(24)\n" \
    "s_cmp_eq_u32 s55, 0\n" \
    "s_cbranch_scc1 QVX6%=\n" \
    "s_waitcnt vmcnt(8)\n" \
    "s_branch QVX6%=\n" \
    "QVW6%=:\n" \
    "s_waitcnt vmcnt(12)\n" \
    "s_waitcnt vmcnt(8)\n" \
    "s_cmp_eq_u32 s55, 0\n" \
    "s_cbranch_scc1 QVX6%=\n" \
    "s_waitcnt vmcnt(8)\n" \
    "s_waitcnt vmcnt(4)\n" \
    "QVX6%=:\n" \
    "s_barrier\n" \
    "s_waitcnt lgkmcnt(6)\n" \
    "s_add_u32 m0, s51, 65536\n" \
    "s_cmp_lg_u32 s55, 0\n" \
    "v_mfma_f32_32x32x16_bf16 v[64:79], a[104:107], v[24:27], v[64:79]\n" \
    "s_cbranch_scc1 QVG6_0%=\n" \
    "global_load_lds_dwordx4 v1, s[42:43]\n" \
    "QVG6_0%=:\n" \
    "ds_read_b64_tr_b16 v[40:41], v16 offset:45056\n" \
    "ds_read_b64_tr_b16 v[42:43], v16 offset:46080\n" \
    "s_waitcnt lgkmcnt(6)\n" \
    "s_add_u32 m0, s51, 66560\n" \
    "s_cmp_lg_u32 s55, 0\n" \
    "v_mfma_f32_32x32x16_bf16 v[80:95], a[104:107], v[28:31], v[80:95]\n" \
    "s_cbranch_scc1 QVG6_1%=\n" \
    "global_load_lds_dwordx4 v2, s[42:43]\n" \
    "QVG6_1%=:\n" \
    "ds_read_b64_tr_b16 v[44:45], v17 offset:45056\n" \
    "ds_read_b64_tr_b16 v[46:47], v17 offset:46080\n" \
    "s_waitcnt lgkmcnt(6)\n" \
    "s_add_u32 m0, s51, 67584\n" \
    "s_cmp_lg_u32 s55, 0\n" \
    "v_mfma_f32_32x32x16_bf16 v[96:111], a[104:107], v[32:35], v[96:111]\n" \
    "s_cbranch_scc1 QVG6_2%=\n" \
    "global_load_lds_dwordx4 v3, s[42:43]\n" \
    "QVG6_2%=:\n" \
    "ds_read_b64_tr_b16 v[48:49], v18 offset:45056\n" \
    "ds_read_b64_tr_b16 v[50:51], v18 offset:46080\n" \
    "s_waitcnt lgkmcnt(6)\n" \
    "s_add_u32 m0, s51, 68608\n" \
    "s_cmp_lg_u32 s55, 0\n" \
    "v_mfma_f32_32x32x16_bf16 v[112:127], a[104:107], v[36:39], v[112:127]\n" \
    "s_cbranch_scc1 QVG6_3%=\n" \
    "global_load_lds_dwordx4 v4, s[42:43]\n" \
    "s_add_u32 s42, s42, 524288\n" \
    "s_addc_u32 s43, s43, 0\n" \
    "QVG6_3%=:\n" \
    "ds_read_b64_tr_b16 v[52:53], v19 offset:45056\n" \
    "ds_read_b64_tr_b16 v[54:55], v19 offset:46080\n" \
    "s_waitcnt lgkmcnt(6)\n" \
    "v_mfma_f32_32x32x16_bf16 v[64:79], a[108:111], v[40:43], v[64:79]\n" \
    "ds_read_b64_tr_b16 v[24:25], v16 offset:49152\n" \
    "ds_read_b64_tr_b16 v[26:27], v16 offset:50176\n" \
    "s_waitcnt lgkmcnt(6)\n" \
    "v_mfma_f32_32x32x16_bf16 v[80:95], a[108:111], v[44:47], v[80:95]\n" \
    "ds_read_b64_tr_b16 v[28:29], v17 offset:49152\n" \
    "ds_read_b64_tr_b16 v[30:31], v17 offset:50176\n" \
    "s_waitcnt lgkmcnt(6)\n" \
    "v_mfma_f32_32x32x16_bf16 v[96:111], a[108:111], v[48:51], v[96:111]\n" \
    "ds_read_b64_tr_b16 v[32:33], v18 offset:49152\n" \
    "ds_read_b64_tr_b16 v[34:35], v18 offset:50176\n" \
    "s_waitcnt lgkmcnt(6)\n" \
    "v_mfma_f32_32x32x16_bf16 v[112:127], a[108:111], v[52:55], v[112:127]\n" \
    "ds_read_b64_tr_b16 v[36:37], v19 offset:49152\n" \
    "ds_read_b64_tr_b16 v[38:39], v19 offset:50176\n" \
    "s_cmp_lg_u32 s54, 0\n" \
    "s_cbranch_scc1 QVC7%=\n" \
    "global_load_dwordx4 a[144:147], v6, s[46:47] offset:0\n" \
    "global_load_dwordx4 a[148:151], v6, s[46:47] offset:1024\n" \
    "global_load_dwordx4 a[152:155], v6, s[46:47] offset:2048\n" \
    "global_load_dwordx4 a[156:159], v6, s[46:47] offset:3072\n" \
    "QVC7%=:\n" \
    "s_waitcnt lgkmcnt(6)\n" \
    "v_mfma_f32_32x32x16_bf16 v[64:79], a[112:115], v[24:27], v[64:79]\n" \
    "ds_read_b64_tr_b16 v[40:41], v16 offset:53248\n" \
    "ds_read_b64_tr_b16 v[42:43], v16 offset:54272\n" \
    "s_waitcnt lgkmcnt(6)\n" \
    "v_mfma_f32_32x32x16_bf16 v[80:95], a[112:115], v[28:31], v[80:95]\n" \
    "ds_read_b64_tr_b16 v[44:45], v17 offset:53248\n" \
    "ds_read_b64_tr_b16 v[46:47], v17 offset:54272\n" \
    "s_waitcnt lgkmcnt(6)\n" \
    "v_mfma_f32_32x32x16_bf16 v[96:111], a[112:115], v[32:35], v[96:111]\n" \
    "ds_read_b64_tr_b16 v[48:49], v18 offset:53248\n" \
    "ds_read_b64_tr_b16 v[50:51], v18 offset:54272\n" \
    "s_waitcnt lgkmcnt(6)\n" \
    "v_mfma_f32_32x32x16_bf16 v[112:127], a[112:115], v[36:39], v[112:127]\n" \
    "ds_read_b64_tr_b16 v[52:53], v19 offset:53248\n" \
    "ds_read_b64_tr_b16 v[54:55], v19 offset:54272\n" \
    "s_waitcnt lgkmcnt(6)\n" \
    "v_mfma_f32_32x32x16_bf16 v[64:79], a[116:119], v[40:43], v[64:79]\n" \
    "ds_read_b64_tr_b16 v[24:25], v16 offset:57344\n" \
    "ds_read_b64_tr_b16 v[26:27], v16 offset:58368\n" \
    "s_waitcnt lgkmcnt(6)\n" \
    "v_mfma_f32_32x32x16_bf16 v[80:95], a[116:119], v[44:47], v[80:95]\n" \
    "ds_read_b64_tr_b16 v[28:29], v17 offset:57344\n" \
    "ds_read_b64_tr_b16 v[30:31], v17 offset:58368\n" \
    "s_waitcnt lgkmcnt(6)\n" \
    "v_mfma_f32_32x32x16_bf16 v[96:111], a[116:119], v[48:51], v[96:111]\n" \
    "ds_read_b64_tr_b16 v[32:33], v18 offset:57344\n" \
    "ds_read_b64_tr_b16 v[34:35], v18 offset:58368\n" \
    "s_waitcnt lgkmcnt(6)\n" \
    "v_mfma_f32_32x32x16_bf16 v[112:127], a[116:119], v[52:55], v[112:127]\n" \
    "ds_read_b64_tr_b16 v[36:37], v19 offset:57344\n" \
    "ds_read_b64_tr_b16 v[38:39], v19 offset:58368\n" \
    "s_cmp_eq_u32 s54, 0\n" \
    "s_cbranch_scc1 QVW7%=\n" \
    "s_waitcnt vmcnt(24)\n" \
    "s_cmp_eq_u32 s55, 0\n" \
    "s_cbranch_scc1 QVX7%=\n" \
    "s_waitcnt vmcnt(4)\n" \
    "s_branch QVX7%=\n" \
    "QVW7%=:\n" \
    "s_waitcnt vmcnt(8)\n" \
    "s_cmp_eq_u32 s55, 0\n" \
    "s_cbranch_scc1 QVX7%=\n" \
    "s_waitcnt vmcnt(4)\n" \
    "QVX7%=:\n" \
    "s_barrier\n" \
    "s_waitcnt lgkmcnt(6)\n" \
    "s_add_u32 m0, s51, 81920\n" \
    "s_cmp_lg_u32 s55, 0\n" \
    "v_mfma_f32_32x32x16_bf16 v[64:79], a[120:123], v[24:27], v[64:79]\n" \
    "s_cbranch_scc1 QVG7_0%=\n" \
    "global_load_lds_dwordx4 v1, s[42:43]\n" \
    "QVG7_0%=:\n" \
    "ds_read_b64_tr_b16 v[40:41], v16 offset:61440\n" \
    "ds_read_b64_tr_b16 v[42:43], v16 offset:62464\n" \
    "s_waitcnt lgkmcnt(6)\n" \
    "s_add_u32 m0, s51, 82944\n" \
    "s_cmp_lg_u32 s55, 0\n" \
    "v_mfma_f32_32x32x16_bf16 v[80:95], a[120:123], v[28:31], v[80:95]\n" \
    "s_cbranch_scc1 QVG7_1%=\n" \
    "global_load_lds_dwordx4 v2, s[42:43]\n" \
    "QVG7_1%=:\n" \
    "ds_read_b64_tr_b16 v[44:45], v17 offset:61440\n" \
    "ds_read_b64_tr_b16 v[46:47], v17 offset:62464\n" \
    "s_waitcnt lgkmcnt(6)\n" \
    "s_add_u32 m0, s51, 83968\n" \
    "s_cmp_lg_u32 s55, 0\n" \
    "v_mfma_f32_32x32x16_bf16 v[96:111], a[120:123], v[32:35], v[96:111]\n" \
    "s_cbranch_scc1 QVG7_2%=\n" \
    "global_load_lds_dwordx4 v3, s[42:43]\n" \
    "QVG7_2%=:\n" \
    "ds_read_b64_tr_b16 v[48:49], v18 offset:61440\n" \
    "ds_read_b64_tr_b16 v[50:51], v18 offset:62464\n" \
    "s_waitcnt lgkmcnt(6)\n" \
    "s_add_u32 m0, s51, 84992\n" \
    "s_cmp_lg_u32 s55, 0\n" \
    "v_mfma_f32_32x32x16_bf16 v[112:127], a[120:123], v[36:39], v[112:127]\n" \
    "s_cbranch_scc1 QVG7_3%=\n" \
    "global_load_lds_dwordx4 v4, s[42:43]\n" \
    "s_add_u32 s42, s42, 524288\n" \
    "s_addc_u32 s43, s43, 0\n" \
    "QVG7_3%=:\n" \
    "ds_read_b64_tr_b16 v[52:53], v19 offset:61440\n" \
    "ds_read_b64_tr_b16 v[54:55], v19 offset:62464\n" \
    "s_waitcnt lgkmcnt(6)\n" \
    "v_mfma_f32_32x32x16_bf16 v[64:79], a[124:127], v[40:43], v[64:79]\n" \
    "ds_read_b64_tr_b16 v[24:25], v20 offset:0\n" \
    "ds_read_b64_tr_b16 v[26:27], v20 offset:1024\n" \
    "s_waitcnt lgkmcnt(6)\n" \
    "v_mfma_f32_32x32x16_bf16 v[80:95], a[124:127], v[44:47], v[80:95]\n" \
    "ds_read_b64_tr_b16 v[28:29], v21 offset:0\n" \
    "ds_read_b64_tr_b16 v[30:31], v21 offset:1024\n" \
    "s_waitcnt lgkmcnt(6)\n" \
    "v_mfma_f32_32x32x16_bf16 v[96:111], a[124:127], v[48:51], v[96:111]\n" \
    "ds_read_b64_tr_b16 v[32:33], v22 offset:0\n" \
    "ds_read_b64_tr_b16 v[34:35], v22 offset:1024\n" \
    "s_waitcnt lgkmcnt(6)\n" \
    "v_mfma_f32_32x32x16_bf16 v[112:127], a[124:127], v[52:55], v[112:127]\n" \
    "ds_read_b64_tr_b16 v[36:37], v23 offset:0\n" \
    "ds_read_b64_tr_b16 v[38:39], v23 offset:1024\n" \
    "s_waitcnt lgkmcnt(6)\n" \
    "v_mfma_f32_32x32x16_bf16 v[64:79], a[128:131], v[24:27], v[64:79]\n" \
    "ds_read_b64_tr_b16 v[40:41], v20 offset:4096\n" \
    "ds_read_b64_tr_b16 v[42:43], v20 offset:5120\n" \
    "s_waitcnt lgkmcnt(6)\n" \
    "v_mfma_f32_32x32x16_bf16 v[80:95], a[128:131], v[28:31], v[80:95]\n" \
    "ds_read_b64_tr_b16 v[44:45], v21 offset:4096\n" \
    "ds_read_b64_tr_b16 v[46:47], v21 offset:5120\n" \
    "s_waitcnt lgkmcnt(6)\n" \
    "v_mfma_f32_32x32x16_bf16 v[96:111], a[128:131], v[32:35], v[96:111]\n" \
    "ds_read_b64_tr_b16 v[48:49], v22 offset:4096\n" \
    "ds_read_b64_tr_b16 v[50:51], v22 offset:5120\n" \
    "s_waitcnt lgkmcnt(6)\n" \
    "v_mfma_f32_32x32x16_bf16 v[112:127], a[128:131], v[36:39], v[112:127]\n" \
    "ds_read_b64_tr_b16 v[52:53], v23 offset:4096\n" \
    "ds_read_b64_tr_b16 v[54:55], v23 offset:5120\n" \
    "s_waitcnt lgkmcnt(6)\n" \
    "v_mfma_f32_32x32x16_bf16 v[64:79], a[132:135], v[40:43], v[64:79]\n" \
    "ds_read_b64_tr_b16 v[24:25], v20 offset:8192\n" \
    "ds_read_b64_tr_b16 v[26:27], v20 offset:9216\n" \
    "s_waitcnt lgkmcnt(6)\n" \
    "v_mfma_f32_32x32x16_bf16 v[80:95], a[132:135], v[44:47], v[80:95]\n" \
    "ds_read_b64_tr_b16 v[28:29], v21 offset:8192\n" \
    "ds_read_b64_tr_b16 v[30:31], v21 offset:9216\n" \
    "s_waitcnt lgkmcnt(6)\n" \
    "v_mfma_f32_32x32x16_bf16 v[96:111], a[132:135], v[48:51], v[96:111]\n" \
    "ds_read_b64_tr_b16 v[32:33], v22 offset:8192\n" \
    "ds_read_b64_tr_b16 v[34:35], v22 offset:9216\n" \
    "s_waitcnt lgkmcnt(6)\n" \
    "v_mfma_f32_32x32x16_bf16 v[112:127], a[132:135], v[52:55], v[112:127]\n" \
    "ds_read_b64_tr_b16 v[36:37], v23 offset:8192\n" \
    "ds_read_b64_tr_b16 v[38:39], v23 offset:9216\n" \
    "s_cmp_eq_u32 s54, 0\n" \
    "s_cbranch_scc1 QVW8%=\n" \
    "s_waitcnt vmcnt(24)\n" \
    "s_cmp_eq_u32 s55, 0\n" \
    "s_cbranch_scc1 QVX8%=\n" \
    "s_waitcnt vmcnt(0)\n" \
    "s_branch QVX8%=\n" \
    "QVW8%=:\n" \
    "s_waitcnt vmcnt(4)\n" \
    "s_cmp_eq_u32 s55, 0\n" \
    "s_cbranch_scc1 QVX8%=\n" \
    "s_waitcnt vmcnt(0)\n" \
    "QVX8%=:\n" \
    "s_barrier\n" \
    "s_waitcnt lgkmcnt(6)\n" \
    "s_add_u32 m0, s51, 98304\n" \
    "s_cmp_lg_u32 s55, 0\n" \
    "v_mfma_f32_32x32x16_bf16 v[64:79], a[136:139], v[24:27], v[64:79]\n" \
    "s_cbranch_scc1 QVG8_0%=\n" \
    "global_load_lds_dwordx4 v1, s[42:43]\n" \
    "QVG8_0%=:\n" \
    "ds_read_b64_tr_b16 v[40:41], v20 offset:12288\n" \
    "ds_read_b64_tr_b16 v[42:43], v20 offset:13312\n" \
    "s_waitcnt lgkmcnt(6)\n" \
    "s_add_u32 m0, s51, 99328\n" \
    "s_cmp_lg_u32 s55, 0\n" \
    "v_mfma_f32_32x32x16_bf16 v[80:95], a[136:139], v[28:31], v[80:95]\n" \
    "s_cbranch_scc1 QVG8_1%=\n" \
    "global_load_lds_dwordx4 v2, s[42:43]\n" \
    "QVG8_1%=:\n" \
    "ds_read_b64_tr_b16 v[44:45], v21 offset:12288\n" \
    "ds_read_b64_tr_b16 v[46:47], v21 offset:13312\n" \
    "s_waitcnt lgkmcnt(6)\n" \
    "s_add_u32 m0, s51, 100352\n" \
    "s_cmp_lg_u32 s55, 0\n" \
    "v_mfma_f32_32x32x16_bf16 v[96:111], a[136:139], v[32:35], v[96:111]\n" \
    "s_cbranch_scc1 QVG8_2%=\n" \
    "global_load_lds_dwordx4 v3, s[42:43]\n" \
    "QVG8_2%=:\n" \
    "ds_read_b64_tr_b16 v[48:49], v22 offset:12288\n" \
    "ds_read_b64_tr_b16 v[50:51], v22 offset:13312\n" \
    "s_waitcnt lgkmcnt(6)\n" \
    "s_add_u32 m0, s51, 101376\n" \
    "s_cmp_lg_u32 s55, 0\n" \
    "v_mfma_f32_32x32x16_bf16 v[112:127], a[136:139], v[36:39], v[112:127]\n" \
    "s_cbranch_scc1 QVG8_3%=\n" \
    "global_load_lds_dwordx4 v4, s[42:43]\n" \
    "s_add_u32 s42, s42, 524288\n" \
    "s_addc_u32 s43, s43, 0\n" \
    "QVG8_3%=:\n" \
    "ds_read_b64_tr_b16 v[52:53], v23 offset:12288\n" \
    "ds_read_b64_tr_b16 v[54:55], v23 offset:13312\n" \
    "s_waitcnt lgkmcnt(6)\n" \
    "v_mfma_f32_32x32x16_bf16 v[64:79], a[140:143], v[40:43], v[64:79]\n" \
    "ds_read_b64_tr_b16 v[24:25], v20 offset:16384\n" \
    "ds_read_b64_tr_b16 v[26:27], v20 offset:17408\n" \
    "s_waitcnt lgkmcnt(6)\n" \
    "v_mfma_f32_32x32x16_bf16 v[80:95], a[140:143], v[44:47], v[80:95]\n" \
    "ds_read_b64_tr_b16 v[28:29], v21 offset:16384\n" \
    "ds_read_b64_tr_b16 v[30:31], v21 offset:17408\n" \
    "s_waitcnt lgkmcnt(6)\n" \
    "v_mfma_f32_32x32x16_bf16 v[96:111], a[140:143], v[48:51], v[96:111]\n" \
    "ds_read_b64_tr_b16 v[32:33], v22 offset:16384\n" \
    "ds_read_b64_tr_b16 v[34:35], v22 offset:17408\n" \
    "s_waitcnt lgkmcnt(6)\n" \
    "v_mfma_f32_32x32x16_bf16 v[112:127], a[140:143], v[52:55], v[112:127]\n" \
    "ds_read_b64_tr_b16 v[36:37], v23 offset:16384\n" \
    "ds_read_b64_tr_b16 v[38:39], v23 offset:17408\n" \
    "s_waitcnt lgkmcnt(6)\n" \
    "v_mfma_f32_32x32x16_bf16 v[64:79], a[144:147], v[24:27], v[64:79]\n" \
    "ds_read_b64_tr_b16 v[40:41], v20 offset:20480\n" \
    "ds_read_b64_tr_b16 v[42:43], v20 offset:21504\n" \
    "s_waitcnt lgkmcnt(6)\n" \
    "v_mfma_f32_32x32x16_bf16 v[80:95], a[144:147], v[28:31], v[80:95]\n" \
    "ds_read_b64_tr_b16 v[44:45], v21 offset:20480\n" \
    "ds_read_b64_tr_b16 v[46:47], v21 offset:21504\n" \
    "s_waitcnt lgkmcnt(6)\n" \
    "v_mfma_f32_32x32x16_bf16 v[96:111], a[144:147], v[32:35], v[96:111]\n" \
    "ds_read_b64_tr_b16 v[48:49], v22 offset:20480\n" \
    "ds_read_b64_tr_b16 v[50:51], v22 offset:21504\n" \
    "s_waitcnt lgkmcnt(6)\n" \
    "v_mfma_f32_32x32x16_bf16 v[112:127], a[144:147], v[36:39], v[112:127]\n" \
    "ds_read_b64_tr_b16 v[52:53], v23 offset:20480\n" \
    "ds_read_b64_tr_b16 v[54:55], v23 offset:21504\n" \
    "s_waitcnt lgkmcnt(6)\n" \
    "v_mfma_f32_32x32x16_bf16 v[64:79], a[148:151], v[40:43], v[64:79]\n" \
    "ds_read_b64_tr_b16 v[24:25], v20 offset:24576\n" \
    "ds_read_b64_tr_b16 v[26:27], v20 offset:25600\n" \
    "s_waitcnt lgkmcnt(6)\n" \
    "v_mfma_f32_32x32x16_bf16 v[80:95], a[148:151], v[44:47], v[80:95]\n" \
    "ds_read_b64_tr_b16 v[28:29], v21 offset:24576\n" \
    "ds_read_b64_tr_b16 v[30:31], v21 offset:25600\n" \
    "s_waitcnt lgkmcnt(6)\n" \
    "v_mfma_f32_32x32x16_bf16 v[96:111], a[148:151], v[48:51], v[96:111]\n" \
    "ds_read_b64_tr_b16 v[32:33], v22 offset:24576\n" \
    "ds_read_b64_tr_b16 v[34:35], v22 offset:25600\n" \
    "s_waitcnt lgkmcnt(6)\n" \
    "v_mfma_f32_32x32x16_bf16 v[112:127], a[148:151], v[52:55], v[112:127]\n" \
    "ds_read_b64_tr_b16 v[36:37], v23 offset:24576\n" \
    "ds_read_b64_tr_b16 v[38:39], v23 offset:25600\n" \
    "s_cmp_eq_u32 s54, 0\n" \
    "s_cbranch_scc1 QVW9%=\n" \
    "s_waitcnt vmcnt(24)\n" \
    "s_cmp_eq_u32 s55, 0\n" \
    "s_cbranch_scc1 QVX9%=\n" \
    "s_branch QVX9%=\n" \
    "QVW9%=:\n" \
    "s_cmp_eq_u32 s55, 0\n" \
    "s_cbranch_scc1 QVX9%=\n" \
    "QVX9%=:\n" \
    "s_barrier\n" \
    "s_waitcnt lgkmcnt(6)\n" \
    "s_add_u32 m0, s51, 114688\n" \
    "s_cmp_lg_u32 s55, 0\n" \
    "v_mfma_f32_32x32x16_bf16 v[64:79], a[152:155], v[24:27], v[64:79]\n" \
    "s_cbranch_scc1 QVG9_0%=\n" \
    "global_load_lds_dwordx4 v1, s[42:43]\n" \
    "QVG9_0%=:\n" \
    "ds_read_b64_tr_b16 v[40:41], v20 offset:28672\n" \
    "ds_read_b64_tr_b16 v[42:43], v20 offset:29696\n" \
    "s_waitcnt lgkmcnt(6)\n" \
    "s_add_u32 m0, s51, 115712\n" \
    "s_cmp_lg_u32 s55, 0\n" \
    "v_mfma_f32_32x32x16_bf16 v[80:95], a[152:155], v[28:31], v[80:95]\n" \
    "s_cbranch_scc1 QVG9_1%=\n" \
    "global_load_lds_dwordx4 v2, s[42:43]\n" \
    "QVG9_1%=:\n" \
    "ds_read_b64_tr_b16 v[44:45], v21 offset:28672\n" \
    "ds_read_b64_tr_b16 v[46:47], v21 offset:29696\n" \
    "s_waitcnt lgkmcnt(6)\n" \
    "s_add_u32 m0, s51, 116736\n" \
    "s_cmp_lg_u32 s55, 0\n" \
    "v_mfma_f32_32x32x16_bf16 v[96:111], a[152:155], v[32:35], v[96:111]\n" \
    "s_cbranch_scc1 QVG9_2%=\n" \
    "global_load_lds_dwordx4 v3, s[42:43]\n" \
    "QVG9_2%=:\n" \
    "ds_read_b64_tr_b16 v[48:49], v22 offset:28672\n" \
    "ds_read_b64_tr_b16 v[50:51], v22 offset:29696\n" \
    "s_waitcnt lgkmcnt(6)\n" \
    "s_add_u32 m0, s51, 117760\n" \
    "s_cmp_lg_u32 s55, 0\n" \
    "v_mfma_f32_32x32x16_bf16 v[112:127], a[152:155], v[36:39], v[112:127]\n" \
    "s_cbranch_scc1 QVG9_3%=\n" \
    "global_load_lds_dwordx4 v4, s[42:43]\n" \
    "s_add_u32 s42, s42, 524288\n" \
    "s_addc_u32 s43, s43, 0\n" \
    "QVG9_3%=:\n" \
    "ds_read_b64_tr_b16 v[52:53], v23 offset:28672\n" \
    "ds_read_b64_tr_b16 v[54:55], v23 offset:29696\n" \
    "s_waitcnt lgkmcnt(6)\n" \
    "v_mfma_f32_32x32x16_bf16 v[64:79], a[156:159], v[40:43], v[64:79]\n" \
    "ds_read_b64_tr_b16 v[24:25], v12 offset:0\n" \
    "ds_read_b64_tr_b16 v[26:27], v12 offset:1024\n" \
    "s_waitcnt lgkmcnt(6)\n" \
    "v_mfma_f32_32x32x16_bf16 v[80:95], a[156:159], v[44:47], v[80:95]\n" \
    "ds_read_b64_tr_b16 v[28:29], v13 offset:0\n" \
    "ds_read_b64_tr_b16 v[30:31], v13 offset:1024\n" \
    "s_waitcnt lgkmcnt(6)\n" \
    "v_mfma_f32_32x32x16_bf16 v[96:111], a[156:159], v[48:51], v[96:111]\n" \
    "ds_read_b64_tr_b16 v[32:33], v14 offset:0\n" \
    "ds_read_b64_tr_b16 v[34:35], v14 offset:1024\n" \
    "s_waitcnt lgkmcnt(6)\n" \
    "v_mfma_f32_32x32x16_bf16 v[112:127], a[156:159], v[52:55], v[112:127]\n" \
    "ds_read_b64_tr_b16 v[36:37], v15 offset:0\n" \
    "ds_read_b64_tr_b16 v[38:39], v15 offset:1024\n" \
    "QE%=:\n" \
    "s_cmp_eq_u32 s52, 0\n" \
    "s_cselect_b64 vcc, -1, 0\n" \
    "v_cndmask_b32 v9, v8, v7, vcc\n" \
    "s_mov_b64 s[46:47], %4\n" \
    "s_lshl_b32 s50, s52, 14\n" \
    "s_and_b32 s50, s50, 16384\n" \
    "s_add_u32 s50, s50, 4096\n" \
    "s_lshl_b32 s54, s52, 12\n" \
    "s_and_b32 s54, s54, 8192\n" \
    "s_xor_b32 s46, s46, s54\n" \
    "s_add_u32 s46, s46, s50\n" \
    "s_addc_u32 s47, s47, 0\n" \
    "s_mov_b64 s[48:49], %6\n" \
    "s_lshl_b32 s50, s52, 2\n" \
    "s_add_u32 s48, s48, s50\n" \
    "s_addc_u32 s49, s49, 0\n" \
    "s_mov_b32 s54, 1\n" \
    "v_cvt_pk_bf16_f32 v128, v64, v65\n" \
    "v_cvt_pk_bf16_f32 v129, v66, v67\n" \
    "v_cvt_pk_bf16_f32 v130, v68, v69\n" \
    "v_cvt_pk_bf16_f32 v131, v70, v71\n" \
    "v_cvt_pk_bf16_f32 v132, v72, v73\n" \
    "v_cvt_pk_bf16_f32 v133, v74, v75\n" \
    "v_cvt_pk_bf16_f32 v134, v76, v77\n" \
    "v_cvt_pk_bf16_f32 v135, v78, v79\n" \
    "v_cvt_pk_bf16_f32 v136, v80, v81\n" \
    "v_cvt_pk_bf16_f32 v137, v82, v83\n" \
    "v_cvt_pk_bf16_f32 v138, v84, v85\n" \
    "v_cvt_pk_bf16_f32 v139, v86, v87\n" \
    "v_cvt_pk_bf16_f32 v140, v88, v89\n" \
    "v_cvt_pk_bf16_f32 v141, v90, v91\n" \
    "v_cvt_pk_bf16_f32 v142, v92, v93\n" \
    "v_cvt_pk_bf16_f32 v143, v94, v95\n" \
    "v_cvt_pk_bf16_f32 v144, v96, v97\n" \
    "v_cvt_pk_bf16_f32 v145, v98, v99\n" \
    "v_cvt_pk_bf16_f32 v146, v100, v101\n" \
    "v_cvt_pk_bf16_f32 v147, v102, v103\n" \
    "v_cvt_pk_bf16_f32 v148, v104, v105\n" \
    "v_cvt_pk_bf16_f32 v149, v106, v107\n" \
    "v_cvt_pk_bf16_f32 v150, v108, v109\n" \
    "v_cvt_pk_bf16_f32 v151, v110, v111\n" \
    "v_cvt_pk_bf16_f32 v152, v112, v113\n" \
    "v_cvt_pk_bf16_f32 v153, v114, v115\n" \
    "v_cvt_pk_bf16_f32 v154, v116, v117\n" \
    "v_cvt_pk_bf16_f32 v155, v118, v119\n" \
    "v_cvt_pk_bf16_f32 v156, v120, v121\n" \
    "v_cvt_pk_bf16_f32 v157, v122, v123\n" \
    "v_cvt_pk_bf16_f32 v158, v124, v125\n" \
    "v_cvt_pk_bf16_f32 v159, v126, v127\n" \
    "s_add_u32 s52, s52, 1\n" \
    "s_cmp_lt_u32 s52, s53\n" \
    "s_cbranch_scc1 QITEM%=\n" \
    "global_store_dwordx4 v9, v[128:131], s[46:47] offset:-4096 sc1\n" \
    "global_store_dwordx4 v9, v[132:135], s[46:47] offset:-3584 sc1\n" \
    "global_store_dwordx4 v9, v[136:139], s[46:47] offset:-2048 sc1\n" \
    "global_store_dwordx4 v9, v[140:143], s[46:47] offset:-1536 sc1\n" \
    "global_store_dwordx4 v9, v[144:147], s[46:47] offset:0 sc1\n" \
    "global_store_dwordx4 v9, v[148:151], s[46:47] offset:512 sc1\n" \
    "global_store_dwordx4 v9, v[152:155], s[46:47] offset:2048 sc1\n" \
    "global_store_dwordx4 v9, v[156:159], s[46:47] offset:2560 sc1\n" \
    "s_waitcnt vmcnt(0) lgkmcnt(0)\n" \
    "v_mov_b32 v10, 0\n" \
    "v_mov_b32 v11, %7\n" \
    "s_mov_b64 exec, 1\n" \
    "global_store_dword v10, v11, s[48:49] sc1\n" \
    "s_mov_b64 exec, -1\n" \
    ""
#define OUTP_ASM \
    "s_mov_b32 s40, %3\n" \
    "s_mov_b32 s52, %4\n" \
    "s_mov_b64 exec, -1\n" \
    "v_mbcnt_lo_u32_b32 v0, -1, 0\n" \
    "v_mbcnt_hi_u32_b32 v0, -1, v0\n" \
    "v_lshrrev_b32 v14, 4, v0\n" \
    "s_lshl_b32 s50, s40, 4\n" \
    "v_add_u32 v15, s50, v14\n" \
    "s_mov_b32 s50, 1280\n" \
    "v_mul_lo_u32 v1, v15, s50\n" \
    "v_lshlrev_b32 v14, 2, v14\n" \
    "v_and_b32 v15, 15, v0\n" \
    "v_xor_b32 v14, v14, v15\n" \
    "v_lshl_add_u32 v1, v14, 4, v1\n" \
    "v_lshrrev_b32 v14, 5, v0\n" \
    "v_lshlrev_b32 v14, 2, v14\n" \
    "v_bfe_u32 v15, v0, 2, 2\n" \
    "v_add_lshl_u32 v14, v14, v15, 8\n" \
    "v_bfe_u32 v2, v0, 4, 1\n" \
    "v_lshl_add_u32 v14, v2, 5, v14\n" \
    "v_and_b32 v2, 3, v0\n" \
    "v_lshl_add_u32 v14, v2, 3, v14\n" \
    "v_xor_b32 v2, 0, v15\n" \
    "v_lshl_add_u32 v2, v2, 6, v14\n" \
    "v_add_u32 v6, 0x10000, v2\n" \
    "v_xor_b32 v3, 1, v15\n" \
    "v_lshl_add_u32 v3, v3, 6, v14\n" \
    "v_add_u32 v7, 0x10000, v3\n" \
    "v_xor_b32 v4, 2, v15\n" \
    "v_lshl_add_u32 v4, v4, 6, v14\n" \
    "v_add_u32 v8, 0x10000, v4\n" \
    "v_xor_b32 v5, 3, v15\n" \
    "v_lshl_add_u32 v5, v5, 6, v14\n" \
    "v_add_u32 v9, 0x10000, v5\n" \
    "v_lshlrev_b32 v10, 4, v0\n" \
    "s_mov_b64 s[42:43], %0\n" \
    "s_mov_b64 s[44:45], %1\n" \
    "s_lshl_b32 s51, s40, 12\n" \
    "v_mov_b64 v[64:65], 0\n" \
    "v_mov_b64 v[66:67], 0\n" \
    "v_mov_b64 v[68:69], 0\n" \
    "v_mov_b64 v[70:71], 0\n" \
    "v_mov_b64 v[72:73], 0\n" \
    "v_mov_b64 v[74:75], 0\n" \
    "v_mov_b64 v[76:77], 0\n" \
    "v_mov_b64 v[78:79], 0\n" \
    "v_mov_b64 v[80:81], 0\n" \
    "v_mov_b64 v[82:83], 0\n" \
    "v_mov_b64 v[84:85], 0\n" \
    "v_mov_b64 v[86:87], 0\n" \
    "v_mov_b64 v[88:89], 0\n" \
    "v_mov_b64 v[90:91], 0\n" \
    "v_mov_b64 v[92:93], 0\n" \
    "v_mov_b64 v[94:95], 0\n" \
    "v_mov_b64 v[96:97], 0\n" \
    "v_mov_b64 v[98:99], 0\n" \
    "v_mov_b64 v[100:101], 0\n" \
    "v_mov_b64 v[102:103], 0\n" \
    "v_mov_b64 v[104:105], 0\n" \
    "v_mov_b64 v[106:107], 0\n" \
    "v_mov_b64 v[108:109], 0\n" \
    "v_mov_b64 v[110:111], 0\n" \
    "v_mov_b64 v[112:113], 0\n" \
    "v_mov_b64 v[114:115], 0\n" \
    "v_mov_b64 v[116:117], 0\n" \
    "v_mov_b64 v[118:119], 0\n" \
    "v_mov_b64 v[120:121], 0\n" \
    "v_mov_b64 v[122:123], 0\n" \
    "v_mov_b64 v[124:125], 0\n" \
    "v_mov_b64 v[126:127], 0\n" \
    "s_barrier\n" \
    "s_add_u32 m0, s51, 0\n" \
    "s_nop 0\n" \
    "global_load_lds_dwordx4 v1, s[42:43]\n" \
    "s_add_u32 s42, s42, 5120\n" \
    "s_addc_u32 s43, s43, 0\n" \
    "s_add_u32 m0, s51, 1024\n" \
    "s_nop 0\n" \
    "global_load_lds_dwordx4 v1, s[42:43]\n" \
    "s_add_u32 s42, s42, 5120\n" \
    "s_addc_u32 s43, s43, 0\n" \
    "s_add_u32 m0, s51, 2048\n" \
    "s_nop 0\n" \
    "global_load_lds_dwordx4 v1, s[42:43]\n" \
    "s_add_u32 s42, s42, 5120\n" \
    "s_addc_u32 s43, s43, 0\n" \
    "s_add_u32 m0, s51, 3072\n" \
    "s_nop 0\n" \
    "global_load_lds_dwordx4 v1, s[42:43]\n" \
    "s_add_u32 s42, s42, 66560\n" \
    "s_addc_u32 s43, s43, 0\n" \
    "s_add_u32 m0, s51, 16384\n" \
    "s_nop 0\n" \
    "global_load_lds_dwordx4 v1, s[42:43]\n" \
    "s_add_u32 s42, s42, 5120\n" \
    "s_addc_u32 s43, s43, 0\n" \
    "s_add_u32 m0, s51, 17408\n" \
    "s_nop 0\n" \
    "global_load_lds_dwordx4 v1, s[42:43]\n" \
    "s_add_u32 s42, s42, 5120\n" \
    "s_addc_u32 s43, s43, 0\n" \
    "s_add_u32 m0, s51, 18432\n" \
    "s_nop 0\n" \
    "global_load_lds_dwordx4 v1, s[42:43]\n" \
    "s_add_u32 s42, s42, 5120\n" \
    "s_addc_u32 s43, s43, 0\n" \
    "s_add_u32 m0, s51, 19456\n" \
    "s_nop 0\n" \
    "global_load_lds_dwordx4 v1, s[42:43]\n" \
    "s_add_u32 s42, s42, 66560\n" \
    "s_addc_u32 s43, s43, 0\n" \
    "s_add_u32 m0, s51, 32768\n" \
    "s_nop 0\n" \
    "global_load_lds_dwordx4 v1, s[42:43]\n" \
    "s_add_u32 s42, s42, 5120\n" \
    "s_addc_u32 s43, s43, 0\n" \
    "s_add_u32 m0, s51, 33792\n" \
    "s_nop 0\n" \
    "global_load_lds_dwordx4 v1, s[42:43]\n" \
    "s_add_u32 s42, s42, 5120\n" \
    "s_addc_u32 s43, s43, 0\n" \
    "s_add_u32 m0, s51, 34816\n" \
    "s_nop 0\n" \
    "global_load_lds_dwordx4 v1, s[42:43]\n" \
    "s_add_u32 s42, s42, 5120\n" \
    "s_addc_u32 s43, s43, 0\n" \
    "s_add_u32 m0, s51, 35840\n" \
    "s_nop 0\n" \
    "global_load_lds_dwordx4 v1, s[42:43]\n" \
    "s_add_u32 s42, s42, 66560\n" \
    "s_addc_u32 s43, s43, 0\n" \
    "s_add_u32 m0, s51, 49152\n" \
    "s_nop 0\n" \
    "global_load_lds_dwordx4 v1, s[42:43]\n" \
    "s_add_u32 s42, s42, 5120\n" \
    "s_addc_u32 s43, s43, 0\n" \
    "s_add_u32 m0, s51, 50176\n" \
    "s_nop 0\n" \
    "global_load_lds_dwordx4 v1, s[42:43]\n" \
    "s_add_u32 s42, s42, 5120\n" \
    "s_addc_u32 s43, s43, 0\n" \
    "s_add_u32 m0, s51, 51200\n" \
    "s_nop 0\n" \
    "global_load_lds_dwordx4 v1, s[42:43]\n" \
    "s_add_u32 s42, s42, 5120\n" \
    "s_addc_u32 s43, s43, 0\n" \
    "s_add_u32 m0, s51, 52224\n" \
    "s_nop 0\n" \
    "global_load_lds_dwordx4 v1, s[42:43]\n" \
    "s_add_u32 s42, s42, 66560\n" \
    "s_addc_u32 s43, s43, 0\n" \
    "s_add_u32 m0, s51, 65536\n" \
    "s_nop 0\n" \
    "global_load_lds_dwordx4 v1, s[42:43]\n" \
    "s_add_u32 s42, s42, 5120\n" \
    "s_addc_u32 s43, s43, 0\n" \
    "s_add_u32 m0, s51, 66560\n" \
    "s_nop 0\n" \
    "global_load_lds_dwordx4 v1, s[42:43]\n" \
    "s_add_u32 s42, s42, 5120\n" \
    "s_addc_u32 s43, s43, 0\n" \
    "s_add_u32 m0, s51, 67584\n" \
    "s_nop 0\n" \
    "global_load_lds_dwordx4 v1, s[42:43]\n" \
    "s_add_u32 s42, s42, 5120\n" \
    "s_addc_u32 s43, s43, 0\n" \
    "s_add_u32 m0, s51, 68608\n" \
    "s_nop 0\n" \
    "global_load_lds_dwordx4 v1, s[42:43]\n" \
    "s_add_u32 s42, s42, 66560\n" \
    "s_addc_u32 s43, s43, 0\n" \
    "s_add_u32 m0, s51, 81920\n" \
    "s_nop 0\n" \
    "global_load_lds_dwordx4 v1, s[42:43]\n" \
    "s_add_u32 s42, s42, 5120\n" \
    "s_addc_u32 s43, s43, 0\n" \
    "s_add_u32 m0, s51, 82944\n" \
    "s_nop 0\n" \
    "global_load_lds_dwordx4 v1, s[42:43]\n" \
    "s_add_u32 s42, s42, 5120\n" \
    "s_addc_u32 s43, s43, 0\n" \
    "s_add_u32 m0, s51, 83968\n" \
    "s_nop 0\n" \
    "global_load_lds_dwordx4 v1, s[42:43]\n" \
    "s_add_u32 s42, s42, 5120\n" \
    "s_addc_u32 s43, s43, 0\n" \
    "s_add_u32 m0, s51, 84992\n" \
    "s_nop 0\n" \
    "global_load_lds_dwordx4 v1, s[42:43]\n" \
    "s_add_u32 s42, s42, 66560\n" \
    "s_addc_u32 s43, s43, 0\n" \
    "s_add_u32 m0, s51, 98304\n" \
    "s_nop 0\n" \
    "global_load_lds_dwordx4 v1, s[42:43]\n" \
    "s_add_u32 s42, s42, 5120\n" \
    "s_addc_u32 s43, s43, 0\n" \
    "s_add_u32 m0, s51, 99328\n" \
    "s_nop 0\n" \
    "global_load_lds_dwordx4 v1, s[42:43]\n" \
    "s_add_u32 s42, s42, 5120\n" \
    "s_addc_u32 s43, s43, 0\n" \
    "s_add_u32 m0, s51, 100352\n" \
    "s_nop 0\n" \
    "global_load_lds_dwordx4 v1, s[42:43]\n" \
    "s_add_u32 s42, s42, 5120\n" \
    "s_addc_u32 s43, s43, 0\n" \
    "s_add_u32 m0, s51, 101376\n" \
    "s_nop 0\n" \
    "global_load_lds_dwordx4 v1, s[42:43]\n" \
    "s_add_u32 s42, s42, 66560\n" \
    "s_addc_u32 s43, s43, 0\n" \
    "v_mov_b32 v14, 0\n" \
    "s_mov_b32 s50, 0\n" \
    "OWAIT%=:\n" \
    "global_load_dword v15, v14, %5 sc1\n" \
    "s_waitcnt vmcnt(0)\n" \
    "v_readfirstlane_b32 s54, v15\n" \
    "s_cmp_ge_u32 s54, %6\n" \
    "s_cbranch_scc1 OGO%=\n" \
    "s_sleep 2\n" \
    "s_add_u32 s50, s50, 1\n" \
    "s_cmp_lt_u32 s50, 131072\n" \
    "s_cbranch_scc1 OWAIT%=\n" \
    "OGO%=:\n" \
    "global_load_dwordx4 v[128:131], v10, s[44:45] offset:0 sc1\n" \
    "global_load_dwordx4 v[132:135], v10, s[44:45] offset:1024 sc1\n" \
    "global_load_dwordx4 v[136:139], v10, s[44:45] offset:2048 sc1\n" \
    "global_load_dwordx4 v[140:143], v10, s[44:45] offset:3072 sc1\n" \
    "s_add_u32 s44, s44, 4096\n" \
    "s_addc_u32 s45, s45, 0\n" \
    "global_load_dwordx4 v[144:147], v10, s[44:45] offset:0 sc1\n" \
    "global_load_dwordx4 v[148:151], v10, s[44:45] offset:1024 sc1\n" \
    "global_load_dwordx4 v[152:155], v10, s[44:45] offset:2048 sc1\n" \
    "global_load_dwordx4 v[156:159], v10, s[44:45] offset:3072 sc1\n" \
    "s_add_u32 s44, s44, 4096\n" \
    "s_addc_u32 s45, s45, 0\n" \
    "global_load_dwordx4 v[160:163], v10, s[44:45] offset:0 sc1\n" \
    "global_load_dwordx4 v[164:167], v10, s[44:45] offset:1024 sc1\n" \
    "global_load_dwordx4 v[168:171], v10, s[44:45] offset:2048 sc1\n" \
    "global_load_dwordx4 v[172:175], v10, s[44:45] offset:3072 sc1\n" \
    "s_add_u32 s44, s44, 4096\n" \
    "s_addc_u32 s45, s45, 0\n" \
    "s_barrier\n" \
    "ds_read_b64_tr_b16 v[16:17], v2 offset:0\n" \
    "ds_read_b64_tr_b16 v[18:19], v2 offset:2048\n" \
    "ds_read_b64_tr_b16 v[20:21], v3 offset:0\n" \
    "ds_read_b64_tr_b16 v[22:23], v3 offset:2048\n" \
    "ds_read_b64_tr_b16 v[24:25], v4 offset:0\n" \
    "ds_read_b64_tr_b16 v[26:27], v4 offset:2048\n" \
    "ds_read_b64_tr_b16 v[28:29], v5 offset:0\n" \
    "ds_read_b64_tr_b16 v[30:31], v5 offset:2048\n" \
    "s_waitcnt vmcnt(0)\n" \
    "s_cmp_le_u32 s52, 4\n" \
    "s_cbranch_scc1 OFIN%=\n" \
    "OLOOP%=:\n" \
    "s_waitcnt vmcnt(20)\n" \
    "s_waitcnt lgkmcnt(6)\n" \
    "v_mfma_f32_32x32x16_bf16 v[64:79], v[128:131], v[16:19], v[64:79]\n" \
    "ds_read_b64_tr_b16 v[32:33], v2 offset:4096\n" \
    "ds_read_b64_tr_b16 v[34:35], v2 offset:6144\n" \
    "global_load_dwordx4 v[176:179], v10, s[44:45] offset:0 sc1\n" \
    "global_load_dwordx4 v[180:183], v10, s[44:45] offset:1024 sc1\n" \
    "s_waitcnt lgkmcnt(6)\n" \
    "v_mfma_f32_32x32x16_bf16 v[80:95], v[128:131], v[20:23], v[80:95]\n" \
    "ds_read_b64_tr_b16 v[36:37], v3 offset:4096\n" \
    "ds_read_b64_tr_b16 v[38:39], v3 offset:6144\n" \
    "global_load_dwordx4 v[184:187], v10, s[44:45] offset:2048 sc1\n" \
    "global_load_dwordx4 v[188:191], v10, s[44:45] offset:3072 sc1\n" \
    "s_add_u32 s44, s44, 4096\n" \
    "s_addc_u32 s45, s45, 0\n" \
    "s_waitcnt lgkmcnt(6)\n" \
    "v_mfma_f32_32x32x16_bf16 v[96:111], v[128:131], v[24:27], v[96:111]\n" \
    "ds_read_b64_tr_b16 v[40:41], v4 offset:4096\n" \
    "ds_read_b64_tr_b16 v[42:43], v4 offset:6144\n" \
    "s_waitcnt lgkmcnt(6)\n" \
    "v_mfma_f32_32x32x16_bf16 v[112:127], v[128:131], v[28:31], v[112:127]\n" \
    "ds_read_b64_tr_b16 v[44:45], v5 offset:4096\n" \
    "ds_read_b64_tr_b16 v[46:47], v5 offset:6144\n" \
    "s_waitcnt lgkmcnt(6)\n" \
    "v_mfma_f32_32x32x16_bf16 v[64:79], v[132:135], v[32:35], v[64:79]\n" \
    "ds_read_b64_tr_b16 v[16:17], v2 offset:8192\n" \
    "ds_read_b64_tr_b16 v[18:19], v2 offset:10240\n" \
    "s_waitcnt lgkmcnt(6)\n" \
    "v_mfma_f32_32x32x16_bf16 v[80:95], v[132:135], v[36:39], v[80:95]\n" \
    "ds_read_b64_tr_b16 v[20:21], v3 offset:8192\n" \
    "ds_read_b64_tr_b16 v[22:23], v3 offset:10240\n" \
    "s_waitcnt lgkmcnt(6)\n" \
    "v_mfma_f32_32x32x16_bf16 v[96:111], v[132:135], v[40:43], v[96:111]\n" \
    "ds_read_b64_tr_b16 v[24:25], v4 offset:8192\n" \
    "ds_read_b64_tr_b16 v[26:27], v4 offset:10240\n" \
    "s_waitcnt lgkmcnt(6)\n" \
    "v_mfma_f32_32x32x16_bf16 v[112:127], v[132:135], v[44:47], v[112:127]\n" \
    "ds_read_b64_tr_b16 v[28:29], v5 offset:8192\n" \
    "ds_read_b64_tr_b16 v[30:31], v5 offset:10240\n" \
    "s_barrier\n" \
    "s_waitcnt lgkmcnt(6)\n" \
    "v_mfma_f32_32x32x16_bf16 v[64:79], v[136:139], v[16:19], v[64:79]\n" \
    "ds_read_b64_tr_b16 v[32:33], v2 offset:12288\n" \
    "ds_read_b64_tr_b16 v[34:35], v2 offset:14336\n" \
    "s_add_u32 m0, s51, 114688\n" \
    "s_nop 0\n" \
    "global_load_lds_dwordx4 v1, s[42:43]\n" \
    "s_add_u32 s42, s42, 5120\n" \
    "s_addc_u32 s43, s43, 0\n" \
    "s_waitcnt lgkmcnt(6)\n" \
    "v_mfma_f32_32x32x16_bf16 v[80:95], v[136:139], v[20:23], v[80:95]\n" \
    "ds_read_b64_tr_b16 v[36:37], v3 offset:12288\n" \
    "ds_read_b64_tr_b16 v[38:39], v3 offset:14336\n" \
    "s_add_u32 m0, s51, 115712\n" \
    "s_nop 0\n" \
    "global_load_lds_dwordx4 v1, s[42:43]\n" \
    "s_add_u32 s42, s42, 5120\n" \
    "s_addc_u32 s43, s43, 0\n" \
    "s_waitcnt lgkmcnt(6)\n" \
    "v_mfma_f32_32x32x16_bf16 v[96:111], v[136:139], v[24:27], v[96:111]\n" \
    "ds_read_b64_tr_b16 v[40:41], v4 offset:12288\n" \
    "ds_read_b64_tr_b16 v[42:43], v4 offset:14336\n" \
    "s_add_u32 m0, s51, 116736\n" \
    "s_nop 0\n" \
    "global_load_lds_dwordx4 v1, s[42:43]\n" \
    "s_add_u32 s42, s42, 5120\n" \
    "s_addc_u32 s43, s43, 0\n" \
    "s_waitcnt lgkmcnt(6)\n" \
    "v_mfma_f32_32x32x16_bf16 v[112:127], v[136:139], v[28:31], v[112:127]\n" \
    "ds_read_b64_tr_b16 v[44:45], v5 offset:12288\n" \
    "ds_read_b64_tr_b16 v[46:47], v5 offset:14336\n" \
    "s_add_u32 m0, s51, 117760\n" \
    "s_nop 0\n" \
    "global_load_lds_dwordx4 v1, s[42:43]\n" \
    "s_add_u32 s42, s42, 66560\n" \
    "s_addc_u32 s43, s43, 0\n" \
    "s_waitcnt lgkmcnt(6)\n" \
    "v_mfma_f32_32x32x16_bf16 v[64:79], v[140:143], v[32:35], v[64:79]\n" \
    "ds_read_b64_tr_b16 v[16:17], v2 offset:16384\n" \
    "ds_read_b64_tr_b16 v[18:19], v2 offset:18432\n" \
    "s_waitcnt lgkmcnt(6)\n" \
    "v_mfma_f32_32x32x16_bf16 v[80:95], v[140:143], v[36:39], v[80:95]\n" \
    "ds_read_b64_tr_b16 v[20:21], v3 offset:16384\n" \
    "ds_read_b64_tr_b16 v[22:23], v3 offset:18432\n" \
    "s_waitcnt lgkmcnt(6)\n" \
    "v_mfma_f32_32x32x16_bf16 v[96:111], v[140:143], v[40:43], v[96:111]\n" \
    "ds_read_b64_tr_b16 v[24:25], v4 offset:16384\n" \
    "ds_read_b64_tr_b16 v[26:27], v4 offset:18432\n" \
    "s_waitcnt lgkmcnt(6)\n" \
    "v_mfma_f32_32x32x16_bf16 v[112:127], v[140:143], v[44:47], v[112:127]\n" \
    "ds_read_b64_tr_b16 v[28:29], v5 offset:16384\n" \
    "ds_read_b64_tr_b16 v[30:31], v5 offset:18432\n" \
    "s_waitcnt vmcnt(20)\n" \
    "s_waitcnt lgkmcnt(6)\n" \
    "v_mfma_f32_32x32x16_bf16 v[64:79], v[144:147], v[16:19], v[64:79]\n" \
    "ds_read_b64_tr_b16 v[32:33], v2 offset:20480\n" \
    "ds_read_b64_tr_b16 v[34:35], v2 offset:22528\n" \
    "global_load_dwordx4 v[128:131], v10, s[44:45] offset:0 sc1\n" \
    "global_load_dwordx4 v[132:135], v10, s[44:45] offset:1024 sc1\n" \
    "s_waitcnt lgkmcnt(6)\n" \
    "v_mfma_f32_32x32x16_bf16 v[80:95], v[144:147], v[20:23], v[80:95]\n" \
    "ds_read_b64_tr_b16 v[36:37], v3 offset:20480\n" \
    "ds_read_b64_tr_b16 v[38:39], v3 offset:22528\n" \
    "global_load_dwordx4 v[136:139], v10, s[44:45] offset:2048 sc1\n" \
    "global_load_dwordx4 v[140:143], v10, s[44:45] offset:3072 sc1\n" \
    "s_add_u32 s44, s44, 4096\n" \
    "s_addc_u32 s45, s45, 0\n" \
    "s_waitcnt lgkmcnt(6)\n" \
    "v_mfma_f32_32x32x16_bf16 v[96:111], v[144:147], v[24:27], v[96:111]\n" \
    "ds_read_b64_tr_b16 v[40:41], v4 offset:20480\n" \
    "ds_read_b64_tr_b16 v[42:43], v4 offset:22528\n" \
    "s_waitcnt lgkmcnt(6)\n" \
    "v_mfma_f32_32x32x16_bf16 v[112:127], v[144:147], v[28:31], v[112:127]\n" \
    "ds_read_b64_tr_b16 v[44:45], v5 offset:20480\n" \
    "ds_read_b64_tr_b16 v[46:47], v5 offset:22528\n" \
    "s_waitcnt lgkmcnt(6)\n" \
    "v_mfma_f32_32x32x16_bf16 v[64:79], v[148:151], v[32:35], v[64:79]\n" \
    "ds_read_b64_tr_b16 v[16:17], v2 offset:24576\n" \
    "ds_read_b64_tr_b16 v[18:19], v2 offset:26624\n" \
    "s_waitcnt lgkmcnt(6)\n" \
    "v_mfma_f32_32x32x16_bf16 v[80:95], v[148:151], v[36:39], v[80:95]\n" \
    "ds_read_b64_tr_b16 v[20:21], v3 offset:24576\n" \
    "ds_read_b64_tr_b16 v[22:23], v3 offset:26624\n" \
    "s_waitcnt lgkmcnt(6)\n" \
    "v_mfma_f32_32x32x16_bf16 v[96:111], v[148:151], v[40:43], v[96:111]\n" \
    "ds_read_b64_tr_b16 v[24:25], v4 offset:24576\n" \
    "ds_read_b64_tr_b16 v[26:27], v4 offset:26624\n" \
    "s_waitcnt lgkmcnt(6)\n" \
    "v_mfma_f32_32x32x16_bf16 v[112:127], v[148:151], v[44:47], v[112:127]\n" \
    "ds_read_b64_tr_b16 v[28:29], v5 offset:24576\n" \
    "ds_read_b64_tr_b16 v[30:31], v5 offset:26624\n" \
    "s_barrier\n" \
    "s_waitcnt lgkmcnt(6)\n" \
    "v_mfma_f32_32x32x16_bf16 v[64:79], v[152:155], v[16:19], v[64:79]\n" \
    "ds_read_b64_tr_b16 v[32:33], v2 offset:28672\n" \
    "ds_read_b64_tr_b16 v[34:35], v2 offset:30720\n" \
    "s_add_u32 m0, s51, 0\n" \
    "s_nop 0\n" \
    "global_load_lds_dwordx4 v1, s[42:43]\n" \
    "s_add_u32 s42, s42, 5120\n" \
    "s_addc_u32 s43, s43, 0\n" \
    "s_waitcnt lgkmcnt(6)\n" \
    "v_mfma_f32_32x32x16_bf16 v[80:95], v[152:155], v[20:23], v[80:95]\n" \
    "ds_read_b64_tr_b16 v[36:37], v3 offset:28672\n" \
    "ds_read_b64_tr_b16 v[38:39], v3 offset:30720\n" \
    "s_add_u32 m0, s51, 1024\n" \
    "s_nop 0\n" \
    "global_load_lds_dwordx4 v1, s[42:43]\n" \
    "s_add_u32 s42, s42, 5120\n" \
    "s_addc_u32 s43, s43, 0\n" \
    "s_waitcnt lgkmcnt(6)\n" \
    "v_mfma_f32_32x32x16_bf16 v[96:111], v[152:155], v[24:27], v[96:111]\n" \
    "ds_read_b64_tr_b16 v[40:41], v4 offset:28672\n" \
    "ds_read_b64_tr_b16 v[42:43], v4 offset:30720\n" \
    "s_add_u32 m0, s51, 2048\n" \
    "s_nop 0\n" \
    "global_load_lds_dwordx4 v1, s[42:43]\n" \
    "s_add_u32 s42, s42, 5120\n" \
    "s_addc_u32 s43, s43, 0\n" \
    "s_waitcnt lgkmcnt(6)\n" \
    "v_mfma_f32_32x32x16_bf16 v[112:127], v[152:155], v[28:31], v[112:127]\n" \
    "ds_read_b64_tr_b16 v[44:45], v5 offset:28672\n" \
    "ds_read_b64_tr_b16 v[46:47], v5 offset:30720\n" \
    "s_add_u32 m0, s51, 3072\n" \
    "s_nop 0\n" \
    "global_load_lds_dwordx4 v1, s[42:43]\n" \
    "s_add_u32 s42, s42, 66560\n" \
    "s_addc_u32 s43, s43, 0\n" \
    "s_waitcnt lgkmcnt(6)\n" \
    "v_mfma_f32_32x32x16_bf16 v[64:79], v[156:159], v[32:35], v[64:79]\n" \
    "ds_read_b64_tr_b16 v[16:17], v2 offset:32768\n" \
    "ds_read_b64_tr_b16 v[18:19], v2 offset:34816\n" \
    "s_waitcnt lgkmcnt(6)\n" \
    "v_mfma_f32_32x32x16_bf16 v[80:95], v[156:159], v[36:39], v[80:95]\n" \
    "ds_read_b64_tr_b16 v[20:21], v3 offset:32768\n" \
    "ds_read_b64_tr_b16 v[22:23], v3 offset:34816\n" \
    "s_waitcnt lgkmcnt(6)\n" \
    "v_mfma_f32_32x32x16_bf16 v[96:111], v[156:159], v[40:43], v[96:111]\n" \
    "ds_read_b64_tr_b16 v[24:25], v4 offset:32768\n" \
    "ds_read_b64_tr_b16 v[26:27], v4 offset:34816\n" \
    "s_waitcnt lgkmcnt(6)\n" \
    "v_mfma_f32_32x32x16_bf16 v[112:127], v[156:159], v[44:47], v[112:127]\n" \
    "ds_read_b64_tr_b16 v[28:29], v5 offset:32768\n" \
    "ds_read_b64_tr_b16 v[30:31], v5 offset:34816\n" \
    "s_waitcnt vmcnt(20)\n" \
    "s_waitcnt lgkmcnt(6)\n" \
    "v_mfma_f32_32x32x16_bf16 v[64:79], v[160:163], v[16:19], v[64:79]\n" \
    "ds_read_b64_tr_b16 v[32:33], v2 offset:36864\n" \
    "ds_read_b64_tr_b16 v[34:35], v2 offset:38912\n" \
    "global_load_dwordx4 v[144:147], v10, s[44:45] offset:0 sc1\n" \
    "global_load_dwordx4 v[148:151], v10, s[44:45] offset:1024 sc1\n" \
    "s_waitcnt lgkmcnt(6)\n" \
    "v_mfma_f32_32x32x16_bf16 v[80:95], v[160:163], v[20:23], v[80:95]\n" \
    "ds_read_b64_tr_b16 v[36:37], v3 offset:36864\n" \
    "ds_read_b64_tr_b16 v[38:39], v3 offset:38912\n" \
    "global_load_dwordx4 v[152:155], v10, s[44:45] offset:2048 sc1\n" \
    "global_load_dwordx4 v[156:159], v10, s[44:45] offset:3072 sc1\n" \
    "s_add_u32 s44, s44, 4096\n" \
    "s_addc_u32 s45, s45, 0\n" \
    "s_waitcnt lgkmcnt(6)\n" \
    "v_mfma_f32_32x32x16_bf16 v[96:111], v[160:163], v[24:27], v[96:111]\n" \
    "ds_read_b64_tr_b16 v[40:41], v4 offset:36864\n" \
    "ds_read_b64_tr_b16 v[42:43], v4 offset:38912\n" \
    "s_waitcnt lgkmcnt(6)\n" \
    "v_mfma_f32_32x32x16_bf16 v[112:127], v[160:163], v[28:31], v[112:127]\n" \
    "ds_read_b64_tr_b16 v[44:45], v5 offset:36864\n" \
    "ds_read_b64_tr_b16 v[46:47], v5 offset:38912\n" \
    "s_waitcnt lgkmcnt(6)\n" \
    "v_mfma_f32_32x32x16_bf16 v[64:79], v[164:167], v[32:35], v[64:79]\n" \
    "ds_read_b64_tr_b16 v[16:17], v2 offset:40960\n" \
    "ds_read_b64_tr_b16 v[18:19], v2 offset:43008\n" \
    "s_waitcnt lgkmcnt(6)\n" \
    "v_mfma_f32_32x32x16_bf16 v[80:95], v[164:167], v[36:39], v[80:95]\n" \
    "ds_read_b64_tr_b16 v[20:21], v3 offset:40960\n" \
    "ds_read_b64_tr_b16 v[22:23], v3 offset:43008\n" \
    "s_waitcnt lgkmcnt(6)\n" \
    "v_mfma_f32_32x32x16_bf16 v[96:111], v[164:167], v[40:43], v[96:111]\n" \
    "ds_read_b64_tr_b16 v[24:25], v4 offset:40960\n" \
    "ds_read_b64_tr_b16 v[26:27], v4 offset:43008\n" \
    "s_waitcnt lgkmcnt(6)\n" \
    "v_mfma_f32_32x32x16_bf16 v[112:127], v[164:167], v[44:47], v[112:127]\n" \
    "ds_read_b64_tr_b16 v[28:29], v5 offset:40960\n" \
    "ds_read_b64_tr_b16 v[30:31], v5 offset:43008\n" \
    "s_barrier\n" \
    "s_waitcnt lgkmcnt(6)\n" \
    "v_mfma_f32_32x32x16_bf16 v[64:79], v[168:171], v[16:19], v[64:79]\n" \
    "ds_read_b64_tr_b16 v[32:33], v2 offset:45056\n" \
    "ds_read_b64_tr_b16 v[34:35], v2 offset:47104\n" \
    "s_add_u32 m0, s51, 16384\n" \
    "s_nop 0\n" \
    "global_load_lds_dwordx4 v1, s[42:43]\n" \
    "s_add_u32 s42, s42, 5120\n" \
    "s_addc_u32 s43, s43, 0\n" \
    "s_waitcnt lgkmcnt(6)\n" \
    "v_mfma_f32_32x32x16_bf16 v[80:95], v[168:171], v[20:23], v[80:95]\n" \
    "ds_read_b64_tr_b16 v[36:37], v3 offset:45056\n" \
    "ds_read_b64_tr_b16 v[38:39], v3 offset:47104\n" \
    "s_add_u32 m0, s51, 17408\n" \
    "s_nop 0\n" \
    "global_load_lds_dwordx4 v1, s[42:43]\n" \
    "s_add_u32 s42, s42, 5120\n" \
    "s_addc_u32 s43, s43, 0\n" \
    "s_waitcnt lgkmcnt(6)\n" \
    "v_mfma_f32_32x32x16_bf16 v[96:111], v[168:171], v[24:27], v[96:111]\n" \
    "ds_read_b64_tr_b16 v[40:41], v4 offset:45056\n" \
    "ds_read_b64_tr_b16 v[42:43], v4 offset:47104\n" \
    "s_add_u32 m0, s51, 18432\n" \
    "s_nop 0\n" \
    "global_load_lds_dwordx4 v1, s[42:43]\n" \
    "s_add_u32 s42, s42, 5120\n" \
    "s_addc_u32 s43, s43, 0\n" \
    "s_waitcnt lgkmcnt(6)\n" \
    "v_mfma_f32_32x32x16_bf16 v[112:127], v[168:171], v[28:31], v[112:127]\n" \
    "ds_read_b64_tr_b16 v[44:45], v5 offset:45056\n" \
    "ds_read_b64_tr_b16 v[46:47], v5 offset:47104\n" \
    "s_add_u32 m0, s51, 19456\n" \
    "s_nop 0\n" \
    "global_load_lds_dwordx4 v1, s[42:43]\n" \
    "s_add_u32 s42, s42, 66560\n" \
    "s_addc_u32 s43, s43, 0\n" \
    "s_waitcnt lgkmcnt(6)\n" \
    "v_mfma_f32_32x32x16_bf16 v[64:79], v[172:175], v[32:35], v[64:79]\n" \
    "ds_read_b64_tr_b16 v[16:17], v2 offset:49152\n" \
    "ds_read_b64_tr_b16 v[18:19], v2 offset:51200\n" \
    "s_waitcnt lgkmcnt(6)\n" \
    "v_mfma_f32_32x32x16_bf16 v[80:95], v[172:175], v[36:39], v[80:95]\n" \
    "ds_read_b64_tr_b16 v[20:21], v3 offset:49152\n" \
    "ds_read_b64_tr_b16 v[22:23], v3 offset:51200\n" \
    "s_waitcnt lgkmcnt(6)\n" \
    "v_mfma_f32_32x32x16_bf16 v[96:111], v[172:175], v[40:43], v[96:111]\n" \
    "ds_read_b64_tr_b16 v[24:25], v4 offset:49152\n" \
    "ds_read_b64_tr_b16 v[26:27], v4 offset:51200\n" \
    "s_waitcnt lgkmcnt(6)\n" \
    "v_mfma_f32_32x32x16_bf16 v[112:127], v[172:175], v[44:47], v[112:127]\n" \
    "ds_read_b64_tr_b16 v[28:29], v5 offset:49152\n" \
    "ds_read_b64_tr_b16 v[30:31], v5 offset:51200\n" \
    "s_waitcnt vmcnt(20)\n" \
    "s_waitcnt lgkmcnt(6)\n" \
    "v_mfma_f32_32x32x16_bf16 v[64:79], v[176:179], v[16:19], v[64:79]\n" \
    "ds_read_b64_tr_b16 v[32:33], v2 offset:53248\n" \
    "ds_read_b64_tr_b16 v[34:35], v2 offset:55296\n" \
    "global_load_dwordx4 v[160:163], v10, s[44:45] offset:0 sc1\n" \
    "global_load_dwordx4 v[164:167], v10, s[44:45] offset:1024 sc1\n" \
    "s_waitcnt lgkmcnt(6)\n" \
    "v_mfma_f32_32x32x16_bf16 v[80:95], v[176:179], v[20:23], v[80:95]\n" \
    "ds_read_b64_tr_b16 v[36:37], v3 offset:53248\n" \
    "ds_read_b64_tr_b16 v[38:39], v3 offset:55296\n" \
    "global_load_dwordx4 v[168:171], v10, s[44:45] offset:2048 sc1\n" \
    "global_load_dwordx4 v[172:175], v10, s[44:45] offset:3072 sc1\n" \
    "s_add_u32 s44, s44, 4096\n" \
    "s_addc_u32 s45, s45, 0\n" \
    "s_waitcnt lgkmcnt(6)\n" \
    "v_mfma_f32_32x32x16_bf16 v[96:111], v[176:179], v[24:27], v[96:111]\n" \
    "ds_read_b64_tr_b16 v[40:41], v4 offset:53248\n" \
    "ds_read_b64_tr_b16 v[42:43], v4 offset:55296\n" \
    "s_waitcnt lgkmcnt(6)\n" \
    "v_mfma_f32_32x32x16_bf16 v[112:127], v[176:179], v[28:31], v[112:127]\n" \
    "ds_read_b64_tr_b16 v[44:45], v5 offset:53248\n" \
    "ds_read_b64_tr_b16 v[46:47], v5 offset:55296\n" \
    "s_waitcnt lgkmcnt(6)\n" \
    "v_mfma_f32_32x32x16_bf16 v[64:79], v[180:183], v[32:35], v[64:79]\n" \
    "ds_read_b64_tr_b16 v[16:17], v2 offset:57344\n" \
    "ds_read_b64_tr_b16 v[18:19], v2 offset:59392\n" \
    "s_waitcnt lgkmcnt(6)\n" \
    "v_mfma_f32_32x32x16_bf16 v[80:95], v[180:183], v[36:39], v[80:95]\n" \
    "ds_read_b64_tr_b16 v[20:21], v3 offset:57344\n" \
    "ds_read_b64_tr_b16 v[22:23], v3 offset:59392\n" \
    "s_waitcnt lgkmcnt(6)\n" \
    "v_mfma_f32_32x32x16_bf16 v[96:111], v[180:183], v[40:43], v[96:111]\n" \
    "ds_read_b64_tr_b16 v[24:25], v4 offset:57344\n" \
    "ds_read_b64_tr_b16 v[26:27], v4 offset:59392\n" \
    "s_waitcnt lgkmcnt(6)\n" \
    "v_mfma_f32_32x32x16_bf16 v[112:127], v[180:183], v[44:47], v[112:127]\n" \
    "ds_read_b64_tr_b16 v[28:29], v5 offset:57344\n" \
    "ds_read_b64_tr_b16 v[30:31], v5 offset:59392\n" \
    "s_barrier\n" \
    "s_waitcnt lgkmcnt(6)\n" \
    "v_mfma_f32_32x32x16_bf16 v[64:79], v[184:187], v[16:19], v[64:79]\n" \
    "ds_read_b64_tr_b16 v[32:33], v2 offset:61440\n" \
    "ds_read_b64_tr_b16 v[34:35], v2 offset:63488\n" \
    "s_add_u32 m0, s51, 32768\n" \
    "s_nop 0\n" \
    "global_load_lds_dwordx4 v1, s[42:43]\n" \
    "s_add_u32 s42, s42, 5120\n" \
    "s_addc_u32 s43, s43, 0\n" \
    "s_waitcnt lgkmcnt(6)\n" \
    "v_mfma_f32_32x32x16_bf16 v[80:95], v[184:187], v[20:23], v[80:95]\n" \
    "ds_read_b64_tr_b16 v[36:37], v3 offset:61440\n" \
    "ds_read_b64_tr_b16 v[38:39], v3 offset:63488\n" \
    "s_add_u32 m0, s51, 33792\n" \
    "s_nop 0\n" \
    "global_load_lds_dwordx4 v1, s[42:43]\n" \
    "s_add_u32 s42, s42, 5120\n" \
    "s_addc_u32 s43, s43, 0\n" \
    "s_waitcnt lgkmcnt(6)\n" \
    "v_mfma_f32_32x32x16_bf16 v[96:111], v[184:187], v[24:27], v[96:111]\n" \
    "ds_read_b64_tr_b16 v[40:41], v4 offset:61440\n" \
    "ds_read_b64_tr_b16 v[42:43], v4 offset:63488\n" \
    "s_add_u32 m0, s51, 34816\n" \
    "s_nop 0\n" \
    "global_load_lds_dwordx4 v1, s[42:43]\n" \
    "s_add_u32 s42, s42, 5120\n" \
    "s_addc_u32 s43, s43, 0\n" \
    "s_waitcnt lgkmcnt(6)\n" \
    "v_mfma_f32_32x32x16_bf16 v[112:127], v[184:187], v[28:31], v[112:127]\n" \
    "ds_read_b64_tr_b16 v[44:45], v5 offset:61440\n" \
    "ds_read_b64_tr_b16 v[46:47], v5 offset:63488\n" \
    "s_add_u32 m0, s51, 35840\n" \
    "s_nop 0\n" \
    "global_load_lds_dwordx4 v1, s[42:43]\n" \
    "s_add_u32 s42, s42, 66560\n" \
    "s_addc_u32 s43, s43, 0\n" \
    "s_waitcnt lgkmcnt(6)\n" \
    "v_mfma_f32_32x32x16_bf16 v[64:79], v[188:191], v[32:35], v[64:79]\n" \
    "ds_read_b64_tr_b16 v[16:17], v6 offset:0\n" \
    "ds_read_b64_tr_b16 v[18:19], v6 offset:2048\n" \
    "s_waitcnt lgkmcnt(6)\n" \
    "v_mfma_f32_32x32x16_bf16 v[80:95], v[188:191], v[36:39], v[80:95]\n" \
    "ds_read_b64_tr_b16 v[20:21], v7 offset:0\n" \
    "ds_read_b64_tr_b16 v[22:23], v7 offset:2048\n" \
    "s_waitcnt lgkmcnt(6)\n" \
    "v_mfma_f32_32x32x16_bf16 v[96:111], v[188:191], v[40:43], v[96:111]\n" \
    "ds_read_b64_tr_b16 v[24:25], v8 offset:0\n" \
    "ds_read_b64_tr_b16 v[26:27], v8 offset:2048\n" \
    "s_waitcnt lgkmcnt(6)\n" \
    "v_mfma_f32_32x32x16_bf16 v[112:127], v[188:191], v[44:47], v[112:127]\n" \
    "ds_read_b64_tr_b16 v[28:29], v9 offset:0\n" \
    "ds_read_b64_tr_b16 v[30:31], v9 offset:2048\n" \
    "s_waitcnt vmcnt(20)\n" \
    "s_waitcnt lgkmcnt(6)\n" \
    "v_mfma_f32_32x32x16_bf16 v[64:79], v[128:131], v[16:19], v[64:79]\n" \
    "ds_read_b64_tr_b16 v[32:33], v6 offset:4096\n" \
    "ds_read_b64_tr_b16 v[34:35], v6 offset:6144\n" \
    "global_load_dwordx4 v[176:179], v10, s[44:45] offset:0 sc1\n" \
    "global_load_dwordx4 v[180:183], v10, s[44:45] offset:1024 sc1\n" \
    "s_waitcnt lgkmcnt(6)\n" \
    "v_mfma_f32_32x32x16_bf16 v[80:95], v[128:131], v[20:23], v[80:95]\n" \
    "ds_read_b64_tr_b16 v[36:37], v7 offset:4096\n" \
    "ds_read_b64_tr_b16 v[38:39], v7 offset:6144\n" \
    "global_load_dwordx4 v[184:187], v10, s[44:45] offset:2048 sc1\n" \
    "global_load_dwordx4 v[188:191], v10, s[44:45] offset:3072 sc1\n" \
    "s_add_u32 s44, s44, 4096\n" \
    "s_addc_u32 s45, s45, 0\n" \
    "s_waitcnt lgkmcnt(6)\n" \
    "v_mfma_f32_32x32x16_bf16 v[96:111], v[128:131], v[24:27], v[96:111]\n" \
    "ds_read_b64_tr_b16 v[40:41], v8 offset:4096\n" \
    "ds_read_b64_tr_b16 v[42:43], v8 offset:6144\n" \
    "s_waitcnt lgkmcnt(6)\n" \
    "v_mfma_f32_32x32x16_bf16 v[112:127], v[128:131], v[28:31], v[112:127]\n" \
    "ds_read_b64_tr_b16 v[44:45], v9 offset:4096\n" \
    "ds_read_b64_tr_b16 v[46:47], v9 offset:6144\n" \
    "s_waitcnt lgkmcnt(6)\n" \
    "v_mfma_f32_32x32x16_bf16 v[64:79], v[132:135], v[32:35], v[64:79]\n" \
    "ds_read_b64_tr_b16 v[16:17], v6 offset:8192\n" \
    "ds_read_b64_tr_b16 v[18:19], v6 offset:10240\n" \
    "s_waitcnt lgkmcnt(6)\n" \
    "v_mfma_f32_32x32x16_bf16 v[80:95], v[132:135], v[36:39], v[80:95]\n" \
    "ds_read_b64_tr_b16 v[20:21], v7 offset:8192\n" \
    "ds_read_b64_tr_b16 v[22:23], v7 offset:10240\n" \
    "s_waitcnt lgkmcnt(6)\n" \
    "v_mfma_f32_32x32x16_bf16 v[96:111], v[132:135], v[40:43], v[96:111]\n" \
    "ds_read_b64_tr_b16 v[24:25], v8 offset:8192\n" \
    "ds_read_b64_tr_b16 v[26:27], v8 offset:10240\n" \
    "s_waitcnt lgkmcnt(6)\n" \
    "v_mfma_f32_32x32x16_bf16 v[112:127], v[132:135], v[44:47], v[112:127]\n" \
    "ds_read_b64_tr_b16 v[28:29], v9 offset:8192\n" \
    "ds_read_b64_tr_b16 v[30:31], v9 offset:10240\n" \
    "s_barrier\n" \
    "s_waitcnt lgkmcnt(6)\n" \
    "v_mfma_f32_32x32x16_bf16 v[64:79], v[136:139], v[16:19], v[64:79]\n" \
    "ds_read_b64_tr_b16 v[32:33], v6 offset:12288\n" \
    "ds_read_b64_tr_b16 v[34:35], v6 offset:14336\n" \
    "s_add_u32 m0, s51, 49152\n" \
    "s_nop 0\n" \
    "global_load_lds_dwordx4 v1, s[42:43]\n" \
    "s_add_u32 s42, s42, 5120\n" \
    "s_addc_u32 s43, s43, 0\n" \
    "s_waitcnt lgkmcnt(6)\n" \
    "v_mfma_f32_32x32x16_bf16 v[80:95], v[136:139], v[20:23], v[80:95]\n" \
    "ds_read_b64_tr_b16 v[36:37], v7 offset:12288\n" \
    "ds_read_b64_tr_b16 v[38:39], v7 offset:14336\n" \
    "s_add_u32 m0, s51, 50176\n" \
    "s_nop 0\n" \
    "global_load_lds_dwordx4 v1, s[42:43]\n" \
    "s_add_u32 s42, s42, 5120\n" \
    "s_addc_u32 s43, s43, 0\n" \
    "s_waitcnt lgkmcnt(6)\n" \
    "v_mfma_f32_32x32x16_bf16 v[96:111], v[136:139], v[24:27], v[96:111]\n" \
    "ds_read_b64_tr_b16 v[40:41], v8 offset:12288\n" \
    "ds_read_b64_tr_b16 v[42:43], v8 offset:14336\n" \
    "s_add_u32 m0, s51, 51200\n" \
    "s_nop 0\n" \
    "global_load_lds_dwordx4 v1, s[42:43]\n" \
    "s_add_u32 s42, s42, 5120\n" \
    "s_addc_u32 s43, s43, 0\n" \
    "s_waitcnt lgkmcnt(6)\n" \
    "v_mfma_f32_32x32x16_bf16 v[112:127], v[136:139], v[28:31], v[112:127]\n" \
    "ds_read_b64_tr_b16 v[44:45], v9 offset:12288\n" \
    "ds_read_b64_tr_b16 v[46:47], v9 offset:14336\n" \
    "s_add_u32 m0, s51, 52224\n" \
    "s_nop 0\n" \
    "global_load_lds_dwordx4 v1, s[42:43]\n" \
    "s_add_u32 s42, s42, 66560\n" \
    "s_addc_u32 s43, s43, 0\n" \
    "s_waitcnt lgkmcnt(6)\n" \
    "v_mfma_f32_32x32x16_bf16 v[64:79], v[140:143], v[32:35], v[64:79]\n" \
    "ds_read_b64_tr_b16 v[16:17], v6 offset:16384\n" \
    "ds_read_b64_tr_b16 v[18:19], v6 offset:18432\n" \
    "s_waitcnt lgkmcnt(6)\n" \
    "v_mfma_f32_32x32x16_bf16 v[80:95], v[140:143], v[36:39], v[80:95]\n" \
    "ds_read_b64_tr_b16 v[20:21], v7 offset:16384\n" \
    "ds_read_b64_tr_b16 v[22:23], v7 offset:18432\n" \
    "s_waitcnt lgkmcnt(6)\n" \
    "v_mfma_f32_32x32x16_bf16 v[96:111], v[140:143], v[40:43], v[96:111]\n" \
    "ds_read_b64_tr_b16 v[24:25], v8 offset:16384\n" \
    "ds_read_b64_tr_b16 v[26:27], v8 offset:18432\n" \
    "s_waitcnt lgkmcnt(6)\n" \
    "v_mfma_f32_32x32x16_bf16 v[112:127], v[140:143], v[44:47], v[112:127]\n" \
    "ds_read_b64_tr_b16 v[28:29], v9 offset:16384\n" \
    "ds_read_b64_tr_b16 v[30:31], v9 offset:18432\n" \
    "s_waitcnt vmcnt(20)\n" \
    "s_waitcnt lgkmcnt(6)\n" \
    "v_mfma_f32_32x32x16_bf16 v[64:79], v[144:147], v[16:19], v[64:79]\n" \
    "ds_read_b64_tr_b16 v[32:33], v6 offset:20480\n" \
    "ds_read_b64_tr_b16 v[34:35], v6 offset:22528\n" \
    "global_load_dwordx4 v[128:131], v10, s[44:45] offset:0 sc1\n" \
    "global_load_dwordx4 v[132:135], v10, s[44:45] offset:1024 sc1\n" \
    "s_waitcnt lgkmcnt(6)\n" \
    "v_mfma_f32_32x32x16_bf16 v[80:95], v[144:147], v[20:23], v[80:95]\n" \
    "ds_read_b64_tr_b16 v[36:37], v7 offset:20480\n" \
    "ds_read_b64_tr_b16 v[38:39], v7 offset:22528\n" \
    "global_load_dwordx4 v[136:139], v10, s[44:45] offset:2048 sc1\n" \
    "global_load_dwordx4 v[140:143], v10, s[44:45] offset:3072 sc1\n" \
    "s_add_u32 s44, s44, 4096\n" \
    "s_addc_u32 s45, s45, 0\n" \
    "s_waitcnt lgkmcnt(6)\n" \
    "v_mfma_f32_32x32x16_bf16 v[96:111], v[144:147], v[24:27], v[96:111]\n" \
    "ds_read_b64_tr_b16 v[40:41], v8 offset:20480\n" \
    "ds_read_b64_tr_b16 v[42:43], v8 offset:22528\n" \
    "s_waitcnt lgkmcnt(6)\n" \
    "v_mfma_f32_32x32x16_bf16 v[112:127], v[144:147], v[28:31], v[112:127]\n" \
    "ds_read_b64_tr_b16 v[44:45], v9 offset:20480\n" \
    "ds_read_b64_tr_b16 v[46:47], v9 offset:22528\n" \
    "s_waitcnt lgkmcnt(6)\n" \
    "v_mfma_f32_32x32x16_bf16 v[64:79], v[148:151], v[32:35], v[64:79]\n" \
    "ds_read_b64_tr_b16 v[16:17], v6 offset:24576\n" \
    "ds_read_b64_tr_b16 v[18:19], v6 offset:26624\n" \
    "s_waitcnt lgkmcnt(6)\n" \
    "v_mfma_f32_32x32x16_bf16 v[80:95], v[148:151], v[36:39], v[80:95]\n" \
    "ds_read_b64_tr_b16 v[20:21], v7 offset:24576\n" \
    "ds_read_b64_tr_b16 v[22:23], v7 offset:26624\n" \
    "s_waitcnt lgkmcnt(6)\n" \
    "v_mfma_f32_32x32x16_bf16 v[96:111], v[148:151], v[40:43], v[96:111]\n" \
    "ds_read_b64_tr_b16 v[24:25], v8 offset:24576\n" \
    "ds_read_b64_tr_b16 v[26:27], v8 offset:26624\n" \
    "s_waitcnt lgkmcnt(6)\n" \
    "v_mfma_f32_32x32x16_bf16 v[112:127], v[148:151], v[44:47], v[112:127]\n" \
    "ds_read_b64_tr_b16 v[28:29], v9 offset:24576\n" \
    "ds_read_b64_tr_b16 v[30:31], v9 offset:26624\n" \
    "s_barrier\n" \
    "s_waitcnt lgkmcnt(6)\n" \
    "v_mfma_f32_32x32x16_bf16 v[64:79], v[152:155], v[16:19], v[64:79]\n" \
    "ds_read_b64_tr_b16 v[32:33], v6 offset:28672\n" \
    "ds_read_b64_tr_b16 v[34:35], v6 offset:30720\n" \
    "s_add_u32 m0, s51, 65536\n" \
    "s_nop 0\n" \
    "global_load_lds_dwordx4 v1, s[42:43]\n" \
    "s_add_u32 s42, s42, 5120\n" \
    "s_addc_u32 s43, s43, 0\n" \
    "s_waitcnt lgkmcnt(6)\n" \
    "v_mfma_f32_32x32x16_bf16 v[80:95], v[152:155], v[20:23], v[80:95]\n" \
    "ds_read_b64_tr_b16 v[36:37], v7 offset:28672\n" \
    "ds_read_b64_tr_b16 v[38:39], v7 offset:30720\n" \
    "s_add_u32 m0, s51, 66560\n" \
    "s_nop 0\n" \
    "global_load_lds_dwordx4 v1, s[42:43]\n" \
    "s_add_u32 s42, s42, 5120\n" \
    "s_addc_u32 s43, s43, 0\n" \
    "s_waitcnt lgkmcnt(6)\n" \
    "v_mfma_f32_32x32x16_bf16 v[96:111], v[152:155], v[24:27], v[96:111]\n" \
    "ds_read_b64_tr_b16 v[40:41], v8 offset:28672\n" \
    "ds_read_b64_tr_b16 v[42:43], v8 offset:30720\n" \
    "s_add_u32 m0, s51, 67584\n" \
    "s_nop 0\n" \
    "global_load_lds_dwordx4 v1, s[42:43]\n" \
    "s_add_u32 s42, s42, 5120\n" \
    "s_addc_u32 s43, s43, 0\n" \
    "s_waitcnt lgkmcnt(6)\n" \
    "v_mfma_f32_32x32x16_bf16 v[112:127], v[152:155], v[28:31], v[112:127]\n" \
    "ds_read_b64_tr_b16 v[44:45], v9 offset:28672\n" \
    "ds_read_b64_tr_b16 v[46:47], v9 offset:30720\n" \
    "s_add_u32 m0, s51, 68608\n" \
    "s_nop 0\n" \
    "global_load_lds_dwordx4 v1, s[42:43]\n" \
    "s_add_u32 s42, s42, 66560\n" \
    "s_addc_u32 s43, s43, 0\n" \
    "s_waitcnt lgkmcnt(6)\n" \
    "v_mfma_f32_32x32x16_bf16 v[64:79], v[156:159], v[32:35], v[64:79]\n" \
    "ds_read_b64_tr_b16 v[16:17], v6 offset:32768\n" \
    "ds_read_b64_tr_b16 v[18:19], v6 offset:34816\n" \
    "s_waitcnt lgkmcnt(6)\n" \
    "v_mfma_f32_32x32x16_bf16 v[80:95], v[156:159], v[36:39], v[80:95]\n" \
    "ds_read_b64_tr_b16 v[20:21], v7 offset:32768\n" \
    "ds_read_b64_tr_b16 v[22:23], v7 offset:34816\n" \
    "s_waitcnt lgkmcnt(6)\n" \
    "v_mfma_f32_32x32x16_bf16 v[96:111], v[156:159], v[40:43], v[96:111]\n" \
    "ds_read_b64_tr_b16 v[24:25], v8 offset:32768\n" \
    "ds_read_b64_tr_b16 v[26:27], v8 offset:34816\n" \
    "s_waitcnt lgkmcnt(6)\n" \
    "v_mfma_f32_32x32x16_bf16 v[112:127], v[156:159], v[44:47], v[112:127]\n" \
    "ds_read_b64_tr_b16 v[28:29], v9 offset:32768\n" \
    "ds_read_b64_tr_b16 v[30:31], v9 offset:34816\n" \
    "s_waitcnt vmcnt(20)\n" \
    "s_waitcnt lgkmcnt(6)\n" \
    "v_mfma_f32_32x32x16_bf16 v[64:79], v[160:163], v[16:19], v[64:79]\n" \
    "ds_read_b64_tr_b16 v[32:33], v6 offset:36864\n" \
    "ds_read_b64_tr_b16 v[34:35], v6 offset:38912\n" \
    "global_load_dwordx4 v[144:147], v10, s[44:45] offset:0 sc1\n" \
    "global_load_dwordx4 v[148:151], v10, s[44:45] offset:1024 sc1\n" \
    "s_waitcnt lgkmcnt(6)\n" \
    "v_mfma_f32_32x32x16_bf16 v[80:95], v[160:163], v[20:23], v[80:95]\n" \
    "ds_read_b64_tr_b16 v[36:37], v7 offset:36864\n" \
    "ds_read_b64_tr_b16 v[38:39], v7 offset:38912\n" \
    "global_load_dwordx4 v[152:155], v10, s[44:45] offset:2048 sc1\n" \
    "global_load_dwordx4 v[156:159], v10, s[44:45] offset:3072 sc1\n" \
    "s_add_u32 s44, s44, 4096\n" \
    "s_addc_u32 s45, s45, 0\n" \
    "s_waitcnt lgkmcnt(6)\n" \
    "v_mfma_f32_32x32x16_bf16 v[96:111], v[160:163], v[24:27], v[96:111]\n" \
    "ds_read_b64_tr_b16 v[40:41], v8 offset:36864\n" \
    "ds_read_b64_tr_b16 v[42:43], v8 offset:38912\n" \
    "s_waitcnt lgkmcnt(6)\n" \
    "v_mfma_f32_32x32x16_bf16 v[112:127], v[160:163], v[28:31], v[112:127]\n" \
    "ds_read_b64_tr_b16 v[44:45], v9 offset:36864\n" \
    "ds_read_b64_tr_b16 v[46:47], v9 offset:38912\n" \
    "s_waitcnt lgkmcnt(6)\n" \
    "v_mfma_f32_32x32x16_bf16 v[64:79], v[164:167], v[32:35], v[64:79]\n" \
    "ds_read_b64_tr_b16 v[16:17], v6 offset:40960\n" \
    "ds_read_b64_tr_b16 v[18:19], v6 offset:43008\n" \
    "s_waitcnt lgkmcnt(6)\n" \
    "v_mfma_f32_32x32x16_bf16 v[80:95], v[164:167], v[36:39], v[80:95]\n" \
    "ds_read_b64_tr_b16 v[20:21], v7 offset:40960\n" \
    "ds_read_b64_tr_b16 v[22:23], v7 offset:43008\n" \
    "s_waitcnt lgkmcnt(6)\n" \
    "v_mfma_f32_32x32x16_bf16 v[96:111], v[164:167], v[40:43], v[96:111]\n" \
    "ds_read_b64_tr_b16 v[24:25], v8 offset:40960\n" \
    "ds_read_b64_tr_b16 v[26:27], v8 offset:43008\n" \
    "s_waitcnt lgkmcnt(6)\n" \
    "v_mfma_f32_32x32x16_bf16 v[112:127], v[164:167], v[44:47], v[112:127]\n" \
    "ds_read_b64_tr_b16 v[28:29], v9 offset:40960\n" \
    "ds_read_b64_tr_b16 v[30:31], v9 offset:43008\n" \
    "s_barrier\n" \
    "s_waitcnt lgkmcnt(6)\n" \
    "v_mfma_f32_32x32x16_bf16 v[64:79], v[168:171], v[16:19], v[64:79]\n" \
    "ds_read_b64_tr_b16 v[32:33], v6 offset:45056\n" \
    "ds_read_b64_tr_b16 v[34:35], v6 offset:47104\n" \
    "s_add_u32 m0, s51, 81920\n" \
    "s_nop 0\n" \
    "global_load_lds_dwordx4 v1, s[42:43]\n" \
    "s_add_u32 s42, s42, 5120\n" \
    "s_addc_u32 s43, s43, 0\n" \
    "s_waitcnt lgkmcnt(6)\n" \
    "v_mfma_f32_32x32x16_bf16 v[80:95], v[168:171], v[20:23], v[80:95]\n" \
    "ds_read_b64_tr_b16 v[36:37], v7 offset:45056\n" \
    "ds_read_b64_tr_b16 v[38:39], v7 offset:47104\n" \
    "s_add_u32 m0, s51, 82944\n" \
    "s_nop 0\n" \
    "global_load_lds_dwordx4 v1, s[42:43]\n" \
    "s_add_u32 s42, s42, 5120\n" \
    "s_addc_u32 s43, s43, 0\n" \
    "s_waitcnt lgkmcnt(6)\n" \
    "v_mfma_f32_32x32x16_bf16 v[96:111], v[168:171], v[24:27], v[96:111]\n" \
    "ds_read_b64_tr_b16 v[40:41], v8 offset:45056\n" \
    "ds_read_b64_tr_b16 v[42:43], v8 offset:47104\n" \
    "s_add_u32 m0, s51, 83968\n" \
    "s_nop 0\n" \
    "global_load_lds_dwordx4 v1, s[42:43]\n" \
    "s_add_u32 s42, s42, 5120\n" \
    "s_addc_u32 s43, s43, 0\n" \
    "s_waitcnt lgkmcnt(6)\n" \
    "v_mfma_f32_32x32x16_bf16 v[112:127], v[168:171], v[28:31], v[112:127]\n" \
    "ds_read_b64_tr_b16 v[44:45], v9 offset:45056\n" \
    "ds_read_b64_tr_b16 v[46:47], v9 offset:47104\n" \
    "s_add_u32 m0, s51, 84992\n" \
    "s_nop 0\n" \
    "global_load_lds_dwordx4 v1, s[42:43]\n" \
    "s_add_u32 s42, s42, 66560\n" \
    "s_addc_u32 s43, s43, 0\n" \
    "s_waitcnt lgkmcnt(6)\n" \
    "v_mfma_f32_32x32x16_bf16 v[64:79], v[172:175], v[32:35], v[64:79]\n" \
    "ds_read_b64_tr_b16 v[16:17], v6 offset:49152\n" \
    "ds_read_b64_tr_b16 v[18:19], v6 offset:51200\n" \
    "s_waitcnt lgkmcnt(6)\n" \
    "v_mfma_f32_32x32x16_bf16 v[80:95], v[172:175], v[36:39], v[80:95]\n" \
    "ds_read_b64_tr_b16 v[20:21], v7 offset:49152\n" \
    "ds_read_b64_tr_b16 v[22:23], v7 offset:51200\n" \
    "s_waitcnt lgkmcnt(6)\n" \
    "v_mfma_f32_32x32x16_bf16 v[96:111], v[172:175], v[40:43], v[96:111]\n" \
    "ds_read_b64_tr_b16 v[24:25], v8 offset:49152\n" \
    "ds_read_b64_tr_b16 v[26:27], v8 offset:51200\n" \
    "s_waitcnt lgkmcnt(6)\n" \
    "v_mfma_f32_32x32x16_bf16 v[112:127], v[172:175], v[44:47], v[112:127]\n" \
    "ds_read_b64_tr_b16 v[28:29], v9 offset:49152\n" \
    "ds_read_b64_tr_b16 v[30:31], v9 offset:51200\n" \
    "s_waitcnt vmcnt(20)\n" \
    "s_waitcnt lgkmcnt(6)\n" \
    "v_mfma_f32_32x32x16_bf16 v[64:79], v[176:179], v[16:19], v[64:79]\n" \
    "ds_read_b64_tr_b16 v[32:33], v6 offset:53248\n" \
    "ds_read_b64_tr_b16 v[34:35], v6 offset:55296\n" \
    "global_load_dwordx4 v[160:163], v10, s[44:45] offset:0 sc1\n" \
    "global_load_dwordx4 v[164:167], v10, s[44:45] offset:1024 sc1\n" \
    "s_waitcnt lgkmcnt(6)\n" \
    "v_mfma_f32_32x32x16_bf16 v[80:95], v[176:179], v[20:23], v[80:95]\n" \
    "ds_read_b64_tr_b16 v[36:37], v7 offset:53248\n" \
    "ds_read_b64_tr_b16 v[38:39], v7 offset:55296\n" \
    "global_load_dwordx4 v[168:171], v10, s[44:45] offset:2048 sc1\n" \
    "global_load_dwordx4 v[172:175], v10, s[44:45] offset:3072 sc1\n" \
    "s_add_u32 s44, s44, 4096\n" \
    "s_addc_u32 s45, s45, 0\n" \
    "s_waitcnt lgkmcnt(6)\n" \
    "v_mfma_f32_32x32x16_bf16 v[96:111], v[176:179], v[24:27], v[96:111]\n" \
    "ds_read_b64_tr_b16 v[40:41], v8 offset:53248\n" \
    "ds_read_b64_tr_b16 v[42:43], v8 offset:55296\n" \
    "s_waitcnt lgkmcnt(6)\n" \
    "v_mfma_f32_32x32x16_bf16 v[112:127], v[176:179], v[28:31], v[112:127]\n" \
    "ds_read_b64_tr_b16 v[44:45], v9 offset:53248\n" \
    "ds_read_b64_tr_b16 v[46:47], v9 offset:55296\n" \
    "s_waitcnt lgkmcnt(6)\n" \
    "v_mfma_f32_32x32x16_bf16 v[64:79], v[180:183], v[32:35], v[64:79]\n" \
    "ds_read_b64_tr_b16 v[16:17], v6 offset:57344\n" \
    "ds_read_b64_tr_b16 v[18:19], v6 offset:59392\n" \
    "s_waitcnt lgkmcnt(6)\n" \
    "v_mfma_f32_32x32x16_bf16 v[80:95], v[180:183], v[36:39], v[80:95]\n" \
    "ds_read_b64_tr_b16 v[20:21], v7 offset:57344\n" \
    "ds_read_b64_tr_b16 v[22:23], v7 offset:59392\n" \
    "s_waitcnt lgkmcnt(6)\n" \
    "v_mfma_f32_32x32x16_bf16 v[96:111], v[180:183], v[40:43], v[96:111]\n" \
    "ds_read_b64_tr_b16 v[24:25], v8 offset:57344\n" \
    "ds_read_b64_tr_b16 v[26:27], v8 offset:59392\n" \
    "s_waitcnt lgkmcnt(6)\n" \
    "v_mfma_f32_32x32x16_bf16 v[112:127], v[180:183], v[44:47], v[112:127]\n" \
    "ds_read_b64_tr_b16 v[28:29], v9 offset:57344\n" \
    "ds_read_b64_tr_b16 v[30:31], v9 offset:59392\n" \
    "s_barrier\n" \
    "s_waitcnt lgkmcnt(6)\n" \
    "v_mfma_f32_32x32x16_bf16 v[64:79], v[184:187], v[16:19], v[64:79]\n" \
    "ds_read_b64_tr_b16 v[32:33], v6 offset:61440\n" \
    "ds_read_b64_tr_b16 v[34:35], v6 offset:63488\n" \
    "s_add_u32 m0, s51, 98304\n" \
    "s_nop 0\n" \
    "global_load_lds_dwordx4 v1, s[42:43]\n" \
    "s_add_u32 s42, s42, 5120\n" \
    "s_addc_u32 s43, s43, 0\n" \
    "s_waitcnt lgkmcnt(6)\n" \
    "v_mfma_f32_32x32x16_bf16 v[80:95], v[184:187], v[20:23], v[80:95]\n" \
    "ds_read_b64_tr_b16 v[36:37], v7 offset:61440\n" \
    "ds_read_b64_tr_b16 v[38:39], v7 offset:63488\n" \
    "s_add_u32 m0, s51, 99328\n" \
    "s_nop 0\n" \
    "global_load_lds_dwordx4 v1, s[42:43]\n" \
    "s_add_u32 s42, s42, 5120\n" \
    "s_addc_u32 s43, s43, 0\n" \
    "s_waitcnt lgkmcnt(6)\n" \
    "v_mfma_f32_32x32x16_bf16 v[96:111], v[184:187], v[24:27], v[96:111]\n" \
    "ds_read_b64_tr_b16 v[40:41], v8 offset:61440\n" \
    "ds_read_b64_tr_b16 v[42:43], v8 offset:63488\n" \
    "s_add_u32 m0, s51, 100352\n" \
    "s_nop 0\n" \
    "global_load_lds_dwordx4 v1, s[42:43]\n" \
    "s_add_u32 s42, s42, 5120\n" \
    "s_addc_u32 s43, s43, 0\n" \
    "s_waitcnt lgkmcnt(6)\n" \
    "v_mfma_f32_32x32x16_bf16 v[112:127], v[184:187], v[28:31], v[112:127]\n" \
    "ds_read_b64_tr_b16 v[44:45], v9 offset:61440\n" \
    "ds_read_b64_tr_b16 v[46:47], v9 offset:63488\n" \
    "s_add_u32 m0, s51, 101376\n" \
    "s_nop 0\n" \
    "global_load_lds_dwordx4 v1, s[42:43]\n" \
    "s_add_u32 s42, s42, 66560\n" \
    "s_addc_u32 s43, s43, 0\n" \
    "s_waitcnt lgkmcnt(6)\n" \
    "v_mfma_f32_32x32x16_bf16 v[64:79], v[188:191], v[32:35], v[64:79]\n" \
    "ds_read_b64_tr_b16 v[16:17], v2 offset:0\n" \
    "ds_read_b64_tr_b16 v[18:19], v2 offset:2048\n" \
    "s_waitcnt lgkmcnt(6)\n" \
    "v_mfma_f32_32x32x16_bf16 v[80:95], v[188:191], v[36:39], v[80:95]\n" \
    "ds_read_b64_tr_b16 v[20:21], v3 offset:0\n" \
    "ds_read_b64_tr_b16 v[22:23], v3 offset:2048\n" \
    "s_waitcnt lgkmcnt(6)\n" \
    "v_mfma_f32_32x32x16_bf16 v[96:111], v[188:191], v[40:43], v[96:111]\n" \
    "ds_read_b64_tr_b16 v[24:25], v4 offset:0\n" \
    "ds_read_b64_tr_b16 v[26:27], v4 offset:2048\n" \
    "s_waitcnt lgkmcnt(6)\n" \
    "v_mfma_f32_32x32x16_bf16 v[112:127], v[188:191], v[44:47], v[112:127]\n" \
    "ds_read_b64_tr_b16 v[28:29], v5 offset:0\n" \
    "ds_read_b64_tr_b16 v[30:31], v5 offset:2048\n" \
    "s_sub_u32 s52, s52, 4\n" \
    "s_cmp_gt_u32 s52, 4\n" \
    "s_cbranch_scc1 OLOOP%=\n" \
    "OFIN%=:\n" \
    "s_waitcnt vmcnt(20)\n" \
    "s_waitcnt lgkmcnt(6)\n" \
    "v_mfma_f32_32x32x16_bf16 v[64:79], v[128:131], v[16:19], v[64:79]\n" \
    "ds_read_b64_tr_b16 v[32:33], v2 offset:4096\n" \
    "ds_read_b64_tr_b16 v[34:35], v2 offset:6144\n" \
    "global_load_dwordx4 v[176:179], v10, s[44:45] offset:0 sc1\n" \
    "global_load_dwordx4 v[180:183], v10, s[44:45] offset:1024 sc1\n" \
    "s_waitcnt lgkmcnt(6)\n" \
    "v_mfma_f32_32x32x16_bf16 v[80:95], v[128:131], v[20:23], v[80:95]\n" \
    "ds_read_b64_tr_b16 v[36:37], v3 offset:4096\n" \
    "ds_read_b64_tr_b16 v[38:39], v3 offset:6144\n" \
    "global_load_dwordx4 v[184:187], v10, s[44:45] offset:2048 sc1\n" \
    "global_load_dwordx4 v[188:191], v10, s[44:45] offset:3072 sc1\n" \
    "s_add_u32 s44, s44, 4096\n" \
    "s_addc_u32 s45, s45, 0\n" \
    "s_waitcnt lgkmcnt(6)\n" \
    "v_mfma_f32_32x32x16_bf16 v[96:111], v[128:131], v[24:27], v[96:111]\n" \
    "ds_read_b64_tr_b16 v[40:41], v4 offset:4096\n" \
    "ds_read_b64_tr_b16 v[42:43], v4 offset:6144\n" \
    "s_waitcnt lgkmcnt(6)\n" \
    "v_mfma_f32_32x32x16_bf16 v[112:127], v[128:131], v[28:31], v[112:127]\n" \
    "ds_read_b64_tr_b16 v[44:45], v5 offset:4096\n" \
    "ds_read_b64_tr_b16 v[46:47], v5 offset:6144\n" \
    "s_waitcnt lgkmcnt(6)\n" \
    "v_mfma_f32_32x32x16_bf16 v[64:79], v[132:135], v[32:35], v[64:79]\n" \
    "ds_read_b64_tr_b16 v[16:17], v2 offset:8192\n" \
    "ds_read_b64_tr_b16 v[18:19], v2 offset:10240\n" \
    "s_waitcnt lgkmcnt(6)\n" \
    "v_mfma_f32_32x32x16_bf16 v[80:95], v[132:135], v[36:39], v[80:95]\n" \
    "ds_read_b64_tr_b16 v[20:21], v3 offset:8192\n" \
    "ds_read_b64_tr_b16 v[22:23], v3 offset:10240\n" \
    "s_waitcnt lgkmcnt(6)\n" \
    "v_mfma_f32_32x32x16_bf16 v[96:111], v[132:135], v[40:43], v[96:111]\n" \
    "ds_read_b64_tr_b16 v[24:25], v4 offset:8192\n" \
    "ds_read_b64_tr_b16 v[26:27], v4 offset:10240\n" \
    "s_waitcnt lgkmcnt(6)\n" \
    "v_mfma_f32_32x32x16_bf16 v[112:127], v[132:135], v[44:47], v[112:127]\n" \
    "ds_read_b64_tr_b16 v[28:29], v5 offset:8192\n" \
    "ds_read_b64_tr_b16 v[30:31], v5 offset:10240\n" \
    "s_barrier\n" \
    "s_waitcnt lgkmcnt(6)\n" \
    "v_mfma_f32_32x32x16_bf16 v[64:79], v[136:139], v[16:19], v[64:79]\n" \
    "ds_read_b64_tr_b16 v[32:33], v2 offset:12288\n" \
    "ds_read_b64_tr_b16 v[34:35], v2 offset:14336\n" \
    "s_add_u32 m0, s51, 114688\n" \
    "s_nop 0\n" \
    "global_load_lds_dwordx4 v1, s[42:43]\n" \
    "s_add_u32 s42, s42, 5120\n" \
    "s_addc_u32 s43, s43, 0\n" \
    "s_waitcnt lgkmcnt(6)\n" \
    "v_mfma_f32_32x32x16_bf16 v[80:95], v[136:139], v[20:23], v[80:95]\n" \
    "ds_read_b64_tr_b16 v[36:37], v3 offset:12288\n" \
    "ds_read_b64_tr_b16 v[38:39], v3 offset:14336\n" \
    "s_add_u32 m0, s51, 115712\n" \
    "s_nop 0\n" \
    "global_load_lds_dwordx4 v1, s[42:43]\n" \
    "s_add_u32 s42, s42, 5120\n" \
    "s_addc_u32 s43, s43, 0\n" \
    "s_waitcnt lgkmcnt(6)\n" \
    "v_mfma_f32_32x32x16_bf16 v[96:111], v[136:139], v[24:27], v[96:111]\n" \
    "ds_read_b64_tr_b16 v[40:41], v4 offset:12288\n" \
    "ds_read_b64_tr_b16 v[42:43], v4 offset:14336\n" \
    "s_add_u32 m0, s51, 116736\n" \
    "s_nop 0\n" \
    "global_load_lds_dwordx4 v1, s[42:43]\n" \
    "s_add_u32 s42, s42, 5120\n" \
    "s_addc_u32 s43, s43, 0\n" \
    "s_waitcnt lgkmcnt(6)\n" \
    "v_mfma_f32_32x32x16_bf16 v[112:127], v[136:139], v[28:31], v[112:127]\n" \
    "ds_read_b64_tr_b16 v[44:45], v5 offset:12288\n" \
    "ds_read_b64_tr_b16 v[46:47], v5 offset:14336\n" \
    "s_add_u32 m0, s51, 117760\n" \
    "s_nop 0\n" \
    "global_load_lds_dwordx4 v1, s[42:43]\n" \
    "s_add_u32 s42, s42, 66560\n" \
    "s_addc_u32 s43, s43, 0\n" \
    "s_waitcnt lgkmcnt(6)\n" \
    "v_mfma_f32_32x32x16_bf16 v[64:79], v[140:143], v[32:35], v[64:79]\n" \
    "ds_read_b64_tr_b16 v[16:17], v2 offset:16384\n" \
    "ds_read_b64_tr_b16 v[18:19], v2 offset:18432\n" \
    "s_waitcnt lgkmcnt(6)\n" \
    "v_mfma_f32_32x32x16_bf16 v[80:95], v[140:143], v[36:39], v[80:95]\n" \
    "ds_read_b64_tr_b16 v[20:21], v3 offset:16384\n" \
    "ds_read_b64_tr_b16 v[22:23], v3 offset:18432\n" \
    "s_waitcnt lgkmcnt(6)\n" \
    "v_mfma_f32_32x32x16_bf16 v[96:111], v[140:143], v[40:43], v[96:111]\n" \
    "ds_read_b64_tr_b16 v[24:25], v4 offset:16384\n" \
    "ds_read_b64_tr_b16 v[26:27], v4 offset:18432\n" \
    "s_waitcnt lgkmcnt(6)\n" \
    "v_mfma_f32_32x32x16_bf16 v[112:127], v[140:143], v[44:47], v[112:127]\n" \
    "ds_read_b64_tr_b16 v[28:29], v5 offset:16384\n" \
    "ds_read_b64_tr_b16 v[30:31], v5 offset:18432\n" \
    "s_waitcnt vmcnt(20)\n" \
    "s_waitcnt lgkmcnt(6)\n" \
    "v_mfma_f32_32x32x16_bf16 v[64:79], v[144:147], v[16:19], v[64:79]\n" \
    "ds_read_b64_tr_b16 v[32:33], v2 offset:20480\n" \
    "ds_read_b64_tr_b16 v[34:35], v2 offset:22528\n" \
    "global_load_dwordx4 v[128:131], v10, s[44:45] offset:0 sc1\n" \
    "global_load_dwordx4 v[132:135], v10, s[44:45] offset:1024 sc1\n" \
    "s_waitcnt lgkmcnt(6)\n" \
    "v_mfma_f32_32x32x16_bf16 v[80:95], v[144:147], v[20:23], v[80:95]\n" \
    "ds_read_b64_tr_b16 v[36:37], v3 offset:20480\n" \
    "ds_read_b64_tr_b16 v[38:39], v3 offset:22528\n" \
    "global_load_dwordx4 v[136:139], v10, s[44:45] offset:2048 sc1\n" \
    "global_load_dwordx4 v[140:143], v10, s[44:45] offset:3072 sc1\n" \
    "s_add_u32 s44, s44, 4096\n" \
    "s_addc_u32 s45, s45, 0\n" \
    "s_waitcnt lgkmcnt(6)\n" \
    "v_mfma_f32_32x32x16_bf16 v[96:111], v[144:147], v[24:27], v[96:111]\n" \
    "ds_read_b64_tr_b16 v[40:41], v4 offset:20480\n" \
    "ds_read_b64_tr_b16 v[42:43], v4 offset:22528\n" \
    "s_waitcnt lgkmcnt(6)\n" \
    "v_mfma_f32_32x32x16_bf16 v[112:127], v[144:147], v[28:31], v[112:127]\n" \
    "ds_read_b64_tr_b16 v[44:45], v5 offset:20480\n" \
    "ds_read_b64_tr_b16 v[46:47], v5 offset:22528\n" \
    "s_waitcnt lgkmcnt(6)\n" \
    "v_mfma_f32_32x32x16_bf16 v[64:79], v[148:151], v[32:35], v[64:79]\n" \
    "ds_read_b64_tr_b16 v[16:17], v2 offset:24576\n" \
    "ds_read_b64_tr_b16 v[18:19], v2 offset:26624\n" \
    "s_waitcnt lgkmcnt(6)\n" \
    "v_mfma_f32_32x32x16_bf16 v[80:95], v[148:151], v[36:39], v[80:95]\n" \
    "ds_read_b64_tr_b16 v[20:21], v3 offset:24576\n" \
    "ds_read_b64_tr_b16 v[22:23], v3 offset:26624\n" \
    "s_waitcnt lgkmcnt(6)\n" \
    "v_mfma_f32_32x32x16_bf16 v[96:111], v[148:151], v[40:43], v[96:111]\n" \
    "ds_read_b64_tr_b16 v[24:25], v4 offset:24576\n" \
    "ds_read_b64_tr_b16 v[26:27], v4 offset:26624\n" \
    "s_waitcnt lgkmcnt(6)\n" \
    "v_mfma_f32_32x32x16_bf16 v[112:127], v[148:151], v[44:47], v[112:127]\n" \
    "ds_read_b64_tr_b16 v[28:29], v5 offset:24576\n" \
    "ds_read_b64_tr_b16 v[30:31], v5 offset:26624\n" \
    "s_barrier\n" \
    "s_waitcnt lgkmcnt(6)\n" \
    "v_mfma_f32_32x32x16_bf16 v[64:79], v[152:155], v[16:19], v[64:79]\n" \
    "ds_read_b64_tr_b16 v[32:33], v2 offset:28672\n" \
    "ds_read_b64_tr_b16 v[34:35], v2 offset:30720\n" \
    "s_waitcnt lgkmcnt(6)\n" \
    "v_mfma_f32_32x32x16_bf16 v[80:95], v[152:155], v[20:23], v[80:95]\n" \
    "ds_read_b64_tr_b16 v[36:37], v3 offset:28672\n" \
    "ds_read_b64_tr_b16 v[38:39], v3 offset:30720\n" \
    "s_waitcnt lgkmcnt(6)\n" \
    "v_mfma_f32_32x32x16_bf16 v[96:111], v[152:155], v[24:27], v[96:111]\n" \
    "ds_read_b64_tr_b16 v[40:41], v4 offset:28672\n" \
    "ds_read_b64_tr_b16 v[42:43], v4 offset:30720\n" \
    "s_waitcnt lgkmcnt(6)\n" \
    "v_mfma_f32_32x32x16_bf16 v[112:127], v[152:155], v[28:31], v[112:127]\n" \
    "ds_read_b64_tr_b16 v[44:45], v5 offset:28672\n" \
    "ds_read_b64_tr_b16 v[46:47], v5 offset:30720\n" \
    "s_waitcnt lgkmcnt(6)\n" \
    "v_mfma_f32_32x32x16_bf16 v[64:79], v[156:159], v[32:35], v[64:79]\n" \
    "ds_read_b64_tr_b16 v[16:17], v2 offset:32768\n" \
    "ds_read_b64_tr_b16 v[18:19], v2 offset:34816\n" \
    "s_waitcnt lgkmcnt(6)\n" \
    "v_mfma_f32_32x32x16_bf16 v[80:95], v[156:159], v[36:39], v[80:95]\n" \
    "ds_read_b64_tr_b16 v[20:21], v3 offset:32768\n" \
    "ds_read_b64_tr_b16 v[22:23], v3 offset:34816\n" \
    "s_waitcnt lgkmcnt(6)\n" \
    "v_mfma_f32_32x32x16_bf16 v[96:111], v[156:159], v[40:43], v[96:111]\n" \
    "ds_read_b64_tr_b16 v[24:25], v4 offset:32768\n" \
    "ds_read_b64_tr_b16 v[26:27], v4 offset:34816\n" \
    "s_waitcnt lgkmcnt(6)\n" \
    "v_mfma_f32_32x32x16_bf16 v[112:127], v[156:159], v[44:47], v[112:127]\n" \
    "ds_read_b64_tr_b16 v[28:29], v5 offset:32768\n" \
    "ds_read_b64_tr_b16 v[30:31], v5 offset:34816\n" \
    "s_waitcnt vmcnt(16)\n" \
    "s_waitcnt lgkmcnt(6)\n" \
    "v_mfma_f32_32x32x16_bf16 v[64:79], v[160:163], v[16:19], v[64:79]\n" \
    "ds_read_b64_tr_b16 v[32:33], v2 offset:36864\n" \
    "ds_read_b64_tr_b16 v[34:35], v2 offset:38912\n" \
    "global_load_dwordx4 v[144:147], v10, s[44:45] offset:0 sc1\n" \
    "global_load_dwordx4 v[148:151], v10, s[44:45] offset:1024 sc1\n" \
    "s_waitcnt lgkmcnt(6)\n" \
    "v_mfma_f32_32x32x16_bf16 v[80:95], v[160:163], v[20:23], v[80:95]\n" \
    "ds_read_b64_tr_b16 v[36:37], v3 offset:36864\n" \
    "ds_read_b64_tr_b16 v[38:39], v3 offset:38912\n" \
    "global_load_dwordx4 v[152:155], v10, s[44:45] offset:2048 sc1\n" \
    "global_load_dwordx4 v[156:159], v10, s[44:45] offset:3072 sc1\n" \
    "s_add_u32 s44, s44, 4096\n" \
    "s_addc_u32 s45, s45, 0\n" \
    "s_waitcnt lgkmcnt(6)\n" \
    "v_mfma_f32_32x32x16_bf16 v[96:111], v[160:163], v[24:27], v[96:111]\n" \
    "ds_read_b64_tr_b16 v[40:41], v4 offset:36864\n" \
    "ds_read_b64_tr_b16 v[42:43], v4 offset:38912\n" \
    "s_waitcnt lgkmcnt(6)\n" \
    "v_mfma_f32_32x32x16_bf16 v[112:127], v[160:163], v[28:31], v[112:127]\n" \
    "ds_read_b64_tr_b16 v[44:45], v5 offset:36864\n" \
    "ds_read_b64_tr_b16 v[46:47], v5 offset:38912\n" \
    "s_waitcnt lgkmcnt(6)\n" \
    "v_mfma_f32_32x32x16_bf16 v[64:79], v[164:167], v[32:35], v[64:79]\n" \
    "ds_read_b64_tr_b16 v[16:17], v2 offset:40960\n" \
    "ds_read_b64_tr_b16 v[18:19], v2 offset:43008\n" \
    "s_waitcnt lgkmcnt(6)\n" \
    "v_mfma_f32_32x32x16_bf16 v[80:95], v[164:167], v[36:39], v[80:95]\n" \
    "ds_read_b64_tr_b16 v[20:21], v3 offset:40960\n" \
    "ds_read_b64_tr_b16 v[22:23], v3 offset:43008\n" \
    "s_waitcnt lgkmcnt(6)\n" \
    "v_mfma_f32_32x32x16_bf16 v[96:111], v[164:167], v[40:43], v[96:111]\n" \
    "ds_read_b64_tr_b16 v[24:25], v4 offset:40960\n" \
    "ds_read_b64_tr_b16 v[26:27], v4 offset:43008\n" \
    "s_waitcnt lgkmcnt(6)\n" \
    "v_mfma_f32_32x32x16_bf16 v[112:127], v[164:167], v[44:47], v[112:127]\n" \
    "ds_read_b64_tr_b16 v[28:29], v5 offset:40960\n" \
    "ds_read_b64_tr_b16 v[30:31], v5 offset:43008\n" \
    "s_barrier\n" \
    "s_waitcnt lgkmcnt(6)\n" \
    "v_mfma_f32_32x32x16_bf16 v[64:79], v[168:171], v[16:19], v[64:79]\n" \
    "ds_read_b64_tr_b16 v[32:33], v2 offset:45056\n" \
    "ds_read_b64_tr_b16 v[34:35], v2 offset:47104\n" \
    "s_waitcnt lgkmcnt(6)\n" \
    "v_mfma_f32_32x32x16_bf16 v[80:95], v[168:171], v[20:23], v[80:95]\n" \
    "ds_read_b64_tr_b16 v[36:37], v3 offset:45056\n" \
    "ds_read_b64_tr_b16 v[38:39], v3 offset:47104\n" \
    "s_waitcnt lgkmcnt(6)\n" \
    "v_mfma_f32_32x32x16_bf16 v[96:111], v[168:171], v[24:27], v[96:111]\n" \
    "ds_read_b64_tr_b16 v[40:41], v4 offset:45056\n" \
    "ds_read_b64_tr_b16 v[42:43], v4 offset:47104\n" \
    "s_waitcnt lgkmcnt(6)\n" \
    "v_mfma_f32_32x32x16_bf16 v[112:127], v[168:171], v[28:31], v[112:127]\n" \
    "ds_read_b64_tr_b16 v[44:45], v5 offset:45056\n" \
    "ds_read_b64_tr_b16 v[46:47], v5 offset:47104\n" \
    "s_waitcnt lgkmcnt(6)\n" \
    "v_mfma_f32_32x32x16_bf16 v[64:79], v[172:175], v[32:35], v[64:79]\n" \
    "ds_read_b64_tr_b16 v[16:17], v2 offset:49152\n" \
    "ds_read_b64_tr_b16 v[18:19], v2 offset:51200\n" \
    "s_waitcnt lgkmcnt(6)\n" \
    "v_mfma_f32_32x32x16_bf16 v[80:95], v[172:175], v[36:39], v[80:95]\n" \
    "ds_read_b64_tr_b16 v[20:21], v3 offset:49152\n" \
    "ds_read_b64_tr_b16 v[22:23], v3 offset:51200\n" \
    "s_waitcnt lgkmcnt(6)\n" \
    "v_mfma_f32_32x32x16_bf16 v[96:111], v[172:175], v[40:43], v[96:111]\n" \
    "ds_read_b64_tr_b16 v[24:25], v4 offset:49152\n" \
    "ds_read_b64_tr_b16 v[26:27], v4 offset:51200\n" \
    "s_waitcnt lgkmcnt(6)\n" \
    "v_mfma_f32_32x32x16_bf16 v[112:127], v[172:175], v[44:47], v[112:127]\n" \
    "ds_read_b64_tr_b16 v[28:29], v5 offset:49152\n" \
    "ds_read_b64_tr_b16 v[30:31], v5 offset:51200\n" \
    "s_waitcnt vmcnt(12)\n" \
    "s_waitcnt lgkmcnt(6)\n" \
    "v_mfma_f32_32x32x16_bf16 v[64:79], v[176:179], v[16:19], v[64:79]\n" \
    "ds_read_b64_tr_b16 v[32:33], v2 offset:53248\n" \
    "ds_read_b64_tr_b16 v[34:35], v2 offset:55296\n" \
    "global_load_dwordx4 v[160:163], v10, s[44:45] offset:0 sc1\n" \
    "global_load_dwordx4 v[164:167], v10, s[44:45] offset:1024 sc1\n" \
    "s_waitcnt lgkmcnt(6)\n" \
    "v_mfma_f32_32x32x16_bf16 v[80:95], v[176:179], v[20:23], v[80:95]\n" \
    "ds_read_b64_tr_b16 v[36:37], v3 offset:53248\n" \
    "ds_read_b64_tr_b16 v[38:39], v3 offset:55296\n" \
    "global_load_dwordx4 v[168:171], v10, s[44:45] offset:2048 sc1\n" \
    "global_load_dwordx4 v[172:175], v10, s[44:45] offset:3072 sc1\n" \
    "s_add_u32 s44, s44, 4096\n" \
    "s_addc_u32 s45, s45, 0\n" \
    "s_waitcnt lgkmcnt(6)\n" \
    "v_mfma_f32_32x32x16_bf16 v[96:111], v[176:179], v[24:27], v[96:111]\n" \
    "ds_read_b64_tr_b16 v[40:41], v4 offset:53248\n" \
    "ds_read_b64_tr_b16 v[42:43], v4 offset:55296\n" \
    "s_waitcnt lgkmcnt(6)\n" \
    "v_mfma_f32_32x32x16_bf16 v[112:127], v[176:179], v[28:31], v[112:127]\n" \
    "ds_read_b64_tr_b16 v[44:45], v5 offset:53248\n" \
    "ds_read_b64_tr_b16 v[46:47], v5 offset:55296\n" \
    "s_waitcnt lgkmcnt(6)\n" \
    "v_mfma_f32_32x32x16_bf16 v[64:79], v[180:183], v[32:35], v[64:79]\n" \
    "ds_read_b64_tr_b16 v[16:17], v2 offset:57344\n" \
    "ds_read_b64_tr_b16 v[18:19], v2 offset:59392\n" \
    "s_waitcnt lgkmcnt(6)\n" \
    "v_mfma_f32_32x32x16_bf16 v[80:95], v[180:183], v[36:39], v[80:95]\n" \
    "ds_read_b64_tr_b16 v[20:21], v3 offset:57344\n" \
    "ds_read_b64_tr_b16 v[22:23], v3 offset:59392\n" \
    "s_waitcnt lgkmcnt(6)\n" \
    "v_mfma_f32_32x32x16_bf16 v[96:111], v[180:183], v[40:43], v[96:111]\n" \
    "ds_read_b64_tr_b16 v[24:25], v4 offset:57344\n" \
    "ds_read_b64_tr_b16 v[26:27], v4 offset:59392\n" \
    "s_waitcnt lgkmcnt(6)\n" \
    "v_mfma_f32_32x32x16_bf16 v[112:127], v[180:183], v[44:47], v[112:127]\n" \
    "ds_read_b64_tr_b16 v[28:29], v5 offset:57344\n" \
    "ds_read_b64_tr_b16 v[30:31], v5 offset:59392\n" \
    "s_barrier\n" \
    "s_waitcnt lgkmcnt(6)\n" \
    "v_mfma_f32_32x32x16_bf16 v[64:79], v[184:187], v[16:19], v[64:79]\n" \
    "ds_read_b64_tr_b16 v[32:33], v2 offset:61440\n" \
    "ds_read_b64_tr_b16 v[34:35], v2 offset:63488\n" \
    "s_waitcnt lgkmcnt(6)\n" \
    "v_mfma_f32_32x32x16_bf16 v[80:95], v[184:187], v[20:23], v[80:95]\n" \
    "ds_read_b64_tr_b16 v[36:37], v3 offset:61440\n" \
    "ds_read_b64_tr_b16 v[38:39], v3 offset:63488\n" \
    "s_waitcnt lgkmcnt(6)\n" \
    "v_mfma_f32_32x32x16_bf16 v[96:111], v[184:187], v[24:27], v[96:111]\n" \
    "ds_read_b64_tr_b16 v[40:41], v4 offset:61440\n" \
    "ds_read_b64_tr_b16 v[42:43], v4 offset:63488\n" \
    "s_waitcnt lgkmcnt(6)\n" \
    "v_mfma_f32_32x32x16_bf16 v[112:127], v[184:187], v[28:31], v[112:127]\n" \
    "ds_read_b64_tr_b16 v[44:45], v5 offset:61440\n" \
    "ds_read_b64_tr_b16 v[46:47], v5 offset:63488\n" \
    "s_waitcnt lgkmcnt(6)\n" \
    "v_mfma_f32_32x32x16_bf16 v[64:79], v[188:191], v[32:35], v[64:79]\n" \
    "ds_read_b64_tr_b16 v[16:17], v6 offset:0\n" \
    "ds_read_b64_tr_b16 v[18:19], v6 offset:2048\n" \
    "s_waitcnt lgkmcnt(6)\n" \
    "v_mfma_f32_32x32x16_bf16 v[80:95], v[188:191], v[36:39], v[80:95]\n" \
    "ds_read_b64_tr_b16 v[20:21], v7 offset:0\n" \
    "ds_read_b64_tr_b16 v[22:23], v7 offset:2048\n" \
    "s_waitcnt lgkmcnt(6)\n" \
    "v_mfma_f32_32x32x16_bf16 v[96:111], v[188:191], v[40:43], v[96:111]\n" \
    "ds_read_b64_tr_b16 v[24:25], v8 offset:0\n" \
    "ds_read_b64_tr_b16 v[26:27], v8 offset:2048\n" \
    "s_waitcnt lgkmcnt(6)\n" \
    "v_mfma_f32_32x32x16_bf16 v[112:127], v[188:191], v[44:47], v[112:127]\n" \
    "ds_read_b64_tr_b16 v[28:29], v9 offset:0\n" \
    "ds_read_b64_tr_b16 v[30:31], v9 offset:2048\n" \
    "s_waitcnt vmcnt(8)\n" \
    "s_waitcnt lgkmcnt(6)\n" \
    "v_mfma_f32_32x32x16_bf16 v[64:79], v[128:131], v[16:19], v[64:79]\n" \
    "ds_read_b64_tr_b16 v[32:33], v6 offset:4096\n" \
    "ds_read_b64_tr_b16 v[34:35], v6 offset:6144\n" \
    "global_load_dwordx4 v[176:179], v10, s[44:45] offset:0 sc1\n" \
    "global_load_dwordx4 v[180:183], v10, s[44:45] offset:1024 sc1\n" \
    "s_waitcnt lgkmcnt(6)\n" \
    "v_mfma_f32_32x32x16_bf16 v[80:95], v[128:131], v[20:23], v[80:95]\n" \
    "ds_read_b64_tr_b16 v[36:37], v7 offset:4096\n" \
    "ds_read_b64_tr_b16 v[38:39], v7 offset:6144\n" \
    "global_load_dwordx4 v[184:187], v10, s[44:45] offset:2048 sc1\n" \
    "global_load_dwordx4 v[188:191], v10, s[44:45] offset:3072 sc1\n" \
    "s_add_u32 s44, s44, 4096\n" \
    "s_addc_u32 s45, s45, 0\n" \
    "s_waitcnt lgkmcnt(6)\n" \
    "v_mfma_f32_32x32x16_bf16 v[96:111], v[128:131], v[24:27], v[96:111]\n" \
    "ds_read_b64_tr_b16 v[40:41], v8 offset:4096\n" \
    "ds_read_b64_tr_b16 v[42:43], v8 offset:6144\n" \
    "s_waitcnt lgkmcnt(6)\n" \
    "v_mfma_f32_32x32x16_bf16 v[112:127], v[128:131], v[28:31], v[112:127]\n" \
    "ds_read_b64_tr_b16 v[44:45], v9 offset:4096\n" \
    "ds_read_b64_tr_b16 v[46:47], v9 offset:6144\n" \
    "s_waitcnt lgkmcnt(6)\n" \
    "v_mfma_f32_32x32x16_bf16 v[64:79], v[132:135], v[32:35], v[64:79]\n" \
    "ds_read_b64_tr_b16 v[16:17], v6 offset:8192\n" \
    "ds_read_b64_tr_b16 v[18:19], v6 offset:10240\n" \
    "s_waitcnt lgkmcnt(6)\n" \
    "v_mfma_f32_32x32x16_bf16 v[80:95], v[132:135], v[36:39], v[80:95]\n" \
    "ds_read_b64_tr_b16 v[20:21], v7 offset:8192\n" \
    "ds_read_b64_tr_b16 v[22:23], v7 offset:10240\n" \
    "s_waitcnt lgkmcnt(6)\n" \
    "v_mfma_f32_32x32x16_bf16 v[96:111], v[132:135], v[40:43], v[96:111]\n" \
    "ds_read_b64_tr_b16 v[24:25], v8 offset:8192\n" \
    "ds_read_b64_tr_b16 v[26:27], v8 offset:10240\n" \
    "s_waitcnt lgkmcnt(6)\n" \
    "v_mfma_f32_32x32x16_bf16 v[112:127], v[132:135], v[44:47], v[112:127]\n" \
    "ds_read_b64_tr_b16 v[28:29], v9 offset:8192\n" \
    "ds_read_b64_tr_b16 v[30:31], v9 offset:10240\n" \
    "s_barrier\n" \
    "s_waitcnt lgkmcnt(6)\n" \
    "v_mfma_f32_32x32x16_bf16 v[64:79], v[136:139], v[16:19], v[64:79]\n" \
    "ds_read_b64_tr_b16 v[32:33], v6 offset:12288\n" \
    "ds_read_b64_tr_b16 v[34:35], v6 offset:14336\n" \
    "s_waitcnt lgkmcnt(6)\n" \
    "v_mfma_f32_32x32x16_bf16 v[80:95], v[136:139], v[20:23], v[80:95]\n" \
    "ds_read_b64_tr_b16 v[36:37], v7 offset:12288\n" \
    "ds_read_b64_tr_b16 v[38:39], v7 offset:14336\n" \
    "s_waitcnt lgkmcnt(6)\n" \
    "v_mfma_f32_32x32x16_bf16 v[96:111], v[136:139], v[24:27], v[96:111]\n" \
    "ds_read_b64_tr_b16 v[40:41], v8 offset:12288\n" \
    "ds_read_b64_tr_b16 v[42:43], v8 offset:14336\n" \
    "s_waitcnt lgkmcnt(6)\n" \
    "v_mfma_f32_32x32x16_bf16 v[112:127], v[136:139], v[28:31], v[112:127]\n" \
    "ds_read_b64_tr_b16 v[44:45], v9 offset:12288\n" \
    "ds_read_b64_tr_b16 v[46:47], v9 offset:14336\n" \
    "s_waitcnt lgkmcnt(6)\n" \
    "v_mfma_f32_32x32x16_bf16 v[64:79], v[140:143], v[32:35], v[64:79]\n" \
    "ds_read_b64_tr_b16 v[16:17], v6 offset:16384\n" \
    "ds_read_b64_tr_b16 v[18:19], v6 offset:18432\n" \
    "s_waitcnt lgkmcnt(6)\n" \
    "v_mfma_f32_32x32x16_bf16 v[80:95], v[140:143], v[36:39], v[80:95]\n" \
    "ds_read_b64_tr_b16 v[20:21], v7 offset:16384\n" \
    "ds_read_b64_tr_b16 v[22:23], v7 offset:18432\n" \
    "s_waitcnt lgkmcnt(6)\n" \
    "v_mfma_f32_32x32x16_bf16 v[96:111], v[140:143], v[40:43], v[96:111]\n" \
    "ds_read_b64_tr_b16 v[24:25], v8 offset:16384\n" \
    "ds_read_b64_tr_b16 v[26:27], v8 offset:18432\n" \
    "s_waitcnt lgkmcnt(6)\n" \
    "v_mfma_f32_32x32x16_bf16 v[112:127], v[140:143], v[44:47], v[112:127]\n" \
    "ds_read_b64_tr_b16 v[28:29], v9 offset:16384\n" \
    "ds_read_b64_tr_b16 v[30:31], v9 offset:18432\n" \
    "s_waitcnt vmcnt(8)\n" \
    "s_waitcnt lgkmcnt(6)\n" \
    "v_mfma_f32_32x32x16_bf16 v[64:79], v[144:147], v[16:19], v[64:79]\n" \
    "ds_read_b64_tr_b16 v[32:33], v6 offset:20480\n" \
    "ds_read_b64_tr_b16 v[34:35], v6 offset:22528\n" \
    "s_waitcnt lgkmcnt(6)\n" \
    "v_mfma_f32_32x32x16_bf16 v[80:95], v[144:147], v[20:23], v[80:95]\n" \
    "ds_read_b64_tr_b16 v[36:37], v7 offset:20480\n" \
    "ds_read_b64_tr_b16 v[38:39], v7 offset:22528\n" \
    "s_waitcnt lgkmcnt(6)\n" \
    "v_mfma_f32_32x32x16_bf16 v[96:111], v[144:147], v[24:27], v[96:111]\n" \
    "ds_read_b64_tr_b16 v[40:41], v8 offset:20480\n" \
    "ds_read_b64_tr_b16 v[42:43], v8 offset:22528\n" \
    "s_waitcnt lgkmcnt(6)\n" \
    "v_mfma_f32_32x32x16_bf16 v[112:127], v[144:147], v[28:31], v[112:127]\n" \
    "ds_read_b64_tr_b16 v[44:45], v9 offset:20480\n" \
    "ds_read_b64_tr_b16 v[46:47], v9 offset:22528\n" \
    "s_waitcnt lgkmcnt(6)\n" \
    "v_mfma_f32_32x32x16_bf16 v[64:79], v[148:151], v[32:35], v[64:79]\n" \
    "ds_read_b64_tr_b16 v[16:17], v6 offset:24576\n" \
    "ds_read_b64_tr_b16 v[18:19], v6 offset:26624\n" \
    "s_waitcnt lgkmcnt(6)\n" \
    "v_mfma_f32_32x32x16_bf16 v[80:95], v[148:151], v[36:39], v[80:95]\n" \
    "ds_read_b64_tr_b16 v[20:21], v7 offset:24576\n" \
    "ds_read_b64_tr_b16 v[22:23], v7 offset:26624\n" \
    "s_waitcnt lgkmcnt(6)\n" \
    "v_mfma_f32_32x32x16_bf16 v[96:111], v[148:151], v[40:43], v[96:111]\n" \
    "ds_read_b64_tr_b16 v[24:25], v8 offset:24576\n" \
    "ds_read_b64_tr_b16 v[26:27], v8 offset:26624\n" \
    "s_waitcnt lgkmcnt(6)\n" \
    "v_mfma_f32_32x32x16_bf16 v[112:127], v[148:151], v[44:47], v[112:127]\n" \
    "ds_read_b64_tr_b16 v[28:29], v9 offset:24576\n" \
    "ds_read_b64_tr_b16 v[30:31], v9 offset:26624\n" \
    "s_barrier\n" \
    "s_waitcnt lgkmcnt(6)\n" \
    "v_mfma_f32_32x32x16_bf16 v[64:79], v[152:155], v[16:19], v[64:79]\n" \
    "ds_read_b64_tr_b16 v[32:33], v6 offset:28672\n" \
    "ds_read_b64_tr_b16 v[34:35], v6 offset:30720\n" \
    "s_waitcnt lgkmcnt(6)\n" \
    "v_mfma_f32_32x32x16_bf16 v[80:95], v[152:155], v[20:23], v[80:95]\n" \
    "ds_read_b64_tr_b16 v[36:37], v7 offset:28672\n" \
    "ds_read_b64_tr_b16 v[38:39], v7 offset:30720\n" \
    "s_waitcnt lgkmcnt(6)\n" \
    "v_mfma_f32_32x32x16_bf16 v[96:111], v[152:155], v[24:27], v[96:111]\n" \
    "ds_read_b64_tr_b16 v[40:41], v8 offset:28672\n" \
    "ds_read_b64_tr_b16 v[42:43], v8 offset:30720\n" \
    "s_waitcnt lgkmcnt(6)\n" \
    "v_mfma_f32_32x32x16_bf16 v[112:127], v[152:155], v[28:31], v[112:127]\n" \
    "ds_read_b64_tr_b16 v[44:45], v9 offset:28672\n" \
    "ds_read_b64_tr_b16 v[46:47], v9 offset:30720\n" \
    "s_waitcnt lgkmcnt(6)\n" \
    "v_mfma_f32_32x32x16_bf16 v[64:79], v[156:159], v[32:35], v[64:79]\n" \
    "ds_read_b64_tr_b16 v[16:17], v6 offset:32768\n" \
    "ds_read_b64_tr_b16 v[18:19], v6 offset:34816\n" \
    "s_waitcnt lgkmcnt(6)\n" \
    "v_mfma_f32_32x32x16_bf16 v[80:95], v[156:159], v[36:39], v[80:95]\n" \
    "ds_read_b64_tr_b16 v[20:21], v7 offset:32768\n" \
    "ds_read_b64_tr_b16 v[22:23], v7 offset:34816\n" \
    "s_waitcnt lgkmcnt(6)\n" \
    "v_mfma_f32_32x32x16_bf16 v[96:111], v[156:159], v[40:43], v[96:111]\n" \
    "ds_read_b64_tr_b16 v[24:25], v8 offset:32768\n" \
    "ds_read_b64_tr_b16 v[26:27], v8 offset:34816\n" \
    "s_waitcnt lgkmcnt(6)\n" \
    "v_mfma_f32_32x32x16_bf16 v[112:127], v[156:159], v[44:47], v[112:127]\n" \
    "ds_read_b64_tr_b16 v[28:29], v9 offset:32768\n" \
    "ds_read_b64_tr_b16 v[30:31], v9 offset:34816\n" \
    "s_waitcnt vmcnt(4)\n" \
    "s_waitcnt lgkmcnt(6)\n" \
    "v_mfma_f32_32x32x16_bf16 v[64:79], v[160:163], v[16:19], v[64:79]\n" \
    "ds_read_b64_tr_b16 v[32:33], v6 offset:36864\n" \
    "ds_read_b64_tr_b16 v[34:35], v6 offset:38912\n" \
    "s_waitcnt lgkmcnt(6)\n" \
    "v_mfma_f32_32x32x16_bf16 v[80:95], v[160:163], v[20:23], v[80:95]\n" \
    "ds_read_b64_tr_b16 v[36:37], v7 offset:36864\n" \
    "ds_read_b64_tr_b16 v[38:39], v7 offset:38912\n" \
    "s_waitcnt lgkmcnt(6)\n" \
    "v_mfma_f32_32x32x16_bf16 v[96:111], v[160:163], v[24:27], v[96:111]\n" \
    "ds_read_b64_tr_b16 v[40:41], v8 offset:36864\n" \
    "ds_read_b64_tr_b16 v[42:43], v8 offset:38912\n" \
    "s_waitcnt lgkmcnt(6)\n" \
    "v_mfma_f32_32x32x16_bf16 v[112:127], v[160:163], v[28:31], v[112:127]\n" \
    "ds_read_b64_tr_b16 v[44:45], v9 offset:36864\n" \
    "ds_read_b64_tr_b16 v[46:47], v9 offset:38912\n" \
    "s_waitcnt lgkmcnt(6)\n" \
    "v_mfma_f32_32x32x16_bf16 v[64:79], v[164:167], v[32:35], v[64:79]\n" \
    "ds_read_b64_tr_b16 v[16:17], v6 offset:40960\n" \
    "ds_read_b64_tr_b16 v[18:19], v6 offset:43008\n" \
    "s_waitcnt lgkmcnt(6)\n" \
    "v_mfma_f32_32x32x16_bf16 v[80:95], v[164:167], v[36:39], v[80:95]\n" \
    "ds_read_b64_tr_b16 v[20:21], v7 offset:40960\n" \
    "ds_read_b64_tr_b16 v[22:23], v7 offset:43008\n" \
    "s_waitcnt lgkmcnt(6)\n" \
    "v_mfma_f32_32x32x16_bf16 v[96:111], v[164:167], v[40:43], v[96:111]\n" \
    "ds_read_b64_tr_b16 v[24:25], v8 offset:40960\n" \
    "ds_read_b64_tr_b16 v[26:27], v8 offset:43008\n" \
    "s_waitcnt lgkmcnt(6)\n" \
    "v_mfma_f32_32x32x16_bf16 v[112:127], v[164:167], v[44:47], v[112:127]\n" \
    "ds_read_b64_tr_b16 v[28:29], v9 offset:40960\n" \
    "ds_read_b64_tr_b16 v[30:31], v9 offset:43008\n" \
    "s_barrier\n" \
    "s_waitcnt lgkmcnt(6)\n" \
    "v_mfma_f32_32x32x16_bf16 v[64:79], v[168:171], v[16:19], v[64:79]\n" \
    "ds_read_b64_tr_b16 v[32:33], v6 offset:45056\n" \
    "ds_read_b64_tr_b16 v[34:35], v6 offset:47104\n" \
    "s_waitcnt lgkmcnt(6)\n" \
    "v_mfma_f32_32x32x16_bf16 v[80:95], v[168:171], v[20:23], v[80:95]\n" \
    "ds_read_b64_tr_b16 v[36:37], v7 offset:45056\n" \
    "ds_read_b64_tr_b16 v[38:39], v7 offset:47104\n" \
    "s_waitcnt lgkmcnt(6)\n" \
    "v_mfma_f32_32x32x16_bf16 v[96:111], v[168:171], v[24:27], v[96:111]\n" \
    "ds_read_b64_tr_b16 v[40:41], v8 offset:45056\n" \
    "ds_read_b64_tr_b16 v[42:43], v8 offset:47104\n" \
    "s_waitcnt lgkmcnt(6)\n" \
    "v_mfma_f32_32x32x16_bf16 v[112:127], v[168:171], v[28:31], v[112:127]\n" \
    "ds_read_b64_tr_b16 v[44:45], v9 offset:45056\n" \
    "ds_read_b64_tr_b16 v[46:47], v9 offset:47104\n" \
    "s_waitcnt lgkmcnt(6)\n" \
    "v_mfma_f32_32x32x16_bf16 v[64:79], v[172:175], v[32:35], v[64:79]\n" \
    "ds_read_b64_tr_b16 v[16:17], v6 offset:49152\n" \
    "ds_read_b64_tr_b16 v[18:19], v6 offset:51200\n" \
    "s_waitcnt lgkmcnt(6)\n" \
    "v_mfma_f32_32x32x16_bf16 v[80:95], v[172:175], v[36:39], v[80:95]\n" \
    "ds_read_b64_tr_b16 v[20:21], v7 offset:49152\n" \
    "ds_read_b64_tr_b16 v[22:23], v7 offset:51200\n" \
    "s_waitcnt lgkmcnt(6)\n" \
    "v_mfma_f32_32x32x16_bf16 v[96:111], v[172:175], v[40:43], v[96:111]\n" \
    "ds_read_b64_tr_b16 v[24:25], v8 offset:49152\n" \
    "ds_read_b64_tr_b16 v[26:27], v8 offset:51200\n" \
    "s_waitcnt lgkmcnt(6)\n" \
    "v_mfma_f32_32x32x16_bf16 v[112:127], v[172:175], v[44:47], v[112:127]\n" \
    "ds_read_b64_tr_b16 v[28:29], v9 offset:49152\n" \
    "ds_read_b64_tr_b16 v[30:31], v9 offset:51200\n" \
    "s_waitcnt vmcnt(0)\n" \
    "s_waitcnt lgkmcnt(6)\n" \
    "v_mfma_f32_32x32x16_bf16 v[64:79], v[176:179], v[16:19], v[64:79]\n" \
    "ds_read_b64_tr_b16 v[32:33], v6 offset:53248\n" \
    "ds_read_b64_tr_b16 v[34:35], v6 offset:55296\n" \
    "s_waitcnt lgkmcnt(6)\n" \
    "v_mfma_f32_32x32x16_bf16 v[80:95], v[176:179], v[20:23], v[80:95]\n" \
    "ds_read_b64_tr_b16 v[36:37], v7 offset:53248\n" \
    "ds_read_b64_tr_b16 v[38:39], v7 offset:55296\n" \
    "s_waitcnt lgkmcnt(6)\n" \
    "v_mfma_f32_32x32x16_bf16 v[96:111], v[176:179], v[24:27], v[96:111]\n" \
    "ds_read_b64_tr_b16 v[40:41], v8 offset:53248\n" \
    "ds_read_b64_tr_b16 v[42:43], v8 offset:55296\n" \
    "s_waitcnt lgkmcnt(6)\n" \
    "v_mfma_f32_32x32x16_bf16 v[112:127], v[176:179], v[28:31], v[112:127]\n" \
    "ds_read_b64_tr_b16 v[44:45], v9 offset:53248\n" \
    "ds_read_b64_tr_b16 v[46:47], v9 offset:55296\n" \
    "s_waitcnt lgkmcnt(6)\n" \
    "v_mfma_f32_32x32x16_bf16 v[64:79], v[180:183], v[32:35], v[64:79]\n" \
    "ds_read_b64_tr_b16 v[16:17], v6 offset:57344\n" \
    "ds_read_b64_tr_b16 v[18:19], v6 offset:59392\n" \
    "s_waitcnt lgkmcnt(6)\n" \
    "v_mfma_f32_32x32x16_bf16 v[80:95], v[180:183], v[36:39], v[80:95]\n" \
    "ds_read_b64_tr_b16 v[20:21], v7 offset:57344\n" \
    "ds_read_b64_tr_b16 v[22:23], v7 offset:59392\n" \
    "s_waitcnt lgkmcnt(6)\n" \
    "v_mfma_f32_32x32x16_bf16 v[96:111], v[180:183], v[40:43], v[96:111]\n" \
    "ds_read_b64_tr_b16 v[24:25], v8 offset:57344\n" \
    "ds_read_b64_tr_b16 v[26:27], v8 offset:59392\n" \
    "s_waitcnt lgkmcnt(6)\n" \
    "v_mfma_f32_32x32x16_bf16 v[112:127], v[180:183], v[44:47], v[112:127]\n" \
    "ds_read_b64_tr_b16 v[28:29], v9 offset:57344\n" \
    "ds_read_b64_tr_b16 v[30:31], v9 offset:59392\n" \
    "s_waitcnt lgkmcnt(6)\n" \
    "v_mfma_f32_32x32x16_bf16 v[64:79], v[184:187], v[16:19], v[64:79]\n" \
    "ds_read_b64_tr_b16 v[32:33], v6 offset:61440\n" \
    "ds_read_b64_tr_b16 v[34:35], v6 offset:63488\n" \
    "s_waitcnt lgkmcnt(6)\n" \
    "v_mfma_f32_32x32x16_bf16 v[80:95], v[184:187], v[20:23], v[80:95]\n" \
    "ds_read_b64_tr_b16 v[36:37], v7 offset:61440\n" \
    "ds_read_b64_tr_b16 v[38:39], v7 offset:63488\n" \
    "s_waitcnt lgkmcnt(6)\n" \
    "v_mfma_f32_32x32x16_bf16 v[96:111], v[184:187], v[24:27], v[96:111]\n" \
    "ds_read_b64_tr_b16 v[40:41], v8 offset:61440\n" \
    "ds_read_b64_tr_b16 v[42:43], v8 offset:63488\n" \
    "s_waitcnt lgkmcnt(6)\n" \
    "v_mfma_f32_32x32x16_bf16 v[112:127], v[184:187], v[28:31], v[112:127]\n" \
    "ds_read_b64_tr_b16 v[44:45], v9 offset:61440\n" \
    "ds_read_b64_tr_b16 v[46:47], v9 offset:63488\n" \
    "s_waitcnt lgkmcnt(6)\n" \
    "v_mfma_f32_32x32x16_bf16 v[64:79], v[188:191], v[32:35], v[64:79]\n" \
    "s_waitcnt lgkmcnt(4)\n" \
    "v_mfma_f32_32x32x16_bf16 v[80:95], v[188:191], v[36:39], v[80:95]\n" \
    "s_waitcnt lgkmcnt(2)\n" \
    "v_mfma_f32_32x32x16_bf16 v[96:111], v[188:191], v[40:43], v[96:111]\n" \
    "s_waitcnt lgkmcnt(0)\n" \
    "v_mfma_f32_32x32x16_bf16 v[112:127], v[188:191], v[44:47], v[112:127]\n" \
    "s_mov_b64 s[54:55], %2\n" \
    "v_lshrrev_b32 v14, 5, v0\n" \
    "v_mul_u32_u24 v14, 10240, v14\n" \
    "v_and_b32 v15, 31, v0\n" \
    "v_lshl_add_u32 v14, v15, 2, v14\n" \
    "v_add_u32 v15, 5120, v14\n" \
    "s_nop 5\n" \
    "global_store_dword v14, v64, s[54:55] offset:0 sc1\n" \
    "global_store_dword v14, v80, s[54:55] offset:128 sc1\n" \
    "global_store_dword v14, v96, s[54:55] offset:256 sc1\n" \
    "global_store_dword v14, v112, s[54:55] offset:384 sc1\n" \
    "global_store_dword v14, v65, s[54:55] offset:2560 sc1\n" \
    "global_store_dword v14, v81, s[54:55] offset:2688 sc1\n" \
    "global_store_dword v14, v97, s[54:55] offset:2816 sc1\n" \
    "global_store_dword v14, v113, s[54:55] offset:2944 sc1\n" \
    "global_store_dword v15, v66, s[54:55] offset:0 sc1\n" \
    "global_store_dword v15, v82, s[54:55] offset:128 sc1\n" \
    "global_store_dword v15, v98, s[54:55] offset:256 sc1\n" \
    "global_store_dword v15, v114, s[54:55] offset:384 sc1\n" \
    "global_store_dword v15, v67, s[54:55] offset:2560 sc1\n" \
    "global_store_dword v15, v83, s[54:55] offset:2688 sc1\n" \
    "global_store_dword v15, v99, s[54:55] offset:2816 sc1\n" \
    "global_store_dword v15, v115, s[54:55] offset:2944 sc1\n" \
    "s_add_u32 s54, s54, 20480\n" \
    "s_addc_u32 s55, s55, 0\n" \
    "global_store_dword v14, v68, s[54:55] offset:0 sc1\n" \
    "global_store_dword v14, v84, s[54:55] offset:128 sc1\n" \
    "global_store_dword v14, v100, s[54:55] offset:256 sc1\n" \
    "global_store_dword v14, v116, s[54:55] offset:384 sc1\n" \
    "global_store_dword v14, v69, s[54:55] offset:2560 sc1\n" \
    "global_store_dword v14, v85, s[54:55] offset:2688 sc1\n" \
    "global_store_dword v14, v101, s[54:55] offset:2816 sc1\n" \
    "global_store_dword v14, v117, s[54:55] offset:2944 sc1\n" \
    "global_store_dword v15, v70, s[54:55] offset:0 sc1\n" \
    "global_store_dword v15, v86, s[54:55] offset:128 sc1\n" \
    "global_store_dword v15, v102, s[54:55] offset:256 sc1\n" \
    "global_store_dword v15, v118, s[54:55] offset:384 sc1\n" \
    "global_store_dword v15, v71, s[54:55] offset:2560 sc1\n" \
    "global_store_dword v15, v87, s[54:55] offset:2688 sc1\n" \
    "global_store_dword v15, v103, s[54:55] offset:2816 sc1\n" \
    "global_store_dword v15, v119, s[54:55] offset:2944 sc1\n" \
    "s_add_u32 s54, s54, 20480\n" \
    "s_addc_u32 s55, s55, 0\n" \
    "global_store_dword v14, v72, s[54:55] offset:0 sc1\n" \
    "global_store_dword v14, v88, s[54:55] offset:128 sc1\n" \
    "global_store_dword v14, v104, s[54:55] offset:256 sc1\n" \
    "global_store_dword v14, v120, s[54:55] offset:384 sc1\n" \
    "global_store_dword v14, v73, s[54:55] offset:2560 sc1\n" \
    "global_store_dword v14, v89, s[54:55] offset:2688 sc1\n" \
    "global_store_dword v14, v105, s[54:55] offset:2816 sc1\n" \
    "global_store_dword v14, v121, s[54:55] offset:2944 sc1\n" \
    "global_store_dword v15, v74, s[54:55] offset:0 sc1\n" \
    "global_store_dword v15, v90, s[54:55] offset:128 sc1\n" \
    "global_store_dword v15, v106, s[54:55] offset:256 sc1\n" \
    "global_store_dword v15, v122, s[54:55] offset:384 sc1\n" \
    "global_store_dword v15, v75, s[54:55] offset:2560 sc1\n" \
    "global_store_dword v15, v91, s[54:55] offset:2688 sc1\n" \
    "global_store_dword v15, v107, s[54:55] offset:2816 sc1\n" \
    "global_store_dword v15, v123, s[54:55] offset:2944 sc1\n" \
    "s_add_u32 s54, s54, 20480\n" \
    "s_addc_u32 s55, s55, 0\n" \
    "global_store_dword v14, v76, s[54:55] offset:0 sc1\n" \
    "global_store_dword v14, v92, s[54:55] offset:128 sc1\n" \
    "global_store_dword v14, v108, s[54:55] offset:256 sc1\n" \
    "global_store_dword v14, v124, s[54:55] offset:384 sc1\n" \
    "global_store_dword v14, v77, s[54:55] offset:2560 sc1\n" \
    "global_store_dword v14, v93, s[54:55] offset:2688 sc1\n" \
    "global_store_dword v14, v109, s[54:55] offset:2816 sc1\n" \
    "global_store_dword v14, v125, s[54:55] offset:2944 sc1\n" \
    "global_store_dword v15, v78, s[54:55] offset:0 sc1\n" \
    "global_store_dword v15, v94, s[54:55] offset:128 sc1\n" \
    "global_store_dword v15, v110, s[54:55] offset:256 sc1\n" \
    "global_store_dword v15, v126, s[54:55] offset:384 sc1\n" \
    "global_store_dword v15, v79, s[54:55] offset:2560 sc1\n" \
    "global_store_dword v15, v95, s[54:55] offset:2688 sc1\n" \
    "global_store_dword v15, v111, s[54:55] offset:2816 sc1\n" \
    "global_store_dword v15, v127, s[54:55] offset:2944 sc1\n" \
    "s_nop 1\n" \
    ""
#define GEMMB_ASM \
    "s_mov_b32 s40, %3\n" \
    "s_mov_b32 s52, %4\n" \
    "s_mov_b32 s46, %2\n" \
    "s_mov_b64 exec, -1\n" \
    "s_mov_b64 s[42:43], %0\n" \
    "s_mov_b64 s[44:45], %1\n" \
    "s_mul_i32 s51, s40, 36864\n" \
    "v_mbcnt_lo_u32_b32 v0, -1, 0\n" \
    "v_mbcnt_hi_u32_b32 v0, -1, v0\n" \
    "v_lshrrev_b32 v14, 3, v0\n" \
    "v_mul_lo_u32 v1, v14, s46\n" \
    "v_lshrrev_b32 v14, 4, v0\n" \
    "v_bfe_u32 v15, v0, 1, 2\n" \
    "v_xor_b32 v14, v14, v15\n" \
    "v_lshl_add_u32 v1, v14, 5, v1\n" \
    "v_and_b32 v14, 1, v0\n" \
    "v_lshl_add_u32 v1, v14, 4, v1\n" \
    "v_lshlrev_b32 v2, 4, v0\n" \
    "v_lshrrev_b32 v14, 5, v0\n" \
    "v_lshlrev_b32 v14, 4, v14\n" \
    "v_bfe_u32 v15, v0, 2, 3\n" \
    "v_add_u32 v14, v14, v15\n" \
    "v_and_b32 v8, 3, v0\n" \
    "v_lshlrev_b32 v8, 3, v8\n" \
    "v_add_u32 v8, s51, v8\n" \
    "v_mul_u32_u24 v7, 160, v14\n" \
    "v_add_u32 v7, v7, v8\n" \
    "v_lshl_add_u32 v14, v14, 7, v8\n" \
    "v_lshrrev_b32 v15, 1, v15\n" \
    "v_xor_b32 v3, 0, v15\n" \
    "v_lshl_add_u32 v3, v3, 5, v14\n" \
    "v_xor_b32 v4, 1, v15\n" \
    "v_lshl_add_u32 v4, v4, 5, v14\n" \
    "v_xor_b32 v5, 2, v15\n" \
    "v_lshl_add_u32 v5, v5, 5, v14\n" \
    "v_xor_b32 v6, 3, v15\n" \
    "v_lshl_add_u32 v6, v6, 5, v14\n" \
    "v_mov_b64 v[96:97], 0\n" \
    "v_mov_b64 v[98:99], 0\n" \
    "v_mov_b64 v[100:101], 0\n" \
    "v_mov_b64 v[102:103], 0\n" \
    "v_mov_b64 v[104:105], 0\n" \
    "v_mov_b64 v[106:107], 0\n" \
    "v_mov_b64 v[108:109], 0\n" \
    "v_mov_b64 v[110:111], 0\n" \
    "v_mov_b64 v[112:113], 0\n" \
    "v_mov_b64 v[114:115], 0\n" \
    "v_mov_b64 v[116:117], 0\n" \
    "v_mov_b64 v[118:119], 0\n" \
    "v_mov_b64 v[120:121], 0\n" \
    "v_mov_b64 v[122:123], 0\n" \
    "v_mov_b64 v[124:125], 0\n" \
    "v_mov_b64 v[126:127], 0\n" \
    "v_mov_b64 v[128:129], 0\n" \
    "v_mov_b64 v[130:131], 0\n" \
    "v_mov_b64 v[132:133], 0\n" \
    "v_mov_b64 v[134:135], 0\n" \
    "v_mov_b64 v[136:137], 0\n" \
    "v_mov_b64 v[138:139], 0\n" \
    "v_mov_b64 v[140:141], 0\n" \
    "v_mov_b64 v[142:143], 0\n" \
    "v_mov_b64 v[144:145], 0\n" \
    "v_mov_b64 v[146:147], 0\n" \
    "v_mov_b64 v[148:149], 0\n" \
    "v_mov_b64 v[150:151], 0\n" \
    "v_mov_b64 v[152:153], 0\n" \
    "v_mov_b64 v[154:155], 0\n" \
    "v_mov_b64 v[156:157], 0\n" \
    "v_mov_b64 v[158:159], 0\n" \
    "v_mov_b64 v[160:161], 0\n" \
    "v_mov_b64 v[162:163], 0\n" \
    "v_mov_b64 v[164:165], 0\n" \
    "v_mov_b64 v[166:167], 0\n" \
    "v_mov_b64 v[168:169], 0\n" \
    "v_mov_b64 v[170:171], 0\n" \
    "v_mov_b64 v[172:173], 0\n" \
    "v_mov_b64 v[174:175], 0\n" \
    "s_lshl_b32 s46, s46, 3\n" \
    "s_add_u32 m0, s51, 0\n" \
    "s_nop 0\n" \
    "global_load_lds_dwordx4 v1, s[42:43]\n" \
    "s_add_u32 s42, s42, s46\n" \
    "s_addc_u32 s43, s43, 0\n" \
    "s_add_u32 m0, s51, 1024\n" \
    "s_nop 0\n" \
    "global_load_lds_dwordx4 v1, s[42:43]\n" \
    "s_add_u32 s42, s42, s46\n" \
    "s_addc_u32 s43, s43, 0\n" \
    "s_add_u32 m0, s51, 2048\n" \
    "s_nop 0\n" \
    "global_load_lds_dwordx4 v1, s[42:43]\n" \
    "s_add_u32 s42, s42, s46\n" \
    "s_addc_u32 s43, s43, 0\n" \
    "s_add_u32 m0, s51, 3072\n" \
    "s_nop 0\n" \
    "global_load_lds_dwordx4 v1, s[42:43]\n" \
    "s_add_u32 s42, s42, s46\n" \
    "s_addc_u32 s43, s43, 0\n" \
    "s_mul_i32 s50, s46, 12\n" \
    "s_add_u32 s42, s42, s50\n" \
    "s_addc_u32 s43, s43, 0\n" \
    "s_add_u32 m0, s51, 4096\n" \
    "s_nop 0\n" \
    "global_load_lds_dwordx4 v2, s[44:45] offset:0\n" \
    "global_load_lds_dwordx4 v2, s[44:45] offset:1024\n" \
    "global_load_lds_dwordx4 v2, s[44:45] offset:2048\n" \
    "global_load_lds_dwordx4 v2, s[44:45] offset:3072\n" \
    "s_add_u32 m0, s51, 8192\n" \
    "s_add_u32 s44, s44, 4096\n" \
    "s_addc_u32 s45, s45, 0\n" \
    "global_load_lds_dwordx4 v2, s[44:45]\n" \
    "s_add_u32 s44, s44, 16384\n" \
    "s_addc_u32 s45, s45, 0\n" \
    "s_add_u32 m0, s51, 9216\n" \
    "s_nop 0\n" \
    "global_load_lds_dwordx4 v1, s[42:43]\n" \
    "s_add_u32 s42, s42, s46\n" \
    "s_addc_u32 s43, s43, 0\n" \
    "s_add_u32 m0, s51, 10240\n" \
    "s_nop 0\n" \
    "global_load_lds_dwordx4 v1, s[42:43]\n" \
    "s_add_u32 s42, s42, s46\n" \
    "s_addc_u32 s43, s43, 0\n" \
    "s_add_u32 m0, s51, 11264\n" \
    "s_nop 0\n" \
    "global_load_lds_dwordx4 v1, s[42:43]\n" \
    "s_add_u32 s42, s42, s46\n" \
    "s_addc_u32 s43, s43, 0\n" \
    "s_add_u32 m0, s51, 12288\n" \
    "s_nop 0\n" \
    "global_load_lds_dwordx4 v1, s[42:43]\n" \
    "s_add_u32 s42, s42, s46\n" \
    "s_addc_u32 s43, s43, 0\n" \
    "s_mul_i32 s50, s46, 12\n" \
    "s_add_u32 s42, s42, s50\n" \
    "s_addc_u32 s43, s43, 0\n" \
    "s_add_u32 m0, s51, 13312\n" \
    "s_nop 0\n" \
    "global_load_lds_dwordx4 v2, s[44:45] offset:0\n" \
    "global_load_lds_dwordx4 v2, s[44:45] offset:1024\n" \
    "global_load_lds_dwordx4 v2, s[44:45] offset:2048\n" \
    "global_load_lds_dwordx4 v2, s[44:45] offset:3072\n" \
    "s_add_u32 m0, s51, 17408\n" \
    "s_add_u32 s44, s44, 4096\n" \
    "s_addc_u32 s45, s45, 0\n" \
    "global_load_lds_dwordx4 v2, s[44:45]\n" \
    "s_add_u32 s44, s44, 16384\n" \
    "s_addc_u32 s45, s45, 0\n" \
    "s_add_u32 m0, s51, 18432\n" \
    "s_nop 0\n" \
    "global_load_lds_dwordx4 v1, s[42:43]\n" \
    "s_add_u32 s42, s42, s46\n" \
    "s_addc_u32 s43, s43, 0\n" \
    "s_add_u32 m0, s51, 19456\n" \
    "s_nop 0\n" \
    "global_load_lds_dwordx4 v1, s[42:43]\n" \
    "s_add_u32 s42, s42, s46\n" \
    "s_addc_u32 s43, s43, 0\n" \
    "s_add_u32 m0, s51, 20480\n" \
    "s_nop 0\n" \
    "global_load_lds_dwordx4 v1, s[42:43]\n" \
    "s_add_u32 s42, s42, s46\n" \
    "s_addc_u32 s43, s43, 0\n" \
    "s_add_u32 m0, s51, 21504\n" \
    "s_nop 0\n" \
    "global_load_lds_dwordx4 v1, s[42:43]\n" \
    "s_add_u32 s42, s42, s46\n" \
    "s_addc_u32 s43, s43, 0\n" \
    "s_mul_i32 s50, s46, 12\n" \
    "s_add_u32 s42, s42, s50\n" \
    "s_addc_u32 s43, s43, 0\n" \
    "s_add_u32 m0, s51, 22528\n" \
    "s_nop 0\n" \
    "global_load_lds_dwordx4 v2, s[44:45] offset:0\n" \
    "global_load_lds_dwordx4 v2, s[44:45] offset:1024\n" \
    "global_load_lds_dwordx4 v2, s[44:45] offset:2048\n" \
    "global_load_lds_dwordx4 v2, s[44:45] offset:3072\n" \
    "s_add_u32 m0, s51, 26624\n" \
    "s_add_u32 s44, s44, 4096\n" \
    "s_addc_u32 s45, s45, 0\n" \
    "global_load_lds_dwordx4 v2, s[44:45]\n" \
    "s_add_u32 s44, s44, 16384\n" \
    "s_addc_u32 s45, s45, 0\n" \
    "s_waitcnt vmcnt(18)\n" \
    "ds_read_b64_tr_b16 v[32:33], v7 offset:4096\n" \
    "ds_read_b64_tr_b16 v[34:35], v7 offset:5376\n" \
    "ds_read_b64_tr_b16 v[36:37], v7 offset:4128\n" \
    "ds_read_b64_tr_b16 v[38:39], v7 offset:5408\n" \
    "ds_read_b64_tr_b16 v[40:41], v7 offset:4160\n" \
    "ds_read_b64_tr_b16 v[42:43], v7 offset:5440\n" \
    "ds_read_b64_tr_b16 v[44:45], v7 offset:4192\n" \
    "ds_read_b64_tr_b16 v[46:47], v7 offset:5472\n" \
    "ds_read_b64_tr_b16 v[48:49], v7 offset:4224\n" \
    "ds_read_b64_tr_b16 v[50:51], v7 offset:5504\n" \
    "ds_read_b64_tr_b16 v[16:17], v3 offset:0\n" \
    "ds_read_b64_tr_b16 v[18:19], v3 offset:1024\n" \
    "s_waitcnt lgkmcnt(8)\n" \
    "ds_read_b64_tr_b16 v[20:21], v4 offset:0\n" \
    "ds_read_b64_tr_b16 v[22:23], v4 offset:1024\n" \
    "ds_read_b64_tr_b16 v[24:25], v5 offset:0\n" \
    "ds_read_b64_tr_b16 v[26:27], v5 offset:1024\n" \
    "ds_read_b64_tr_b16 v[28:29], v6 offset:0\n" \
    "ds_read_b64_tr_b16 v[30:31], v6 offset:1024\n" \
    "GLOOP%=:\n" \
    "s_cmp_lt_u32 s52, 7\n" \
    "s_cbranch_scc1 GTAIL%=\n" \
    "s_waitcnt lgkmcnt(0)\n" \
    "v_mfma_f32_16x16x32_bf16 v[96:99], v[32:35], v[16:19], v[96:99]\n" \
    "s_add_u32 m0, s51, 27648\n" \
    "s_nop 0\n" \
    "global_load_lds_dwordx4 v1, s[42:43]\n" \
    "s_add_u32 s42, s42, s46\n" \
    "s_addc_u32 s43, s43, 0\n" \
    "v_mfma_f32_16x16x32_bf16 v[100:103], v[36:39], v[16:19], v[100:103]\n" \
    "s_add_u32 m0, s51, 28672\n" \
    "s_nop 0\n" \
    "global_load_lds_dwordx4 v1, s[42:43]\n" \
    "s_add_u32 s42, s42, s46\n" \
    "s_addc_u32 s43, s43, 0\n" \
    "v_mfma_f32_16x16x32_bf16 v[104:107], v[40:43], v[16:19], v[104:107]\n" \
    "s_add_u32 m0, s51, 29696\n" \
    "s_nop 0\n" \
    "global_load_lds_dwordx4 v1, s[42:43]\n" \
    "s_add_u32 s42, s42, s46\n" \
    "s_addc_u32 s43, s43, 0\n" \
    "v_mfma_f32_16x16x32_bf16 v[108:111], v[44:47], v[16:19], v[108:111]\n" \
    "s_add_u32 m0, s51, 30720\n" \
    "s_nop 0\n" \
    "global_load_lds_dwordx4 v1, s[42:43]\n" \
    "s_add_u32 s42, s42, s46\n" \
    "s_addc_u32 s43, s43, 0\n" \
    "s_mul_i32 s50, s46, 12\n" \
    "s_add_u32 s42, s42, s50\n" \
    "s_addc_u32 s43, s43, 0\n" \
    "v_mfma_f32_16x16x32_bf16 v[112:115], v[48:51], v[16:19], v[112:115]\n" \
    "s_add_u32 m0, s51, 31744\n" \
    "s_nop 0\n" \
    "global_load_lds_dwordx4 v2, s[44:45] offset:0\n" \
    "global_load_lds_dwordx4 v2, s[44:45] offset:1024\n" \
    "v_mfma_f32_16x16x32_bf16 v[116:119], v[32:35], v[20:23], v[116:119]\n" \
    "global_load_lds_dwordx4 v2, s[44:45] offset:2048\n" \
    "global_load_lds_dwordx4 v2, s[44:45] offset:3072\n" \
    "v_mfma_f32_16x16x32_bf16 v[120:123], v[36:39], v[20:23], v[120:123]\n" \
    "s_add_u32 m0, s51, 35840\n" \
    "s_add_u32 s44, s44, 4096\n" \
    "s_addc_u32 s45, s45, 0\n" \
    "global_load_lds_dwordx4 v2, s[44:45]\n" \
    "s_add_u32 s44, s44, 16384\n" \
    "s_addc_u32 s45, s45, 0\n" \
    "v_mfma_f32_16x16x32_bf16 v[124:127], v[40:43], v[20:23], v[124:127]\n" \
    "s_waitcnt vmcnt(18)\n" \
    "v_mfma_f32_16x16x32_bf16 v[128:131], v[44:47], v[20:23], v[128:131]\n" \
    "ds_read_b64_tr_b16 v[68:69], v7 offset:13312\n" \
    "ds_read_b64_tr_b16 v[70:71], v7 offset:14592\n" \
    "v_mfma_f32_16x16x32_bf16 v[132:135], v[48:51], v[20:23], v[132:135]\n" \
    "ds_read_b64_tr_b16 v[72:73], v7 offset:13344\n" \
    "ds_read_b64_tr_b16 v[74:75], v7 offset:14624\n" \
    "v_mfma_f32_16x16x32_bf16 v[136:139], v[32:35], v[24:27], v[136:139]\n" \
    "ds_read_b64_tr_b16 v[76:77], v7 offset:13376\n" \
    "ds_read_b64_tr_b16 v[78:79], v7 offset:14656\n" \
    "v_mfma_f32_16x16x32_bf16 v[140:143], v[36:39], v[24:27], v[140:143]\n" \
    "ds_read_b64_tr_b16 v[80:81], v7 offset:13408\n" \
    "ds_read_b64_tr_b16 v[82:83], v7 offset:14688\n" \
    "v_mfma_f32_16x16x32_bf16 v[144:147], v[40:43], v[24:27], v[144:147]\n" \
    "ds_read_b64_tr_b16 v[84:85], v7 offset:13440\n" \
    "ds_read_b64_tr_b16 v[86:87], v7 offset:14720\n" \
    "v_mfma_f32_16x16x32_bf16 v[148:151], v[44:47], v[24:27], v[148:151]\n" \
    "ds_read_b64_tr_b16 v[52:53], v3 offset:9216\n" \
    "ds_read_b64_tr_b16 v[54:55], v3 offset:10240\n" \
    "v_mfma_f32_16x16x32_bf16 v[152:155], v[48:51], v[24:27], v[152:155]\n" \
    "ds_read_b64_tr_b16 v[56:57], v4 offset:9216\n" \
    "ds_read_b64_tr_b16 v[58:59], v4 offset:10240\n" \
    "s_waitcnt lgkmcnt(10)\n" \
    "v_mfma_f32_16x16x32_bf16 v[156:159], v[32:35], v[28:31], v[156:159]\n" \
    "ds_read_b64_tr_b16 v[60:61], v5 offset:9216\n" \
    "ds_read_b64_tr_b16 v[62:63], v5 offset:10240\n" \
    "v_mfma_f32_16x16x32_bf16 v[160:163], v[36:39], v[28:31], v[160:163]\n" \
    "ds_read_b64_tr_b16 v[64:65], v6 offset:9216\n" \
    "ds_read_b64_tr_b16 v[66:67], v6 offset:10240\n" \
    "v_mfma_f32_16x16x32_bf16 v[164:167], v[40:43], v[28:31], v[164:167]\n" \
    "v_mfma_f32_16x16x32_bf16 v[168:171], v[44:47], v[28:31], v[168:171]\n" \
    "v_mfma_f32_16x16x32_bf16 v[172:175], v[48:51], v[28:31], v[172:175]\n" \
    "s_waitcnt lgkmcnt(0)\n" \
    "v_mfma_f32_16x16x32_bf16 v[96:99], v[68:71], v[52:55], v[96:99]\n" \
    "s_add_u32 m0, s51, 0\n" \
    "s_nop 0\n" \
    "global_load_lds_dwordx4 v1, s[42:43]\n" \
    "s_add_u32 s42, s42, s46\n" \
    "s_addc_u32 s43, s43, 0\n" \
    "v_mfma_f32_16x16x32_bf16 v[100:103], v[72:75], v[52:55], v[100:103]\n" \
    "s_add_u32 m0, s51, 1024\n" \
    "s_nop 0\n" \
    "global_load_lds_dwordx4 v1, s[42:43]\n" \
    "s_add_u32 s42, s42, s46\n" \
    "s_addc_u32 s43, s43, 0\n" \
    "v_mfma_f32_16x16x32_bf16 v[104:107], v[76:79], v[52:55], v[104:107]\n" \
    "s_add_u32 m0, s51, 2048\n" \
    "s_nop 0\n" \
    "global_load_lds_dwordx4 v1, s[42:43]\n" \
    "s_add_u32 s42, s42, s46\n" \
    "s_addc_u32 s43, s43, 0\n" \
    "v_mfma_f32_16x16x32_bf16 v[108:111], v[80:83], v[52:55], v[108:111]\n" \
    "s_add_u32 m0, s51, 3072\n" \
    "s_nop 0\n" \
    "global_load_lds_dwordx4 v1, s[42:43]\n" \
    "s_add_u32 s42, s42, s46\n" \
    "s_addc_u32 s43, s43, 0\n" \
    "s_mul_i32 s50, s46, 12\n" \
    "s_add_u32 s42, s42, s50\n" \
    "s_addc_u32 s43, s43, 0\n" \
    "v_mfma_f32_16x16x32_bf16 v[112:115], v[84:87], v[52:55], v[112:115]\n" \
    "s_add_u32 m0, s51, 4096\n" \
    "s_nop 0\n" \
    "global_load_lds_dwordx4 v2, s[44:45] offset:0\n" \
    "global_load_lds_dwordx4 v2, s[44:45] offset:1024\n" \
    "v_mfma_f32_16x16x32_bf16 v[116:119], v[68:71], v[56:59], v[116:119]\n" \
    "global_load_lds_dwordx4 v2, s[44:45] offset:2048\n" \
    "global_load_lds_dwordx4 v2, s[44:45] offset:3072\n" \
    "v_mfma_f32_16x16x32_bf16 v[120:123], v[72:75], v[56:59], v[120:123]\n" \
    "s_add_u32 m0, s51, 8192\n" \
    "s_add_u32 s44, s44, 4096\n" \
    "s_addc_u32 s45, s45, 0\n" \
    "global_load_lds_dwordx4 v2, s[44:45]\n" \
    "s_add_u32 s44, s44, 16384\n" \
    "s_addc_u32 s45, s45, 0\n" \
    "v_mfma_f32_16x16x32_bf16 v[124:127], v[76:79], v[56:59], v[124:127]\n" \
    "s_waitcnt vmcnt(18)\n" \
    "v_mfma_f32_16x16x32_bf16 v[128:131], v[80:83], v[56:59], v[128:131]\n" \
    "ds_read_b64_tr_b16 v[32:33], v7 offset:22528\n" \
    "ds_read_b64_tr_b16 v[34:35], v7 offset:23808\n" \
    "v_mfma_f32_16x16x32_bf16 v[132:135], v[84:87], v[56:59], v[132:135]\n" \
    "ds_read_b64_tr_b16 v[36:37], v7 offset:22560\n" \
    "ds_read_b64_tr_b16 v[38:39], v7 offset:23840\n" \
    "v_mfma_f32_16x16x32_bf16 v[136:139], v[68:71], v[60:63], v[136:139]\n" \
    "ds_read_b64_tr_b16 v[40:41], v7 offset:22592\n" \
    "ds_read_b64_tr_b16 v[42:43], v7 offset:23872\n" \
    "v_mfma_f32_16x16x32_bf16 v[140:143], v[72:75], v[60:63], v[140:143]\n" \
    "ds_read_b64_tr_b16 v[44:45], v7 offset:22624\n" \
    "ds_read_b64_tr_b16 v[46:47], v7 offset:23904\n" \
    "v_mfma_f32_16x16x32_bf16 v[144:147], v[76:79], v[60:63], v[144:147]\n" \
    "ds_read_b64_tr_b16 v[48:49], v7 offset:22656\n" \
    "ds_read_b64_tr_b16 v[50:51], v7 offset:23936\n" \
    "v_mfma_f32_16x16x32_bf16 v[148:151], v[80:83], v[60:63], v[148:151]\n" \
    "ds_read_b64_tr_b16 v[16:17], v3 offset:18432\n" \
    "ds_read_b64_tr_b16 v[18:19], v3 offset:19456\n" \
    "v_mfma_f32_16x16x32_bf16 v[152:155], v[84:87], v[60:63], v[152:155]\n" \
    "ds_read_b64_tr_b16 v[20:21], v4 offset:18432\n" \
    "ds_read_b64_tr_b16 v[22:23], v4 offset:19456\n" \
    "s_waitcnt lgkmcnt(10)\n" \
    "v_mfma_f32_16x16x32_bf16 v[156:159], v[68:71], v[64:67], v[156:159]\n" \
    "ds_read_b64_tr_b16 v[24:25], v5 offset:18432\n" \
    "ds_read_b64_tr_b16 v[26:27], v5 offset:19456\n" \
    "v_mfma_f32_16x16x32_bf16 v[160:163], v[72:75], v[64:67], v[160:163]\n" \
    "ds_read_b64_tr_b16 v[28:29], v6 offset:18432\n" \
    "ds_read_b64_tr_b16 v[30:31], v6 offset:19456\n" \
    "v_mfma_f32_16x16x32_bf16 v[164:167], v[76:79], v[64:67], v[164:167]\n" \
    "v_mfma_f32_16x16x32_bf16 v[168:171], v[80:83], v[64:67], v[168:171]\n" \
    "v_mfma_f32_16x16x32_bf16 v[172:175], v[84:87], v[64:67], v[172:175]\n" \
    "s_waitcnt lgkmcnt(0)\n" \
    "v_mfma_f32_16x16x32_bf16 v[96:99], v[32:35], v[16:19], v[96:99]\n" \
    "s_add_u32 m0, s51, 9216\n" \
    "s_nop 0\n" \
    "global_load_lds_dwordx4 v1, s[42:43]\n" \
    "s_add_u32 s42, s42, s46\n" \
    "s_addc_u32 s43, s43, 0\n" \
    "v_mfma_f32_16x16x32_bf16 v[100:103], v[36:39], v[16:19], v[100:103]\n" \
    "s_add_u32 m0, s51, 10240\n" \
    "s_nop 0\n" \
    "global_load_lds_dwordx4 v1, s[42:43]\n" \
    "s_add_u32 s42, s42, s46\n" \
    "s_addc_u32 s43, s43, 0\n" \
    "v_mfma_f32_16x16x32_bf16 v[104:107], v[40:43], v[16:19], v[104:107]\n" \
    "s_add_u32 m0, s51, 11264\n" \
    "s_nop 0\n" \
    "global_load_lds_dwordx4 v1, s[42:43]\n" \
    "s_add_u32 s42, s42, s46\n" \
    "s_addc_u32 s43, s43, 0\n" \
    "v_mfma_f32_16x16x32_bf16 v[108:111], v[44:47], v[16:19], v[108:111]\n" \
    "s_add_u32 m0, s51, 12288\n" \
    "s_nop 0\n" \
    "global_load_lds_dwordx4 v1, s[42:43]\n" \
    "s_add_u32 s42, s42, s46\n" \
    "s_addc_u32 s43, s43, 0\n" \
    "s_mul_i32 s50, s46, 12\n" \
    "s_add_u32 s42, s42, s50\n" \
    "s_addc_u32 s43, s43, 0\n" \
    "v_mfma_f32_16x16x32_bf16 v[112:115], v[48:51], v[16:19], v[112:115]\n" \
    "s_add_u32 m0, s51, 13312\n" \
    "s_nop 0\n" \
    "global_load_lds_dwordx4 v2, s[44:45] offset:0\n" \
    "global_load_lds_dwordx4 v2, s[44:45] offset:1024\n" \
    "v_mfma_f32_16x16x32_bf16 v[116:119], v[32:35], v[20:23], v[116:119]\n" \
    "global_load_lds_dwordx4 v2, s[44:45] offset:2048\n" \
    "global_load_lds_dwordx4 v2, s[44:45] offset:3072\n" \
    "v_mfma_f32_16x16x32_bf16 v[120:123], v[36:39], v[20:23], v[120:123]\n" \
    "s_add_u32 m0, s51, 17408\n" \
    "s_add_u32 s44, s44, 4096\n" \
    "s_addc_u32 s45, s45, 0\n" \
    "global_load_lds_dwordx4 v2, s[44:45]\n" \
    "s_add_u32 s44, s44, 16384\n" \
    "s_addc_u32 s45, s45, 0\n" \
    "v_mfma_f32_16x16x32_bf16 v[124:127], v[40:43], v[20:23], v[124:127]\n" \
    "s_waitcnt vmcnt(18)\n" \
    "v_mfma_f32_16x16x32_bf16 v[128:131], v[44:47], v[20:23], v[128:131]\n" \
    "ds_read_b64_tr_b16 v[68:69], v7 offset:31744\n" \
    "ds_read_b64_tr_b16 v[70:71], v7 offset:33024\n" \
    "v_mfma_f32_16x16x32_bf16 v[132:135], v[48:51], v[20:23], v[132:135]\n" \
    "ds_read_b64_tr_b16 v[72:73], v7 offset:31776\n" \
    "ds_read_b64_tr_b16 v[74:75], v7 offset:33056\n" \
    "v_mfma_f32_16x16x32_bf16 v[136:139], v[32:35], v[24:27], v[136:139]\n" \
    "ds_read_b64_tr_b16 v[76:77], v7 offset:31808\n" \
    "ds_read_b64_tr_b16 v[78:79], v7 offset:33088\n" \
    "v_mfma_f32_16x16x32_bf16 v[140:143], v[36:39], v[24:27], v[140:143]\n" \
    "ds_read_b64_tr_b16 v[80:81], v7 offset:31840\n" \
    "ds_read_b64_tr_b16 v[82:83], v7 offset:33120\n" \
    "v_mfma_f32_16x16x32_bf16 v[144:147], v[40:43], v[24:27], v[144:147]\n" \
    "ds_read_b64_tr_b16 v[84:85], v7 offset:31872\n" \
    "ds_read_b64_tr_b16 v[86:87], v7 offset:33152\n" \
    "v_mfma_f32_16x16x32_bf16 v[148:151], v[44:47], v[24:27], v[148:151]\n" \
    "ds_read_b64_tr_b16 v[52:53], v3 offset:27648\n" \
    "ds_read_b64_tr_b16 v[54:55], v3 offset:28672\n" \
    "v_mfma_f32_16x16x32_bf16 v[152:155], v[48:51], v[24:27], v[152:155]\n" \
    "ds_read_b64_tr_b16 v[56:57], v4 offset:27648\n" \
    "ds_read_b64_tr_b16 v[58:59], v4 offset:28672\n" \
    "s_waitcnt lgkmcnt(10)\n" \
    "v_mfma_f32_16x16x32_bf16 v[156:159], v[32:35], v[28:31], v[156:159]\n" \
    "ds_read_b64_tr_b16 v[60:61], v5 offset:27648\n" \
    "ds_read_b64_tr_b16 v[62:63], v5 offset:28672\n" \
    "v_mfma_f32_16x16x32_bf16 v[160:163], v[36:39], v[28:31], v[160:163]\n" \
    "ds_read_b64_tr_b16 v[64:65], v6 offset:27648\n" \
    "ds_read_b64_tr_b16 v[66:67], v6 offset:28672\n" \
    "v_mfma_f32_16x16x32_bf16 v[164:167], v[40:43], v[28:31], v[164:167]\n" \
    "v_mfma_f32_16x16x32_bf16 v[168:171], v[44:47], v[28:31], v[168:171]\n" \
    "v_mfma_f32_16x16x32_bf16 v[172:175], v[48:51], v[28:31], v[172:175]\n" \
    "s_waitcnt lgkmcnt(0)\n" \
    "v_mfma_f32_16x16x32_bf16 v[96:99], v[68:71], v[52:55], v[96:99]\n" \
    "s_add_u32 m0, s51, 18432\n" \
    "s_nop 0\n" \
    "global_load_lds_dwordx4 v1, s[42:43]\n" \
    "s_add_u32 s42, s42, s46\n" \
    "s_addc_u32 s43, s43, 0\n" \
    "v_mfma_f32_16x16x32_bf16 v[100:103], v[72:75], v[52:55], v[100:103]\n" \
    "s_add_u32 m0, s51, 19456\n" \
    "s_nop 0\n" \
    "global_load_lds_dwordx4 v1, s[42:43]\n" \
    "s_add_u32 s42, s42, s46\n" \
    "s_addc_u32 s43, s43, 0\n" \
    "v_mfma_f32_16x16x32_bf16 v[104:107], v[76:79], v[52:55], v[104:107]\n" \
    "s_add_u32 m0, s51, 20480\n" \
    "s_nop 0\n" \
    "global_load_lds_dwordx4 v1, s[42:43]\n" \
    "s_add_u32 s42, s42, s46\n" \
    "s_addc_u32 s43, s43, 0\n" \
    "v_mfma_f32_16x16x32_bf16 v[108:111], v[80:83], v[52:55], v[108:111]\n" \
    "s_add_u32 m0, s51, 21504\n" \
    "s_nop 0\n" \
    "global_load_lds_dwordx4 v1, s[42:43]\n" \
    "s_add_u32 s42, s42, s46\n" \
    "s_addc_u32 s43, s43, 0\n" \
    "s_mul_i32 s50, s46, 12\n" \
    "s_add_u32 s42, s42, s50\n" \
    "s_addc_u32 s43, s43, 0\n" \
    "v_mfma_f32_16x16x32_bf16 v[112:115], v[84:87], v[52:55], v[112:115]\n" \
    "s_add_u32 m0, s51, 22528\n" \
    "s_nop 0\n" \
    "global_load_lds_dwordx4 v2, s[44:45] offset:0\n" \
    "global_load_lds_dwordx4 v2, s[44:45] offset:1024\n" \
    "v_mfma_f32_16x16x32_bf16 v[116:119], v[68:71], v[56:59], v[116:119]\n" \
    "global_load_lds_dwordx4 v2, s[44:45] offset:2048\n" \
    "global_load_lds_dwordx4 v2, s[44:45] offset:3072\n" \
    "v_mfma_f32_16x16x32_bf16 v[120:123], v[72:75], v[56:59], v[120:123]\n" \
    "s_add_u32 m0, s51, 26624\n" \
    "s_add_u32 s44, s44, 4096\n" \
    "s_addc_u32 s45, s45, 0\n" \
    "global_load_lds_dwordx4 v2, s[44:45]\n" \
    "s_add_u32 s44, s44, 16384\n" \
    "s_addc_u32 s45, s45, 0\n" \
    "v_mfma_f32_16x16x32_bf16 v[124:127], v[76:79], v[56:59], v[124:127]\n" \
    "s_waitcnt vmcnt(18)\n" \
    "v_mfma_f32_16x16x32_bf16 v[128:131], v[80:83], v[56:59], v[128:131]\n" \
    "ds_read_b64_tr_b16 v[32:33], v7 offset:4096\n" \
    "ds_read_b64_tr_b16 v[34:35], v7 offset:5376\n" \
    "v_mfma_f32_16x16x32_bf16 v[132:135], v[84:87], v[56:59], v[132:135]\n" \
    "ds_read_b64_tr_b16 v[36:37], v7 offset:4128\n" \
    "ds_read_b64_tr_b16 v[38:39], v7 offset:5408\n" \
    "v_mfma_f32_16x16x32_bf16 v[136:139], v[68:71], v[60:63], v[136:139]\n" \
    "ds_read_b64_tr_b16 v[40:41], v7 offset:4160\n" \
    "ds_read_b64_tr_b16 v[42:43], v7 offset:5440\n" \
    "v_mfma_f32_16x16x32_bf16 v[140:143], v[72:75], v[60:63], v[140:143]\n" \
    "ds_read_b64_tr_b16 v[44:45], v7 offset:4192\n" \
    "ds_read_b64_tr_b16 v[46:47], v7 offset:5472\n" \
    "v_mfma_f32_16x16x32_bf16 v[144:147], v[76:79], v[60:63], v[144:147]\n" \
    "ds_read_b64_tr_b16 v[48:49], v7 offset:4224\n" \
    "ds_read_b64_tr_b16 v[50:51], v7 offset:5504\n" \
    "v_mfma_f32_16x16x32_bf16 v[148:151], v[80:83], v[60:63], v[148:151]\n" \
    "ds_read_b64_tr_b16 v[16:17], v3 offset:0\n" \
    "ds_read_b64_tr_b16 v[18:19], v3 offset:1024\n" \
    "v_mfma_f32_16x16x32_bf16 v[152:155], v[84:87], v[60:63], v[152:155]\n" \
    "ds_read_b64_tr_b16 v[20:21], v4 offset:0\n" \
    "ds_read_b64_tr_b16 v[22:23], v4 offset:1024\n" \
    "s_waitcnt lgkmcnt(10)\n" \
    "v_mfma_f32_16x16x32_bf16 v[156:159], v[68:71], v[64:67], v[156:159]\n" \
    "ds_read_b64_tr_b16 v[24:25], v5 offset:0\n" \
    "ds_read_b64_tr_b16 v[26:27], v5 offset:1024\n" \
    "v_mfma_f32_16x16x32_bf16 v[160:163], v[72:75], v[64:67], v[160:163]\n" \
    "ds_read_b64_tr_b16 v[28:29], v6 offset:0\n" \
    "ds_read_b64_tr_b16 v[30:31], v6 offset:1024\n" \
    "v_mfma_f32_16x16x32_bf16 v[164:167], v[76:79], v[64:67], v[164:167]\n" \
    "v_mfma_f32_16x16x32_bf16 v[168:171], v[80:83], v[64:67], v[168:171]\n" \
    "v_mfma_f32_16x16x32_bf16 v[172:175], v[84:87], v[64:67], v[172:175]\n" \
    "s_sub_u32 s52, s52, 4\n" \
    "s_branch GLOOP%=\n" \
    "GTAIL%=:\n" \
    "s_cmp_lg_u32 s52, 3\n" \
    "s_cbranch_scc1 GT3%=\n" \
    "s_waitcnt lgkmcnt(0)\n" \
    "v_mfma_f32_16x16x32_bf16 v[96:99], v[32:35], v[16:19], v[96:99]\n" \
    "v_mfma_f32_16x16x32_bf16 v[100:103], v[36:39], v[16:19], v[100:103]\n" \
    "v_mfma_f32_16x16x32_bf16 v[104:107], v[40:43], v[16:19], v[104:107]\n" \
    "v_mfma_f32_16x16x32_bf16 v[108:111], v[44:47], v[16:19], v[108:111]\n" \
    "v_mfma_f32_16x16x32_bf16 v[112:115], v[48:51], v[16:19], v[112:115]\n" \
    "v_mfma_f32_16x16x32_bf16 v[116:119], v[32:35], v[20:23], v[116:119]\n" \
    "v_mfma_f32_16x16x32_bf16 v[120:123], v[36:39], v[20:23], v[120:123]\n" \
    "v_mfma_f32_16x16x32_bf16 v[124:127], v[40:43], v[20:23], v[124:127]\n" \
    "s_waitcnt vmcnt(9)\n" \
    "v_mfma_f32_16x16x32_bf16 v[128:131], v[44:47], v[20:23], v[128:131]\n" \
    "ds_read_b64_tr_b16 v[68:69], v7 offset:13312\n" \
    "ds_read_b64_tr_b16 v[70:71], v7 offset:14592\n" \
    "v_mfma_f32_16x16x32_bf16 v[132:135], v[48:51], v[20:23], v[132:135]\n" \
    "ds_read_b64_tr_b16 v[72:73], v7 offset:13344\n" \
    "ds_read_b64_tr_b16 v[74:75], v7 offset:14624\n" \
    "v_mfma_f32_16x16x32_bf16 v[136:139], v[32:35], v[24:27], v[136:139]\n" \
    "ds_read_b64_tr_b16 v[76:77], v7 offset:13376\n" \
    "ds_read_b64_tr_b16 v[78:79], v7 offset:14656\n" \
    "v_mfma_f32_16x16x32_bf16 v[140:143], v[36:39], v[24:27], v[140:143]\n" \
    "ds_read_b64_tr_b16 v[80:81], v7 offset:13408\n" \
    "ds_read_b64_tr_b16 v[82:83], v7 offset:14688\n" \
    "v_mfma_f32_16x16x32_bf16 v[144:147], v[40:43], v[24:27], v[144:147]\n" \
    "ds_read_b64_tr_b16 v[84:85], v7 offset:13440\n" \
    "ds_read_b64_tr_b16 v[86:87], v7 offset:14720\n" \
    "v_mfma_f32_16x16x32_bf16 v[148:151], v[44:47], v[24:27], v[148:151]\n" \
    "ds_read_b64_tr_b16 v[52:53], v3 offset:9216\n" \
    "ds_read_b64_tr_b16 v[54:55], v3 offset:10240\n" \
    "v_mfma_f32_16x16x32_bf16 v[152:155], v[48:51], v[24:27], v[152:155]\n" \
    "ds_read_b64_tr_b16 v[56:57], v4 offset:9216\n" \
    "ds_read_b64_tr_b16 v[58:59], v4 offset:10240\n" \
    "s_waitcnt lgkmcnt(10)\n" \
    "v_mfma_f32_16x16x32_bf16 v[156:159], v[32:35], v[28:31], v[156:159]\n" \
    "ds_read_b64_tr_b16 v[60:61], v5 offset:9216\n" \
    "ds_read_b64_tr_b16 v[62:63], v5 offset:10240\n" \
    "v_mfma_f32_16x16x32_bf16 v[160:163], v[36:39], v[28:31], v[160:163]\n" \
    "ds_read_b64_tr_b16 v[64:65], v6 offset:9216\n" \
    "ds_read_b64_tr_b16 v[66:67], v6 offset:10240\n" \
    "v_mfma_f32_16x16x32_bf16 v[164:167], v[40:43], v[28:31], v[164:167]\n" \
    "v_mfma_f32_16x16x32_bf16 v[168:171], v[44:47], v[28:31], v[168:171]\n" \
    "v_mfma_f32_16x16x32_bf16 v[172:175], v[48:51], v[28:31], v[172:175]\n" \
    "s_waitcnt lgkmcnt(0)\n" \
    "v_mfma_f32_16x16x32_bf16 v[96:99], v[68:71], v[52:55], v[96:99]\n" \
    "v_mfma_f32_16x16x32_bf16 v[100:103], v[72:75], v[52:55], v[100:103]\n" \
    "v_mfma_f32_16x16x32_bf16 v[104:107], v[76:79], v[52:55], v[104:107]\n" \
    "v_mfma_f32_16x16x32_bf16 v[108:111], v[80:83], v[52:55], v[108:111]\n" \
    "v_mfma_f32_16x16x32_bf16 v[112:115], v[84:87], v[52:55], v[112:115]\n" \
    "v_mfma_f32_16x16x32_bf16 v[116:119], v[68:71], v[56:59], v[116:119]\n" \
    "v_mfma_f32_16x16x32_bf16 v[120:123], v[72:75], v[56:59], v[120:123]\n" \
    "v_mfma_f32_16x16x32_bf16 v[124:127], v[76:79], v[56:59], v[124:127]\n" \
    "s_waitcnt vmcnt(0)\n" \
    "v_mfma_f32_16x16x32_bf16 v[128:131], v[80:83], v[56:59], v[128:131]\n" \
    "ds_read_b64_tr_b16 v[32:33], v7 offset:22528\n" \
    "ds_read_b64_tr_b16 v[34:35], v7 offset:23808\n" \
    "v_mfma_f32_16x16x32_bf16 v[132:135], v[84:87], v[56:59], v[132:135]\n" \
    "ds_read_b64_tr_b16 v[36:37], v7 offset:22560\n" \
    "ds_read_b64_tr_b16 v[38:39], v7 offset:23840\n" \
    "v_mfma_f32_16x16x32_bf16 v[136:139], v[68:71], v[60:63], v[136:139]\n" \
    "ds_read_b64_tr_b16 v[40:41], v7 offset:22592\n" \
    "ds_read_b64_tr_b16 v[42:43], v7 offset:23872\n" \
    "v_mfma_f32_16x16x32_bf16 v[140:143], v[72:75], v[60:63], v[140:143]\n" \
    "ds_read_b64_tr_b16 v[44:45], v7 offset:22624\n" \
    "ds_read_b64_tr_b16 v[46:47], v7 offset:23904\n" \
    "v_mfma_f32_16x16x32_bf16 v[144:147], v[76:79], v[60:63], v[144:147]\n" \
    "ds_read_b64_tr_b16 v[48:49], v7 offset:22656\n" \
    "ds_read_b64_tr_b16 v[50:51], v7 offset:23936\n" \
    "v_mfma_f32_16x16x32_bf16 v[148:151], v[80:83], v[60:63], v[148:151]\n" \
    "ds_read_b64_tr_b16 v[16:17], v3 offset:18432\n" \
    "ds_read_b64_tr_b16 v[18:19], v3 offset:19456\n" \
    "v_mfma_f32_16x16x32_bf16 v[152:155], v[84:87], v[60:63], v[152:155]\n" \
    "ds_read_b64_tr_b16 v[20:21], v4 offset:18432\n" \
    "ds_read_b64_tr_b16 v[22:23], v4 offset:19456\n" \
    "s_waitcnt lgkmcnt(10)\n" \
    "v_mfma_f32_16x16x32_bf16 v[156:159], v[68:71], v[64:67], v[156:159]\n" \
    "ds_read_b64_tr_b16 v[24:25], v5 offset:18432\n" \
    "ds_read_b64_tr_b16 v[26:27], v5 offset:19456\n" \
    "v_mfma_f32_16x16x32_bf16 v[160:163], v[72:75], v[64:67], v[160:163]\n" \
    "ds_read_b64_tr_b16 v[28:29], v6 offset:18432\n" \
    "ds_read_b64_tr_b16 v[30:31], v6 offset:19456\n" \
    "v_mfma_f32_16x16x32_bf16 v[164:167], v[76:79], v[64:67], v[164:167]\n" \
    "v_mfma_f32_16x16x32_bf16 v[168:171], v[80:83], v[64:67], v[168:171]\n" \
    "v_mfma_f32_16x16x32_bf16 v[172:175], v[84:87], v[64:67], v[172:175]\n" \
    "s_waitcnt lgkmcnt(0)\n" \
    "v_mfma_f32_16x16x32_bf16 v[96:99], v[32:35], v[16:19], v[96:99]\n" \
    "v_mfma_f32_16x16x32_bf16 v[100:103], v[36:39], v[16:19], v[100:103]\n" \
    "v_mfma_f32_16x16x32_bf16 v[104:107], v[40:43], v[16:19], v[104:107]\n" \
    "v_mfma_f32_16x16x32_bf16 v[108:111], v[44:47], v[16:19], v[108:111]\n" \
    "v_mfma_f32_16x16x32_bf16 v[112:115], v[48:51], v[16:19], v[112:115]\n" \
    "v_mfma_f32_16x16x32_bf16 v[116:119], v[32:35], v[20:23], v[116:119]\n" \
    "v_mfma_f32_16x16x32_bf16 v[120:123], v[36:39], v[20:23], v[120:123]\n" \
    "v_mfma_f32_16x16x32_bf16 v[124:127], v[40:43], v[20:23], v[124:127]\n" \
    "v_mfma_f32_16x16x32_bf16 v[128:131], v[44:47], v[20:23], v[128:131]\n" \
    "v_mfma_f32_16x16x32_bf16 v[132:135], v[48:51], v[20:23], v[132:135]\n" \
    "v_mfma_f32_16x16x32_bf16 v[136:139], v[32:35], v[24:27], v[136:139]\n" \
    "v_mfma_f32_16x16x32_bf16 v[140:143], v[36:39], v[24:27], v[140:143]\n" \
    "v_mfma_f32_16x16x32_bf16 v[144:147], v[40:43], v[24:27], v[144:147]\n" \
    "v_mfma_f32_16x16x32_bf16 v[148:151], v[44:47], v[24:27], v[148:151]\n" \
    "v_mfma_f32_16x16x32_bf16 v[152:155], v[48:51], v[24:27], v[152:155]\n" \
    "v_mfma_f32_16x16x32_bf16 v[156:159], v[32:35], v[28:31], v[156:159]\n" \
    "v_mfma_f32_16x16x32_bf16 v[160:163], v[36:39], v[28:31], v[160:163]\n" \
    "v_mfma_f32_16x16x32_bf16 v[164:167], v[40:43], v[28:31], v[164:167]\n" \
    "v_mfma_f32_16x16x32_bf16 v[168:171], v[44:47], v[28:31], v[168:171]\n" \
    "v_mfma_f32_16x16x32_bf16 v[172:175], v[48:51], v[28:31], v[172:175]\n" \
    "s_branch GEND%=\n" \
    "GT3%=:\n" \
    "s_cmp_lg_u32 s52, 4\n" \
    "s_cbranch_scc1 GT4%=\n" \
    "s_waitcnt lgkmcnt(0)\n" \
    "v_mfma_f32_16x16x32_bf16 v[96:99], v[32:35], v[16:19], v[96:99]\n" \
    "s_add_u32 m0, s51, 27648\n" \
    "s_nop 0\n" \
    "global_load_lds_dwordx4 v1, s[42:43]\n" \
    "s_add_u32 s42, s42, s46\n" \
    "s_addc_u32 s43, s43, 0\n" \
    "v_mfma_f32_16x16x32_bf16 v[100:103], v[36:39], v[16:19], v[100:103]\n" \
    "s_add_u32 m0, s51, 28672\n" \
    "s_nop 0\n" \
    "global_load_lds_dwordx4 v1, s[42:43]\n" \
    "s_add_u32 s42, s42, s46\n" \
    "s_addc_u32 s43, s43, 0\n" \
    "v_mfma_f32_16x16x32_bf16 v[104:107], v[40:43], v[16:19], v[104:107]\n" \
    "s_add_u32 m0, s51, 29696\n" \
    "s_nop 0\n" \
    "global_load_lds_dwordx4 v1, s[42:43]\n" \
    "s_add_u32 s42, s42, s46\n" \
    "s_addc_u32 s43, s43, 0\n" \
    "v_mfma_f32_16x16x32_bf16 v[108:111], v[44:47], v[16:19], v[108:111]\n" \
    "s_add_u32 m0, s51, 30720\n" \
    "s_nop 0\n" \
    "global_load_lds_dwordx4 v1, s[42:43]\n" \
    "s_add_u32 s42, s42, s46\n" \
    "s_addc_u32 s43, s43, 0\n" \
    "s_mul_i32 s50, s46, 12\n" \
    "s_add_u32 s42, s42, s50\n" \
    "s_addc_u32 s43, s43, 0\n" \
    "v_mfma_f32_16x16x32_bf16 v[112:115], v[48:51], v[16:19], v[112:115]\n" \
    "s_add_u32 m0, s51, 31744\n" \
    "s_nop 0\n" \
    "global_load_lds_dwordx4 v2, s[44:45] offset:0\n" \
    "global_load_lds_dwordx4 v2, s[44:45] offset:1024\n" \
    "v_mfma_f32_16x16x32_bf16 v[116:119], v[32:35], v[20:23], v[116:119]\n" \
    "global_load_lds_dwordx4 v2, s[44:45] offset:2048\n" \
    "global_load_lds_dwordx4 v2, s[44:45] offset:3072\n" \
    "v_mfma_f32_16x16x32_bf16 v[120:123], v[36:39], v[20:23], v[120:123]\n" \
    "s_add_u32 m0, s51, 35840\n" \
    "s_add_u32 s44, s44, 4096\n" \
    "s_addc_u32 s45, s45, 0\n" \
    "global_load_lds_dwordx4 v2, s[44:45]\n" \
    "s_add_u32 s44, s44, 16384\n" \
    "s_addc_u32 s45, s45, 0\n" \
    "v_mfma_f32_16x16x32_bf16 v[124:127], v[40:43], v[20:23], v[124:127]\n" \
    "s_waitcnt vmcnt(18)\n" \
    "v_mfma_f32_16x16x32_bf16 v[128:131], v[44:47], v[20:23], v[128:131]\n" \
    "ds_read_b64_tr_b16 v[68:69], v7 offset:13312\n" \
    "ds_read_b64_tr_b16 v[70:71], v7 offset:14592\n" \
    "v_mfma_f32_16x16x32_bf16 v[132:135], v[48:51], v[20:23], v[132:135]\n" \
    "ds_read_b64_tr_b16 v[72:73], v7 offset:13344\n" \
    "ds_read_b64_tr_b16 v[74:75], v7 offset:14624\n" \
    "v_mfma_f32_16x16x32_bf16 v[136:139], v[32:35], v[24:27], v[136:139]\n" \
    "ds_read_b64_tr_b16 v[76:77], v7 offset:13376\n" \
    "ds_read_b64_tr_b16 v[78:79], v7 offset:14656\n" \
    "v_mfma_f32_16x16x32_bf16 v[140:143], v[36:39], v[24:27], v[140:143]\n" \
    "ds_read_b64_tr_b16 v[80:81], v7 offset:13408\n" \
    "ds_read_b64_tr_b16 v[82:83], v7 offset:14688\n" \
    "v_mfma_f32_16x16x32_bf16 v[144:147], v[40:43], v[24:27], v[144:147]\n" \
    "ds_read_b64_tr_b16 v[84:85], v7 offset:13440\n" \
    "ds_read_b64_tr_b16 v[86:87], v7 offset:14720\n" \
    "v_mfma_f32_16x16x32_bf16 v[148:151], v[44:47], v[24:27], v[148:151]\n" \
    "ds_read_b64_tr_b16 v[52:53], v3 offset:9216\n" \
    "ds_read_b64_tr_b16 v[54:55], v3 offset:10240\n" \
    "v_mfma_f32_16x16x32_bf16 v[152:155], v[48:51], v[24:27], v[152:155]\n" \
    "ds_read_b64_tr_b16 v[56:57], v4 offset:9216\n" \
    "ds_read_b64_tr_b16 v[58:59], v4 offset:10240\n" \
    "s_waitcnt lgkmcnt(10)\n" \
    "v_mfma_f32_16x16x32_bf16 v[156:159], v[32:35], v[28:31], v[156:159]\n" \
    "ds_read_b64_tr_b16 v[60:61], v5 offset:9216\n" \
    "ds_read_b64_tr_b16 v[62:63], v5 offset:10240\n" \
    "v_mfma_f32_16x16x32_bf16 v[160:163], v[36:39], v[28:31], v[160:163]\n" \
    "ds_read_b64_tr_b16 v[64:65], v6 offset:9216\n" \
    "ds_read_b64_tr_b16 v[66:67], v6 offset:10240\n" \
    "v_mfma_f32_16x16x32_bf16 v[164:167], v[40:43], v[28:31], v[164:167]\n" \
    "v_mfma_f32_16x16x32_bf16 v[168:171], v[44:47], v[28:31], v[168:171]\n" \
    "v_mfma_f32_16x16x32_bf16 v[172:175], v[48:51], v[28:31], v[172:175]\n" \
    "s_waitcnt lgkmcnt(0)\n" \
    "v_mfma_f32_16x16x32_bf16 v[96:99], v[68:71], v[52:55], v[96:99]\n" \
    "v_mfma_f32_16x16x32_bf16 v[100:103], v[72:75], v[52:55], v[100:103]\n" \
    "v_mfma_f32_16x16x32_bf16 v[104:107], v[76:79], v[52:55], v[104:107]\n" \
    "v_mfma_f32_16x16x32_bf16 v[108:111], v[80:83], v[52:55], v[108:111]\n" \
    "v_mfma_f32_16x16x32_bf16 v[112:115], v[84:87], v[52:55], v[112:115]\n" \
    "v_mfma_f32_16x16x32_bf16 v[116:119], v[68:71], v[56:59], v[116:119]\n" \
    "v_mfma_f32_16x16x32_bf16 v[120:123], v[72:75], v[56:59], v[120:123]\n" \
    "v_mfma_f32_16x16x32_bf16 v[124:127], v[76:79], v[56:59], v[124:127]\n" \
    "s_waitcnt vmcnt(9)\n" \
    "v_mfma_f32_16x16x32_bf16 v[128:131], v[80:83], v[56:59], v[128:131]\n" \
    "ds_read_b64_tr_b16 v[32:33], v7 offset:22528\n" \
    "ds_read_b64_tr_b16 v[34:35], v7 offset:23808\n" \
    "v_mfma_f32_16x16x32_bf16 v[132:135], v[84:87], v[56:59], v[132:135]\n" \
    "ds_read_b64_tr_b16 v[36:37], v7 offset:22560\n" \
    "ds_read_b64_tr_b16 v[38:39], v7 offset:23840\n" \
    "v_mfma_f32_16x16x32_bf16 v[136:139], v[68:71], v[60:63], v[136:139]\n" \
    "ds_read_b64_tr_b16 v[40:41], v7 offset:22592\n" \
    "ds_read_b64_tr_b16 v[42:43], v7 offset:23872\n" \
    "v_mfma_f32_16x16x32_bf16 v[140:143], v[72:75], v[60:63], v[140:143]\n" \
    "ds_read_b64_tr_b16 v[44:45], v7 offset:22624\n" \
    "ds_read_b64_tr_b16 v[46:47], v7 offset:23904\n" \
    "v_mfma_f32_16x16x32_bf16 v[144:147], v[76:79], v[60:63], v[144:147]\n" \
    "ds_read_b64_tr_b16 v[48:49], v7 offset:22656\n" \
    "ds_read_b64_tr_b16 v[50:51], v7 offset:23936\n" \
    "v_mfma_f32_16x16x32_bf16 v[148:151], v[80:83], v[60:63], v[148:151]\n" \
    "ds_read_b64_tr_b16 v[16:17], v3 offset:18432\n" \
    "ds_read_b64_tr_b16 v[18:19], v3 offset:19456\n" \
    "v_mfma_f32_16x16x32_bf16 v[152:155], v[84:87], v[60:63], v[152:155]\n" \
    "ds_read_b64_tr_b16 v[20:21], v4 offset:18432\n" \
    "ds_read_b64_tr_b16 v[22:23], v4 offset:19456\n" \
    "s_waitcnt lgkmcnt(10)\n" \
    "v_mfma_f32_16x16x32_bf16 v[156:159], v[68:71], v[64:67], v[156:159]\n" \
    "ds_read_b64_tr_b16 v[24:25], v5 offset:18432\n" \
    "ds_read_b64_tr_b16 v[26:27], v5 offset:19456\n" \
    "v_mfma_f32_16x16x32_bf16 v[160:163], v[72:75], v[64:67], v[160:163]\n" \
    "ds_read_b64_tr_b16 v[28:29], v6 offset:18432\n" \
    "ds_read_b64_tr_b16 v[30:31], v6 offset:19456\n" \
    "v_mfma_f32_16x16x32_bf16 v[164:167], v[76:79], v[64:67], v[164:167]\n" \
    "v_mfma_f32_16x16x32_bf16 v[168:171], v[80:83], v[64:67], v[168:171]\n" \
    "v_mfma_f32_16x16x32_bf16 v[172:175], v[84:87], v[64:67], v[172:175]\n" \
    "s_waitcnt lgkmcnt(0)\n" \
    "v_mfma_f32_16x16x32_bf16 v[96:99], v[32:35], v[16:19], v[96:99]\n" \
    "v_mfma_f32_16x16x32_bf16 v[100:103], v[36:39], v[16:19], v[100:103]\n" \
    "v_mfma_f32_16x16x32_bf16 v[104:107], v[40:43], v[16:19], v[104:107]\n" \
    "v_mfma_f32_16x16x32_bf16 v[108:111], v[44:47], v[16:19], v[108:111]\n" \
    "v_mfma_f32_16x16x32_bf16 v[112:115], v[48:51], v[16:19], v[112:115]\n" \
    "v_mfma_f32_16x16x32_bf16 v[116:119], v[32:35], v[20:23], v[116:119]\n" \
    "v_mfma_f32_16x16x32_bf16 v[120:123], v[36:39], v[20:23], v[120:123]\n" \
    "v_mfma_f32_16x16x32_bf16 v[124:127], v[40:43], v[20:23], v[124:127]\n" \
    "s_waitcnt vmcnt(0)\n" \
    "v_mfma_f32_16x16x32_bf16 v[128:131], v[44:47], v[20:23], v[128:131]\n" \
    "ds_read_b64_tr_b16 v[68:69], v7 offset:31744\n" \
    "ds_read_b64_tr_b16 v[70:71], v7 offset:33024\n" \
    "v_mfma_f32_16x16x32_bf16 v[132:135], v[48:51], v[20:23], v[132:135]\n" \
    "ds_read_b64_tr_b16 v[72:73], v7 offset:31776\n" \
    "ds_read_b64_tr_b16 v[74:75], v7 offset:33056\n" \
    "v_mfma_f32_16x16x32_bf16 v[136:139], v[32:35], v[24:27], v[136:139]\n" \
    "ds_read_b64_tr_b16 v[76:77], v7 offset:31808\n" \
    "ds_read_b64_tr_b16 v[78:79], v7 offset:33088\n" \
    "v_mfma_f32_16x16x32_bf16 v[140:143], v[36:39], v[24:27], v[140:143]\n" \
    "ds_read_b64_tr_b16 v[80:81], v7 offset:31840\n" \
    "ds_read_b64_tr_b16 v[82:83], v7 offset:33120\n" \
    "v_mfma_f32_16x16x32_bf16 v[144:147], v[40:43], v[24:27], v[144:147]\n" \
    "ds_read_b64_tr_b16 v[84:85], v7 offset:31872\n" \
    "ds_read_b64_tr_b16 v[86:87], v7 offset:33152\n" \
    "v_mfma_f32_16x16x32_bf16 v[148:151], v[44:47], v[24:27], v[148:151]\n" \
    "ds_read_b64_tr_b16 v[52:53], v3 offset:27648\n" \
    "ds_read_b64_tr_b16 v[54:55], v3 offset:28672\n" \
    "v_mfma_f32_16x16x32_bf16 v[152:155], v[48:51], v[24:27], v[152:155]\n" \
    "ds_read_b64_tr_b16 v[56:57], v4 offset:27648\n" \
    "ds_read_b64_tr_b16 v[58:59], v4 offset:28672\n" \
    "s_waitcnt lgkmcnt(10)\n" \
    "v_mfma_f32_16x16x32_bf16 v[156:159], v[32:35], v[28:31], v[156:159]\n" \
    "ds_read_b64_tr_b16 v[60:61], v5 offset:27648\n" \
    "ds_read_b64_tr_b16 v[62:63], v5 offset:28672\n" \
    "v_mfma_f32_16x16x32_bf16 v[160:163], v[36:39], v[28:31], v[160:163]\n" \
    "ds_read_b64_tr_b16 v[64:65], v6 offset:27648\n" \
    "ds_read_b64_tr_b16 v[66:67], v6 offset:28672\n" \
    "v_mfma_f32_16x16x32_bf16 v[164:167], v[40:43], v[28:31], v[164:167]\n" \
    "v_mfma_f32_16x16x32_bf16 v[168:171], v[44:47], v[28:31], v[168:171]\n" \
    "v_mfma_f32_16x16x32_bf16 v[172:175], v[48:51], v[28:31], v[172:175]\n" \
    "s_waitcnt lgkmcnt(0)\n" \
    "v_mfma_f32_16x16x32_bf16 v[96:99], v[68:71], v[52:55], v[96:99]\n" \
    "v_mfma_f32_16x16x32_bf16 v[100:103], v[72:75], v[52:55], v[100:103]\n" \
    "v_mfma_f32_16x16x32_bf16 v[104:107], v[76:79], v[52:55], v[104:107]\n" \
    "v_mfma_f32_16x16x32_bf16 v[108:111], v[80:83], v[52:55], v[108:111]\n" \
    "v_mfma_f32_16x16x32_bf16 v[112:115], v[84:87], v[52:55], v[112:115]\n" \
    "v_mfma_f32_16x16x32_bf16 v[116:119], v[68:71], v[56:59], v[116:119]\n" \
    "v_mfma_f32_16x16x32_bf16 v[120:123], v[72:75], v[56:59], v[120:123]\n" \
    "v_mfma_f32_16x16x32_bf16 v[124:127], v[76:79], v[56:59], v[124:127]\n" \
    "v_mfma_f32_16x16x32_bf16 v[128:131], v[80:83], v[56:59], v[128:131]\n" \
    "v_mfma_f32_16x16x32_bf16 v[132:135], v[84:87], v[56:59], v[132:135]\n" \
    "v_mfma_f32_16x16x32_bf16 v[136:139], v[68:71], v[60:63], v[136:139]\n" \
    "v_mfma_f32_16x16x32_bf16 v[140:143], v[72:75], v[60:63], v[140:143]\n" \
    "v_mfma_f32_16x16x32_bf16 v[144:147], v[76:79], v[60:63], v[144:147]\n" \
    "v_mfma_f32_16x16x32_bf16 v[148:151], v[80:83], v[60:63], v[148:151]\n" \
    "v_mfma_f32_16x16x32_bf16 v[152:155], v[84:87], v[60:63], v[152:155]\n" \
    "v_mfma_f32_16x16x32_bf16 v[156:159], v[68:71], v[64:67], v[156:159]\n" \
    "v_mfma_f32_16x16x32_bf16 v[160:163], v[72:75], v[64:67], v[160:163]\n" \
    "v_mfma_f32_16x16x32_bf16 v[164:167], v[76:79], v[64:67], v[164:167]\n" \
    "v_mfma_f32_16x16x32_bf16 v[168:171], v[80:83], v[64:67], v[168:171]\n" \
    "v_mfma_f32_16x16x32_bf16 v[172:175], v[84:87], v[64:67], v[172:175]\n" \
    "s_branch GEND%=\n" \
    "GT4%=:\n" \
    "s_cmp_lg_u32 s52, 5\n" \
    "s_cbranch_scc1 GT5%=\n" \
    "s_waitcnt lgkmcnt(0)\n" \
    "v_mfma_f32_16x16x32_bf16 v[96:99], v[32:35], v[16:19], v[96:99]\n" \
    "s_add_u32 m0, s51, 27648\n" \
    "s_nop 0\n" \
    "global_load_lds_dwordx4 v1, s[42:43]\n" \
    "s_add_u32 s42, s42, s46\n" \
    "s_addc_u32 s43, s43, 0\n" \
    "v_mfma_f32_16x16x32_bf16 v[100:103], v[36:39], v[16:19], v[100:103]\n" \
    "s_add_u32 m0, s51, 28672\n" \
    "s_nop 0\n" \
    "global_load_lds_dwordx4 v1, s[42:43]\n" \
    "s_add_u32 s42, s42, s46\n" \
    "s_addc_u32 s43, s43, 0\n" \
    "v_mfma_f32_16x16x32_bf16 v[104:107], v[40:43], v[16:19], v[104:107]\n" \
    "s_add_u32 m0, s51, 29696\n" \
    "s_nop 0\n" \
    "global_load_lds_dwordx4 v1, s[42:43]\n" \
    "s_add_u32 s42, s42, s46\n" \
    "s_addc_u32 s43, s43, 0\n" \
    "v_mfma_f32_16x16x32_bf16 v[108:111], v[44:47], v[16:19], v[108:111]\n" \
    "s_add_u32 m0, s51, 30720\n" \
    "s_nop 0\n" \
    "global_load_lds_dwordx4 v1, s[42:43]\n" \
    "s_add_u32 s42, s42, s46\n" \
    "s_addc_u32 s43, s43, 0\n" \
    "s_mul_i32 s50, s46, 12\n" \
    "s_add_u32 s42, s42, s50\n" \
    "s_addc_u32 s43, s43, 0\n" \
    "v_mfma_f32_16x16x32_bf16 v[112:115], v[48:51], v[16:19], v[112:115]\n" \
    "s_add_u32 m0, s51, 31744\n" \
    "s_nop 0\n" \
    "global_load_lds_dwordx4 v2, s[44:45] offset:0\n" \
    "global_load_lds_dwordx4 v2, s[44:45] offset:1024\n" \
    "v_mfma_f32_16x16x32_bf16 v[116:119], v[32:35], v[20:23], v[116:119]\n" \
    "global_load_lds_dwordx4 v2, s[44:45] offset:2048\n" \
    "global_load_lds_dwordx4 v2, s[44:45] offset:3072\n" \
    "v_mfma_f32_16x16x32_bf16 v[120:123], v[36:39], v[20:23], v[120:123]\n" \
    "s_add_u32 m0, s51, 35840\n" \
    "s_add_u32 s44, s44, 4096\n" \
    "s_addc_u32 s45, s45, 0\n" \
    "global_load_lds_dwordx4 v2, s[44:45]\n" \
    "s_add_u32 s44, s44, 16384\n" \
    "s_addc_u32 s45, s45, 0\n" \
    "v_mfma_f32_16x16x32_bf16 v[124:127], v[40:43], v[20:23], v[124:127]\n" \
    "s_waitcnt vmcnt(18)\n" \
    "v_mfma_f32_16x16x32_bf16 v[128:131], v[44:47], v[20:23], v[128:131]\n" \
    "ds_read_b64_tr_b16 v[68:69], v7 offset:13312\n" \
    "ds_read_b64_tr_b16 v[70:71], v7 offset:14592\n" \
    "v_mfma_f32_16x16x32_bf16 v[132:135], v[48:51], v[20:23], v[132:135]\n" \
    "ds_read_b64_tr_b16 v[72:73], v7 offset:13344\n" \
    "ds_read_b64_tr_b16 v[74:75], v7 offset:14624\n" \
    "v_mfma_f32_16x16x32_bf16 v[136:139], v[32:35], v[24:27], v[136:139]\n" \
    "ds_read_b64_tr_b16 v[76:77], v7 offset:13376\n" \
    "ds_read_b64_tr_b16 v[78:79], v7 offset:14656\n" \
    "v_mfma_f32_16x16x32_bf16 v[140:143], v[36:39], v[24:27], v[140:143]\n" \
    "ds_read_b64_tr_b16 v[80:81], v7 offset:13408\n" \
    "ds_read_b64_tr_b16 v[82:83], v7 offset:14688\n" \
    "v_mfma_f32_16x16x32_bf16 v[144:147], v[40:43], v[24:27], v[144:147]\n" \
    "ds_read_b64_tr_b16 v[84:85], v7 offset:13440\n" \
    "ds_read_b64_tr_b16 v[86:87], v7 offset:14720\n" \
    "v_mfma_f32_16x16x32_bf16 v[148:151], v[44:47], v[24:27], v[148:151]\n" \
    "ds_read_b64_tr_b16 v[52:53], v3 offset:9216\n" \
    "ds_read_b64_tr_b16 v[54:55], v3 offset:10240\n" \
    "v_mfma_f32_16x16x32_bf16 v[152:155], v[48:51], v[24:27], v[152:155]\n" \
    "ds_read_b64_tr_b16 v[56:57], v4 offset:9216\n" \
    "ds_read_b64_tr_b16 v[58:59], v4 offset:10240\n" \
    "s_waitcnt lgkmcnt(10)\n" \
    "v_mfma_f32_16x16x32_bf16 v[156:159], v[32:35], v[28:31], v[156:159]\n" \
    "ds_read_b64_tr_b16 v[60:61], v5 offset:9216\n" \
    "ds_read_b64_tr_b16 v[62:63], v5 offset:10240\n" \
    "v_mfma_f32_16x16x32_bf16 v[160:163], v[36:39], v[28:31], v[160:163]\n" \
    "ds_read_b64_tr_b16 v[64:65], v6 offset:9216\n" \
    "ds_read_b64_tr_b16 v[66:67], v6 offset:10240\n" \
    "v_mfma_f32_16x16x32_bf16 v[164:167], v[40:43], v[28:31], v[164:167]\n" \
    "v_mfma_f32_16x16x32_bf16 v[168:171], v[44:47], v[28:31], v[168:171]\n" \
    "v_mfma_f32_16x16x32_bf16 v[172:175], v[48:51], v[28:31], v[172:175]\n" \
    "s_waitcnt lgkmcnt(0)\n" \
    "v_mfma_f32_16x16x32_bf16 v[96:99], v[68:71], v[52:55], v[96:99]\n" \
    "s_add_u32 m0, s51, 0\n" \
    "s_nop 0\n" \
    "global_load_lds_dwordx4 v1, s[42:43]\n" \
    "s_add_u32 s42, s42, s46\n" \
    "s_addc_u32 s43, s43, 0\n" \
    "v_mfma_f32_16x16x32_bf16 v[100:103], v[72:75], v[52:55], v[100:103]\n" \
    "s_add_u32 m0, s51, 1024\n" \
    "s_nop 0\n" \
    "global_load_lds_dwordx4 v1, s[42:43]\n" \
    "s_add_u32 s42, s42, s46\n" \
    "s_addc_u32 s43, s43, 0\n" \
    "v_mfma_f32_16x16x32_bf16 v[104:107], v[76:79], v[52:55], v[104:107]\n" \
    "s_add_u32 m0, s51, 2048\n" \
    "s_nop 0\n" \
    "global_load_lds_dwordx4 v1, s[42:43]\n" \
    "s_add_u32 s42, s42, s46\n" \
    "s_addc_u32 s43, s43, 0\n" \
    "v_mfma_f32_16x16x32_bf16 v[108:111], v[80:83], v[52:55], v[108:111]\n" \
    "s_add_u32 m0, s51, 3072\n" \
    "s_nop 0\n" \
    "global_load_lds_dwordx4 v1, s[42:43]\n" \
    "s_add_u32 s42, s42, s46\n" \
    "s_addc_u32 s43, s43, 0\n" \
    "s_mul_i32 s50, s46, 12\n" \
    "s_add_u32 s42, s42, s50\n" \
    "s_addc_u32 s43, s43, 0\n" \
    "v_mfma_f32_16x16x32_bf16 v[112:115], v[84:87], v[52:55], v[112:115]\n" \
    "s_add_u32 m0, s51, 4096\n" \
    "s_nop 0\n" \
    "global_load_lds_dwordx4 v2, s[44:45] offset:0\n" \
    "global_load_lds_dwordx4 v2, s[44:45] offset:1024\n" \
    "v_mfma_f32_16x16x32_bf16 v[116:119], v[68:71], v[56:59], v[116:119]\n" \
    "global_load_lds_dwordx4 v2, s[44:45] offset:2048\n" \
    "global_load_lds_dwordx4 v2, s[44:45] offset:3072\n" \
    "v_mfma_f32_16x16x32_bf16 v[120:123], v[72:75], v[56:59], v[120:123]\n" \
    "s_add_u32 m0, s51, 8192\n" \
    "s_add_u32 s44, s44, 4096\n" \
    "s_addc_u32 s45, s45, 0\n" \
    "global_load_lds_dwordx4 v2, s[44:45]\n" \
    "s_add_u32 s44, s44, 16384\n" \
    "s_addc_u32 s45, s45, 0\n" \
    "v_mfma_f32_16x16x32_bf16 v[124:127], v[76:79], v[56:59], v[124:127]\n" \
    "s_waitcnt vmcnt(18)\n" \
    "v_mfma_f32_16x16x32_bf16 v[128:131], v[80:83], v[56:59], v[128:131]\n" \
    "ds_read_b64_tr_b16 v[32:33], v7 offset:22528\n" \
    "ds_read_b64_tr_b16 v[34:35], v7 offset:23808\n" \
    "v_mfma_f32_16x16x32_bf16 v[132:135], v[84:87], v[56:59], v[132:135]\n" \
    "ds_read_b64_tr_b16 v[36:37], v7 offset:22560\n" \
    "ds_read_b64_tr_b16 v[38:39], v7 offset:23840\n" \
    "v_mfma_f32_16x16x32_bf16 v[136:139], v[68:71], v[60:63], v[136:139]\n" \
    "ds_read_b64_tr_b16 v[40:41], v7 offset:22592\n" \
    "ds_read_b64_tr_b16 v[42:43], v7 offset:23872\n" \
    "v_mfma_f32_16x16x32_bf16 v[140:143], v[72:75], v[60:63], v[140:143]\n" \
    "ds_read_b64_tr_b16 v[44:45], v7 offset:22624\n" \
    "ds_read_b64_tr_b16 v[46:47], v7 offset:23904\n" \
    "v_mfma_f32_16x16x32_bf16 v[144:147], v[76:79], v[60:63], v[144:147]\n" \
    "ds_read_b64_tr_b16 v[48:49], v7 offset:22656\n" \
    "ds_read_b64_tr_b16 v[50:51], v7 offset:23936\n" \
    "v_mfma_f32_16x16x32_bf16 v[148:151], v[80:83], v[60:63], v[148:151]\n" \
    "ds_read_b64_tr_b16 v[16:17], v3 offset:18432\n" \
    "ds_read_b64_tr_b16 v[18:19], v3 offset:19456\n" \
    "v_mfma_f32_16x16x32_bf16 v[152:155], v[84:87], v[60:63], v[152:155]\n" \
    "ds_read_b64_tr_b16 v[20:21], v4 offset:18432\n" \
    "ds_read_b64_tr_b16 v[22:23], v4 offset:19456\n" \
    "s_waitcnt lgkmcnt(10)\n" \
    "v_mfma_f32_16x16x32_bf16 v[156:159], v[68:71], v[64:67], v[156:159]\n" \
    "ds_read_b64_tr_b16 v[24:25], v5 offset:18432\n" \
    "ds_read_b64_tr_b16 v[26:27], v5 offset:19456\n" \
    "v_mfma_f32_16x16x32_bf16 v[160:163], v[72:75], v[64:67], v[160:163]\n" \
    "ds_read_b64_tr_b16 v[28:29], v6 offset:18432\n" \
    "ds_read_b64_tr_b16 v[30:31], v6 offset:19456\n" \
    "v_mfma_f32_16x16x32_bf16 v[164:167], v[76:79], v[64:67], v[164:167]\n" \
    "v_mfma_f32_16x16x32_bf16 v[168:171], v[80:83], v[64:67], v[168:171]\n" \
    "v_mfma_f32_16x16x32_bf16 v[172:175], v[84:87], v[64:67], v[172:175]\n" \
    "s_waitcnt lgkmcnt(0)\n" \
    "v_mfma_f32_16x16x32_bf16 v[96:99], v[32:35], v[16:19], v[96:99]\n" \
    "v_mfma_f32_16x16x32_bf16 v[100:103], v[36:39], v[16:19], v[100:103]\n" \
    "v_mfma_f32_16x16x32_bf16 v[104:107], v[40:43], v[16:19], v[104:107]\n" \
    "v_mfma_f32_16x16x32_bf16 v[108:111], v[44:47], v[16:19], v[108:111]\n" \
    "v_mfma_f32_16x16x32_bf16 v[112:115], v[48:51], v[16:19], v[112:115]\n" \
    "v_mfma_f32_16x16x32_bf16 v[116:119], v[32:35], v[20:23], v[116:119]\n" \
    "v_mfma_f32_16x16x32_bf16 v[120:123], v[36:39], v[20:23], v[120:123]\n" \
    "v_mfma_f32_16x16x32_bf16 v[124:127], v[40:43], v[20:23], v[124:127]\n" \
    "s_waitcnt vmcnt(9)\n" \
    "v_mfma_f32_16x16x32_bf16 v[128:131], v[44:47], v[20:23], v[128:131]\n" \
    "ds_read_b64_tr_b16 v[68:69], v7 offset:31744\n" \
    "ds_read_b64_tr_b16 v[70:71], v7 offset:33024\n" \
    "v_mfma_f32_16x16x32_bf16 v[132:135], v[48:51], v[20:23], v[132:135]\n" \
    "ds_read_b64_tr_b16 v[72:73], v7 offset:31776\n" \
    "ds_read_b64_tr_b16 v[74:75], v7 offset:33056\n" \
    "v_mfma_f32_16x16x32_bf16 v[136:139], v[32:35], v[24:27], v[136:139]\n" \
    "ds_read_b64_tr_b16 v[76:77], v7 offset:31808\n" \
    "ds_read_b64_tr_b16 v[78:79], v7 offset:33088\n" \
    "v_mfma_f32_16x16x32_bf16 v[140:143], v[36:39], v[24:27], v[140:143]\n" \
    "ds_read_b64_tr_b16 v[80:81], v7 offset:31840\n" \
    "ds_read_b64_tr_b16 v[82:83], v7 offset:33120\n" \
    "v_mfma_f32_16x16x32_bf16 v[144:147], v[40:43], v[24:27], v[144:147]\n" \
    "ds_read_b64_tr_b16 v[84:85], v7 offset:31872\n" \
    "ds_read_b64_tr_b16 v[86:87], v7 offset:33152\n" \
    "v_mfma_f32_16x16x32_bf16 v[148:151], v[44:47], v[24:27], v[148:151]\n" \
    "ds_read_b64_tr_b16 v[52:53], v3 offset:27648\n" \
    "ds_read_b64_tr_b16 v[54:55], v3 offset:28672\n" \
    "v_mfma_f32_16x16x32_bf16 v[152:155], v[48:51], v[24:27], v[152:155]\n" \
    "ds_read_b64_tr_b16 v[56:57], v4 offset:27648\n" \
    "ds_read_b64_tr_b16 v[58:59], v4 offset:28672\n" \
    "s_waitcnt lgkmcnt(10)\n" \
    "v_mfma_f32_16x16x32_bf16 v[156:159], v[32:35], v[28:31], v[156:159]\n" \
    "ds_read_b64_tr_b16 v[60:61], v5 offset:27648\n" \
    "ds_read_b64_tr_b16 v[62:63], v5 offset:28672\n" \
    "v_mfma_f32_16x16x32_bf16 v[160:163], v[36:39], v[28:31], v[160:163]\n" \
    "ds_read_b64_tr_b16 v[64:65], v6 offset:27648\n" \
    "ds_read_b64_tr_b16 v[66:67], v6 offset:28672\n" \
    "v_mfma_f32_16x16x32_bf16 v[164:167], v[40:43], v[28:31], v[164:167]\n" \
    "v_mfma_f32_16x16x32_bf16 v[168:171], v[44:47], v[28:31], v[168:171]\n" \
    "v_mfma_f32_16x16x32_bf16 v[172:175], v[48:51], v[28:31], v[172:175]\n" \
    "s_waitcnt lgkmcnt(0)\n" \
    "v_mfma_f32_16x16x32_bf16 v[96:99], v[68:71], v[52:55], v[96:99]\n" \
    "v_mfma_f32_16x16x32_bf16 v[100:103], v[72:75], v[52:55], v[100:103]\n" \
    "v_mfma_f32_16x16x32_bf16 v[104:107], v[76:79], v[52:55], v[104:107]\n" \
    "v_mfma_f32_16x16x32_bf16 v[108:111], v[80:83], v[52:55], v[108:111]\n" \
    "v_mfma_f32_16x16x32_bf16 v[112:115], v[84:87], v[52:55], v[112:115]\n" \
    "v_mfma_f32_16x16x32_bf16 v[116:119], v[68:71], v[56:59], v[116:119]\n" \
    "v_mfma_f32_16x16x32_bf16 v[120:123], v[72:75], v[56:59], v[120:123]\n" \
    "v_mfma_f32_16x16x32_bf16 v[124:127], v[76:79], v[56:59], v[124:127]\n" \
    "s_waitcnt vmcnt(0)\n" \
    "v_mfma_f32_16x16x32_bf16 v[128:131], v[80:83], v[56:59], v[128:131]\n" \
    "ds_read_b64_tr_b16 v[32:33], v7 offset:4096\n" \
    "ds_read_b64_tr_b16 v[34:35], v7 offset:5376\n" \
    "v_mfma_f32_16x16x32_bf16 v[132:135], v[84:87], v[56:59], v[132:135]\n" \
    "ds_read_b64_tr_b16 v[36:37], v7 offset:4128\n" \
    "ds_read_b64_tr_b16 v[38:39], v7 offset:5408\n" \
    "v_mfma_f32_16x16x32_bf16 v[136:139], v[68:71], v[60:63], v[136:139]\n" \
    "ds_read_b64_tr_b16 v[40:41], v7 offset:4160\n" \
    "ds_read_b64_tr_b16 v[42:43], v7 offset:5440\n" \
    "v_mfma_f32_16x16x32_bf16 v[140:143], v[72:75], v[60:63], v[140:143]\n" \
    "ds_read_b64_tr_b16 v[44:45], v7 offset:4192\n" \
    "ds_read_b64_tr_b16 v[46:47], v7 offset:5472\n" \
    "v_mfma_f32_16x16x32_bf16 v[144:147], v[76:79], v[60:63], v[144:147]\n" \
    "ds_read_b64_tr_b16 v[48:49], v7 offset:4224\n" \
    "ds_read_b64_tr_b16 v[50:51], v7 offset:5504\n" \
    "v_mfma_f32_16x16x32_bf16 v[148:151], v[80:83], v[60:63], v[148:151]\n" \
    "ds_read_b64_tr_b16 v[16:17], v3 offset:0\n" \
    "ds_read_b64_tr_b16 v[18:19], v3 offset:1024\n" \
    "v_mfma_f32_16x16x32_bf16 v[152:155], v[84:87], v[60:63], v[152:155]\n" \
    "ds_read_b64_tr_b16 v[20:21], v4 offset:0\n" \
    "ds_read_b64_tr_b16 v[22:23], v4 offset:1024\n" \
    "s_waitcnt lgkmcnt(10)\n" \
    "v_mfma_f32_16x16x32_bf16 v[156:159], v[68:71], v[64:67], v[156:159]\n" \
    "ds_read_b64_tr_b16 v[24:25], v5 offset:0\n" \
    "ds_read_b64_tr_b16 v[26:27], v5 offset:1024\n" \
    "v_mfma_f32_16x16x32_bf16 v[160:163], v[72:75], v[64:67], v[160:163]\n" \
    "ds_read_b64_tr_b16 v[28:29], v6 offset:0\n" \
    "ds_read_b64_tr_b16 v[30:31], v6 offset:1024\n" \
    "v_mfma_f32_16x16x32_bf16 v[164:167], v[76:79], v[64:67], v[164:167]\n" \
    "v_mfma_f32_16x16x32_bf16 v[168:171], v[80:83], v[64:67], v[168:171]\n" \
    "v_mfma_f32_16x16x32_bf16 v[172:175], v[84:87], v[64:67], v[172:175]\n" \
    "s_waitcnt lgkmcnt(0)\n" \
    "v_mfma_f32_16x16x32_bf16 v[96:99], v[32:35], v[16:19], v[96:99]\n" \
    "v_mfma_f32_16x16x32_bf16 v[100:103], v[36:39], v[16:19], v[100:103]\n" \
    "v_mfma_f32_16x16x32_bf16 v[104:107], v[40:43], v[16:19], v[104:107]\n" \
    "v_mfma_f32_16x16x32_bf16 v[108:111], v[44:47], v[16:19], v[108:111]\n" \
    "v_mfma_f32_16x16x32_bf16 v[112:115], v[48:51], v[16:19], v[112:115]\n" \
    "v_mfma_f32_16x16x32_bf16 v[116:119], v[32:35], v[20:23], v[116:119]\n" \
    "v_mfma_f32_16x16x32_bf16 v[120:123], v[36:39], v[20:23], v[120:123]\n" \
    "v_mfma_f32_16x16x32_bf16 v[124:127], v[40:43], v[20:23], v[124:127]\n" \
    "v_mfma_f32_16x16x32_bf16 v[128:131], v[44:47], v[20:23], v[128:131]\n" \
    "v_mfma_f32_16x16x32_bf16 v[132:135], v[48:51], v[20:23], v[132:135]\n" \
    "v_mfma_f32_16x16x32_bf16 v[136:139], v[32:35], v[24:27], v[136:139]\n" \
    "v_mfma_f32_16x16x32_bf16 v[140:143], v[36:39], v[24:27], v[140:143]\n" \
    "v_mfma_f32_16x16x32_bf16 v[144:147], v[40:43], v[24:27], v[144:147]\n" \
    "v_mfma_f32_16x16x32_bf16 v[148:151], v[44:47], v[24:27], v[148:151]\n" \
    "v_mfma_f32_16x16x32_bf16 v[152:155], v[48:51], v[24:27], v[152:155]\n" \
    "v_mfma_f32_16x16x32_bf16 v[156:159], v[32:35], v[28:31], v[156:159]\n" \
    "v_mfma_f32_16x16x32_bf16 v[160:163], v[36:39], v[28:31], v[160:163]\n" \
    "v_mfma_f32_16x16x32_bf16 v[164:167], v[40:43], v[28:31], v[164:167]\n" \
    "v_mfma_f32_16x16x32_bf16 v[168:171], v[44:47], v[28:31], v[168:171]\n" \
    "v_mfma_f32_16x16x32_bf16 v[172:175], v[48:51], v[28:31], v[172:175]\n" \
    "s_branch GEND%=\n" \
    "GT5%=:\n" \
    "s_waitcnt lgkmcnt(0)\n" \
    "v_mfma_f32_16x16x32_bf16 v[96:99], v[32:35], v[16:19], v[96:99]\n" \
    "s_add_u32 m0, s51, 27648\n" \
    "s_nop 0\n" \
    "global_load_lds_dwordx4 v1, s[42:43]\n" \
    "s_add_u32 s42, s42, s46\n" \
    "s_addc_u32 s43, s43, 0\n" \
    "v_mfma_f32_16x16x32_bf16 v[100:103], v[36:39], v[16:19], v[100:103]\n" \
    "s_add_u32 m0, s51, 28672\n" \
    "s_nop 0\n" \
    "global_load_lds_dwordx4 v1, s[42:43]\n" \
    "s_add_u32 s42, s42, s46\n" \
    "s_addc_u32 s43, s43, 0\n" \
    "v_mfma_f32_16x16x32_bf16 v[104:107], v[40:43], v[16:19], v[104:107]\n" \
    "s_add_u32 m0, s51, 29696\n" \
    "s_nop 0\n" \
    "global_load_lds_dwordx4 v1, s[42:43]\n" \
    "s_add_u32 s42, s42, s46\n" \
    "s_addc_u32 s43, s43, 0\n" \
    "v_mfma_f32_16x16x32_bf16 v[108:111], v[44:47], v[16:19], v[108:111]\n" \
    "s_add_u32 m0, s51, 30720\n" \
    "s_nop 0\n" \
    "global_load_lds_dwordx4 v1, s[42:43]\n" \
    "s_add_u32 s42, s42, s46\n" \
    "s_addc_u32 s43, s43, 0\n" \
    "s_mul_i32 s50, s46, 12\n" \
    "s_add_u32 s42, s42, s50\n" \
    "s_addc_u32 s43, s43, 0\n" \
    "v_mfma_f32_16x16x32_bf16 v[112:115], v[48:51], v[16:19], v[112:115]\n" \
    "s_add_u32 m0, s51, 31744\n" \
    "s_nop 0\n" \
    "global_load_lds_dwordx4 v2, s[44:45] offset:0\n" \
    "global_load_lds_dwordx4 v2, s[44:45] offset:1024\n" \
    "v_mfma_f32_16x16x32_bf16 v[116:119], v[32:35], v[20:23], v[116:119]\n" \
    "global_load_lds_dwordx4 v2, s[44:45] offset:2048\n" \
    "global_load_lds_dwordx4 v2, s[44:45] offset:3072\n" \
    "v_mfma_f32_16x16x32_bf16 v[120:123], v[36:39], v[20:23], v[120:123]\n" \
    "s_add_u32 m0, s51, 35840\n" \
    "s_add_u32 s44, s44, 4096\n" \
    "s_addc_u32 s45, s45, 0\n" \
    "global_load_lds_dwordx4 v2, s[44:45]\n" \
    "s_add_u32 s44, s44, 16384\n" \
    "s_addc_u32 s45, s45, 0\n" \
    "v_mfma_f32_16x16x32_bf16 v[124:127], v[40:43], v[20:23], v[124:127]\n" \
    "s_waitcnt vmcnt(18)\n" \
    "v_mfma_f32_16x16x32_bf16 v[128:131], v[44:47], v[20:23], v[128:131]\n" \
    "ds_read_b64_tr_b16 v[68:69], v7 offset:13312\n" \
    "ds_read_b64_tr_b16 v[70:71], v7 offset:14592\n" \
    "v_mfma_f32_16x16x32_bf16 v[132:135], v[48:51], v[20:23], v[132:135]\n" \
    "ds_read_b64_tr_b16 v[72:73], v7 offset:13344\n" \
    "ds_read_b64_tr_b16 v[74:75], v7 offset:14624\n" \
    "v_mfma_f32_16x16x32_bf16 v[136:139], v[32:35], v[24:27], v[136:139]\n" \
    "ds_read_b64_tr_b16 v[76:77], v7 offset:13376\n" \
    "ds_read_b64_tr_b16 v[78:79], v7 offset:14656\n" \
    "v_mfma_f32_16x16x32_bf16 v[140:143], v[36:39], v[24:27], v[140:143]\n" \
    "ds_read_b64_tr_b16 v[80:81], v7 offset:13408\n" \
    "ds_read_b64_tr_b16 v[82:83], v7 offset:14688\n" \
    "v_mfma_f32_16x16x32_bf16 v[144:147], v[40:43], v[24:27], v[144:147]\n" \
    "ds_read_b64_tr_b16 v[84:85], v7 offset:13440\n" \
    "ds_read_b64_tr_b16 v[86:87], v7 offset:14720\n" \
    "v_mfma_f32_16x16x32_bf16 v[148:151], v[44:47], v[24:27], v[148:151]\n" \
    "ds_read_b64_tr_b16 v[52:53], v3 offset:9216\n" \
    "ds_read_b64_tr_b16 v[54:55], v3 offset:10240\n" \
    "v_mfma_f32_16x16x32_bf16 v[152:155], v[48:51], v[24:27], v[152:155]\n" \
    "ds_read_b64_tr_b16 v[56:57], v4 offset:9216\n" \
    "ds_read_b64_tr_b16 v[58:59], v4 offset:10240\n" \
    "s_waitcnt lgkmcnt(10)\n" \
    "v_mfma_f32_16x16x32_bf16 v[156:159], v[32:35], v[28:31], v[156:159]\n" \
    "ds_read_b64_tr_b16 v[60:61], v5 offset:9216\n" \
    "ds_read_b64_tr_b16 v[62:63], v5 offset:10240\n" \
    "v_mfma_f32_16x16x32_bf16 v[160:163], v[36:39], v[28:31], v[160:163]\n" \
    "ds_read_b64_tr_b16 v[64:65], v6 offset:9216\n" \
    "ds_read_b64_tr_b16 v[66:67], v6 offset:10240\n" \
    "v_mfma_f32_16x16x32_bf16 v[164:167], v[40:43], v[28:31], v[164:167]\n" \
    "v_mfma_f32_16x16x32_bf16 v[168:171], v[44:47], v[28:31], v[168:171]\n" \
    "v_mfma_f32_16x16x32_bf16 v[172:175], v[48:51], v[28:31], v[172:175]\n" \
    "s_waitcnt lgkmcnt(0)\n" \
    "v_mfma_f32_16x16x32_bf16 v[96:99], v[68:71], v[52:55], v[96:99]\n" \
    "s_add_u32 m0, s51, 0\n" \
    "s_nop 0\n" \
    "global_load_lds_dwordx4 v1, s[42:43]\n" \
    "s_add_u32 s42, s42, s46\n" \
    "s_addc_u32 s43, s43, 0\n" \
    "v_mfma_f32_16x16x32_bf16 v[100:103], v[72:75], v[52:55], v[100:103]\n" \
    "s_add_u32 m0, s51, 1024\n" \
    "s_nop 0\n" \
    "global_load_lds_dwordx4 v1, s[42:43]\n" \
    "s_add_u32 s42, s42, s46\n" \
    "s_addc_u32 s43, s43, 0\n" \
    "v_mfma_f32_16x16x32_bf16 v[104:107], v[76:79], v[52:55], v[104:107]\n" \
    "s_add_u32 m0, s51, 2048\n" \
    "s_nop 0\n" \
    "global_load_lds_dwordx4 v1, s[42:43]\n" \
    "s_add_u32 s42, s42, s46\n" \
    "s_addc_u32 s43, s43, 0\n" \
    "v_mfma_f32_16x16x32_bf16 v[108:111], v[80:83], v[52:55], v[108:111]\n" \
    "s_add_u32 m0, s51, 3072\n" \
    "s_nop 0\n" \
    "global_load_lds_dwordx4 v1, s[42:43]\n" \
    "s_add_u32 s42, s42, s46\n" \
    "s_addc_u32 s43, s43, 0\n" \
    "s_mul_i32 s50, s46, 12\n" \
    "s_add_u32 s42, s42, s50\n" \
    "s_addc_u32 s43, s43, 0\n" \
    "v_mfma_f32_16x16x32_bf16 v[112:115], v[84:87], v[52:55], v[112:115]\n" \
    "s_add_u32 m0, s51, 4096\n" \
    "s_nop 0\n" \
    "global_load_lds_dwordx4 v2, s[44:45] offset:0\n" \
    "global_load_lds_dwordx4 v2, s[44:45] offset:1024\n" \
    "v_mfma_f32_16x16x32_bf16 v[116:119], v[68:71], v[56:59], v[116:119]\n" \
    "global_load_lds_dwordx4 v2, s[44:45] offset:2048\n" \
    "global_load_lds_dwordx4 v2, s[44:45] offset:3072\n" \
    "v_mfma_f32_16x16x32_bf16 v[120:123], v[72:75], v[56:59], v[120:123]\n" \
    "s_add_u32 m0, s51, 8192\n" \
    "s_add_u32 s44, s44, 4096\n" \
    "s_addc_u32 s45, s45, 0\n" \
    "global_load_lds_dwordx4 v2, s[44:45]\n" \
    "s_add_u32 s44, s44, 16384\n" \
    "s_addc_u32 s45, s45, 0\n" \
    "v_mfma_f32_16x16x32_bf16 v[124:127], v[76:79], v[56:59], v[124:127]\n" \
    "s_waitcnt vmcnt(18)\n" \
    "v_mfma_f32_16x16x32_bf16 v[128:131], v[80:83], v[56:59], v[128:131]\n" \
    "ds_read_b64_tr_b16 v[32:33], v7 offset:22528\n" \
    "ds_read_b64_tr_b16 v[34:35], v7 offset:23808\n" \
    "v_mfma_f32_16x16x32_bf16 v[132:135], v[84:87], v[56:59], v[132:135]\n" \
    "ds_read_b64_tr_b16 v[36:37], v7 offset:22560\n" \
    "ds_read_b64_tr_b16 v[38:39], v7 offset:23840\n" \
    "v_mfma_f32_16x16x32_bf16 v[136:139], v[68:71], v[60:63], v[136:139]\n" \
    "ds_read_b64_tr_b16 v[40:41], v7 offset:22592\n" \
    "ds_read_b64_tr_b16 v[42:43], v7 offset:23872\n" \
    "v_mfma_f32_16x16x32_bf16 v[140:143], v[72:75], v[60:63], v[140:143]\n" \
    "ds_read_b64_tr_b16 v[44:45], v7 offset:22624\n" \
    "ds_read_b64_tr_b16 v[46:47], v7 offset:23904\n" \
    "v_mfma_f32_16x16x32_bf16 v[144:147], v[76:79], v[60:63], v[144:147]\n" \
    "ds_read_b64_tr_b16 v[48:49], v7 offset:22656\n" \
    "ds_read_b64_tr_b16 v[50:51], v7 offset:23936\n" \
    "v_mfma_f32_16x16x32_bf16 v[148:151], v[80:83], v[60:63], v[148:151]\n" \
    "ds_read_b64_tr_b16 v[16:17], v3 offset:18432\n" \
    "ds_read_b64_tr_b16 v[18:19], v3 offset:19456\n" \
    "v_mfma_f32_16x16x32_bf16 v[152:155], v[84:87], v[60:63], v[152:155]\n" \
    "ds_read_b64_tr_b16 v[20:21], v4 offset:18432\n" \
    "ds_read_b64_tr_b16 v[22:23], v4 offset:19456\n" \
    "s_waitcnt lgkmcnt(10)\n" \
    "v_mfma_f32_16x16x32_bf16 v[156:159], v[68:71], v[64:67], v[156:159]\n" \
    "ds_read_b64_tr_b16 v[24:25], v5 offset:18432\n" \
    "ds_read_b64_tr_b16 v[26:27], v5 offset:19456\n" \
    "v_mfma_f32_16x16x32_bf16 v[160:163], v[72:75], v[64:67], v[160:163]\n" \
    "ds_read_b64_tr_b16 v[28:29], v6 offset:18432\n" \
    "ds_read_b64_tr_b16 v[30:31], v6 offset:19456\n" \
    "v_mfma_f32_16x16x32_bf16 v[164:167], v[76:79], v[64:67], v[164:167]\n" \
    "v_mfma_f32_16x16x32_bf16 v[168:171], v[80:83], v[64:67], v[168:171]\n" \
    "v_mfma_f32_16x16x32_bf16 v[172:175], v[84:87], v[64:67], v[172:175]\n" \
    "s_waitcnt lgkmcnt(0)\n" \
    "v_mfma_f32_16x16x32_bf16 v[96:99], v[32:35], v[16:19], v[96:99]\n" \
    "s_add_u32 m0, s51, 9216\n" \
    "s_nop 0\n" \
    "global_load_lds_dwordx4 v1, s[42:43]\n" \
    "s_add_u32 s42, s42, s46\n" \
    "s_addc_u32 s43, s43, 0\n" \
    "v_mfma_f32_16x16x32_bf16 v[100:103], v[36:39], v[16:19], v[100:103]\n" \
    "s_add_u32 m0, s51, 10240\n" \
    "s_nop 0\n" \
    "global_load_lds_dwordx4 v1, s[42:43]\n" \
    "s_add_u32 s42, s42, s46\n" \
    "s_addc_u32 s43, s43, 0\n" \
    "v_mfma_f32_16x16x32_bf16 v[104:107], v[40:43], v[16:19], v[104:107]\n" \
    "s_add_u32 m0, s51, 11264\n" \
    "s_nop 0\n" \
    "global_load_lds_dwordx4 v1, s[42:43]\n" \
    "s_add_u32 s42, s42, s46\n" \
    "s_addc_u32 s43, s43, 0\n" \
    "v_mfma_f32_16x16x32_bf16 v[108:111], v[44:47], v[16:19], v[108:111]\n" \
    "s_add_u32 m0, s51, 12288\n" \
    "s_nop 0\n" \
    "global_load_lds_dwordx4 v1, s[42:43]\n" \
    "s_add_u32 s42, s42, s46\n" \
    "s_addc_u32 s43, s43, 0\n" \
    "s_mul_i32 s50, s46, 12\n" \
    "s_add_u32 s42, s42, s50\n" \
    "s_addc_u32 s43, s43, 0\n" \
    "v_mfma_f32_16x16x32_bf16 v[112:115], v[48:51], v[16:19], v[112:115]\n" \
    "s_add_u32 m0, s51, 13312\n" \
    "s_nop 0\n" \
    "global_load_lds_dwordx4 v2, s[44:45] offset:0\n" \
    "global_load_lds_dwordx4 v2, s[44:45] offset:1024\n" \
    "v_mfma_f32_16x16x32_bf16 v[116:119], v[32:35], v[20:23], v[116:119]\n" \
    "global_load_lds_dwordx4 v2, s[44:45] offset:2048\n" \
    "global_load_lds_dwordx4 v2, s[44:45] offset:3072\n" \
    "v_mfma_f32_16x16x32_bf16 v[120:123], v[36:39], v[20:23], v[120:123]\n" \
    "s_add_u32 m0, s51, 17408\n" \
    "s_add_u32 s44, s44, 4096\n" \
    "s_addc_u32 s45, s45, 0\n" \
    "global_load_lds_dwordx4 v2, s[44:45]\n" \
    "s_add_u32 s44, s44, 16384\n" \
    "s_addc_u32 s45, s45, 0\n" \
    "v_mfma_f32_16x16x32_bf16 v[124:127], v[40:43], v[20:23], v[124:127]\n" \
    "s_waitcnt vmcnt(18)\n" \
    "v_mfma_f32_16x16x32_bf16 v[128:131], v[44:47], v[20:23], v[128:131]\n" \
    "ds_read_b64_tr_b16 v[68:69], v7 offset:31744\n" \
    "ds_read_b64_tr_b16 v[70:71], v7 offset:33024\n" \
    "v_mfma_f32_16x16x32_bf16 v[132:135], v[48:51], v[20:23], v[132:135]\n" \
    "ds_read_b64_tr_b16 v[72:73], v7 offset:31776\n" \
    "ds_read_b64_tr_b16 v[74:75], v7 offset:33056\n" \
    "v_mfma_f32_16x16x32_bf16 v[136:139], v[32:35], v[24:27], v[136:139]\n" \
    "ds_read_b64_tr_b16 v[76:77], v7 offset:31808\n" \
    "ds_read_b64_tr_b16 v[78:79], v7 offset:33088\n" \
    "v_mfma_f32_16x16x32_bf16 v[140:143], v[36:39], v[24:27], v[140:143]\n" \
    "ds_read_b64_tr_b16 v[80:81], v7 offset:31840\n" \
    "ds_read_b64_tr_b16 v[82:83], v7 offset:33120\n" \
    "v_mfma_f32_16x16x32_bf16 v[144:147], v[40:43], v[24:27], v[144:147]\n" \
    "ds_read_b64_tr_b16 v[84:85], v7 offset:31872\n" \
    "ds_read_b64_tr_b16 v[86:87], v7 offset:33152\n" \
    "v_mfma_f32_16x16x32_bf16 v[148:151], v[44:47], v[24:27], v[148:151]\n" \
    "ds_read_b64_tr_b16 v[52:53], v3 offset:27648\n" \
    "ds_read_b64_tr_b16 v[54:55], v3 offset:28672\n" \
    "v_mfma_f32_16x16x32_bf16 v[152:155], v[48:51], v[24:27], v[152:155]\n" \
    "ds_read_b64_tr_b16 v[56:57], v4 offset:27648\n" \
    "ds_read_b64_tr_b16 v[58:59], v4 offset:28672\n" \
    "s_waitcnt lgkmcnt(10)\n" \
    "v_mfma_f32_16x16x32_bf16 v[156:159], v[32:35], v[28:31], v[156:159]\n" \
    "ds_read_b64_tr_b16 v[60:61], v5 offset:27648\n" \
    "ds_read_b64_tr_b16 v[62:63], v5 offset:28672\n" \
    "v_mfma_f32_16x16x32_bf16 v[160:163], v[36:39], v[28:31], v[160:163]\n" \
    "ds_read_b64_tr_b16 v[64:65], v6 offset:27648\n" \
    "ds_read_b64_tr_b16 v[66:67], v6 offset:28672\n" \
    "v_mfma_f32_16x16x32_bf16 v[164:167], v[40:43], v[28:31], v[164:167]\n" \
    "v_mfma_f32_16x16x32_bf16 v[168:171], v[44:47], v[28:31], v[168:171]\n" \
    "v_mfma_f32_16x16x32_bf16 v[172:175], v[48:51], v[28:31], v[172:175]\n" \
    "s_waitcnt lgkmcnt(0)\n" \
    "v_mfma_f32_16x16x32_bf16 v[96:99], v[68:71], v[52:55], v[96:99]\n" \
    "v_mfma_f32_16x16x32_bf16 v[100:103], v[72:75], v[52:55], v[100:103]\n" \
    "v_mfma_f32_16x16x32_bf16 v[104:107], v[76:79], v[52:55], v[104:107]\n" \
    "v_mfma_f32_16x16x32_bf16 v[108:111], v[80:83], v[52:55], v[108:111]\n" \
    "v_mfma_f32_16x16x32_bf16 v[112:115], v[84:87], v[52:55], v[112:115]\n" \
    "v_mfma_f32_16x16x32_bf16 v[116:119], v[68:71], v[56:59], v[116:119]\n" \
    "v_mfma_f32_16x16x32_bf16 v[120:123], v[72:75], v[56:59], v[120:123]\n" \
    "v_mfma_f32_16x16x32_bf16 v[124:127], v[76:79], v[56:59], v[124:127]\n" \
    "s_waitcnt vmcnt(9)\n" \
    "v_mfma_f32_16x16x32_bf16 v[128:131], v[80:83], v[56:59], v[128:131]\n" \
    "ds_read_b64_tr_b16 v[32:33], v7 offset:4096\n" \
    "ds_read_b64_tr_b16 v[34:35], v7 offset:5376\n" \
    "v_mfma_f32_16x16x32_bf16 v[132:135], v[84:87], v[56:59], v[132:135]\n" \
    "ds_read_b64_tr_b16 v[36:37], v7 offset:4128\n" \
    "ds_read_b64_tr_b16 v[38:39], v7 offset:5408\n" \
    "v_mfma_f32_16x16x32_bf16 v[136:139], v[68:71], v[60:63], v[136:139]\n" \
    "ds_read_b64_tr_b16 v[40:41], v7 offset:4160\n" \
    "ds_read_b64_tr_b16 v[42:43], v7 offset:5440\n" \
    "v_mfma_f32_16x16x32_bf16 v[140:143], v[72:75], v[60:63], v[140:143]\n" \
    "ds_read_b64_tr_b16 v[44:45], v7 offset:4192\n" \
    "ds_read_b64_tr_b16 v[46:47], v7 offset:5472\n" \
    "v_mfma_f32_16x16x32_bf16 v[144:147], v[76:79], v[60:63], v[144:147]\n" \
    "ds_read_b64_tr_b16 v[48:49], v7 offset:4224\n" \
    "ds_read_b64_tr_b16 v[50:51], v7 offset:5504\n" \
    "v_mfma_f32_16x16x32_bf16 v[148:151], v[80:83], v[60:63], v[148:151]\n" \
    "ds_read_b64_tr_b16 v[16:17], v3 offset:0\n" \
    "ds_read_b64_tr_b16 v[18:19], v3 offset:1024\n" \
    "v_mfma_f32_16x16x32_bf16 v[152:155], v[84:87], v[60:63], v[152:155]\n" \
    "ds_read_b64_tr_b16 v[20:21], v4 offset:0\n" \
    "ds_read_b64_tr_b16 v[22:23], v4 offset:1024\n" \
    "s_waitcnt lgkmcnt(10)\n" \
    "v_mfma_f32_16x16x32_bf16 v[156:159], v[68:71], v[64:67], v[156:159]\n" \
    "ds_read_b64_tr_b16 v[24:25], v5 offset:0\n" \
    "ds_read_b64_tr_b16 v[26:27], v5 offset:1024\n" \
    "v_mfma_f32_16x16x32_bf16 v[160:163], v[72:75], v[64:67], v[160:163]\n" \
    "ds_read_b64_tr_b16 v[28:29], v6 offset:0\n" \
    "ds_read_b64_tr_b16 v[30:31], v6 offset:1024\n" \
    "v_mfma_f32_16x16x32_bf16 v[164:167], v[76:79], v[64:67], v[164:167]\n" \
    "v_mfma_f32_16x16x32_bf16 v[168:171], v[80:83], v[64:67], v[168:171]\n" \
    "v_mfma_f32_16x16x32_bf16 v[172:175], v[84:87], v[64:67], v[172:175]\n" \
    "s_waitcnt lgkmcnt(0)\n" \
    "v_mfma_f32_16x16x32_bf16 v[96:99], v[32:35], v[16:19], v[96:99]\n" \
    "v_mfma_f32_16x16x32_bf16 v[100:103], v[36:39], v[16:19], v[100:103]\n" \
    "v_mfma_f32_16x16x32_bf16 v[104:107], v[40:43], v[16:19], v[104:107]\n" \
    "v_mfma_f32_16x16x32_bf16 v[108:111], v[44:47], v[16:19], v[108:111]\n" \
    "v_mfma_f32_16x16x32_bf16 v[112:115], v[48:51], v[16:19], v[112:115]\n" \
    "v_mfma_f32_16x16x32_bf16 v[116:119], v[32:35], v[20:23], v[116:119]\n" \
    "v_mfma_f32_16x16x32_bf16 v[120:123], v[36:39], v[20:23], v[120:123]\n" \
    "v_mfma_f32_16x16x32_bf16 v[124:127], v[40:43], v[20:23], v[124:127]\n" \
    "s_waitcnt vmcnt(0)\n" \
    "v_mfma_f32_16x16x32_bf16 v[128:131], v[44:47], v[20:23], v[128:131]\n" \
    "ds_read_b64_tr_b16 v[68:69], v7 offset:13312\n" \
    "ds_read_b64_tr_b16 v[70:71], v7 offset:14592\n" \
    "v_mfma_f32_16x16x32_bf16 v[132:135], v[48:51], v[20:23], v[132:135]\n" \
    "ds_read_b64_tr_b16 v[72:73], v7 offset:13344\n" \
    "ds_read_b64_tr_b16 v[74:75], v7 offset:14624\n" \
    "v_mfma_f32_16x16x32_bf16 v[136:139], v[32:35], v[24:27], v[136:139]\n" \
    "ds_read_b64_tr_b16 v[76:77], v7 offset:13376\n" \
    "ds_read_b64_tr_b16 v[78:79], v7 offset:14656\n" \
    "v_mfma_f32_16x16x32_bf16 v[140:143], v[36:39], v[24:27], v[140:143]\n" \
    "ds_read_b64_tr_b16 v[80:81], v7 offset:13408\n" \
    "ds_read_b64_tr_b16 v[82:83], v7 offset:14688\n" \
    "v_mfma_f32_16x16x32_bf16 v[144:147], v[40:43], v[24:27], v[144:147]\n" \
    "ds_read_b64_tr_b16 v[84:85], v7 offset:13440\n" \
    "ds_read_b64_tr_b16 v[86:87], v7 offset:14720\n" \
    "v_mfma_f32_16x16x32_bf16 v[148:151], v[44:47], v[24:27], v[148:151]\n" \
    "ds_read_b64_tr_b16 v[52:53], v3 offset:9216\n" \
    "ds_read_b64_tr_b16 v[54:55], v3 offset:10240\n" \
    "v_mfma_f32_16x16x32_bf16 v[152:155], v[48:51], v[24:27], v[152:155]\n" \
    "ds_read_b64_tr_b16 v[56:57], v4 offset:9216\n" \
    "ds_read_b64_tr_b16 v[58:59], v4 offset:10240\n" \
    "s_waitcnt lgkmcnt(10)\n" \
    "v_mfma_f32_16x16x32_bf16 v[156:159], v[32:35], v[28:31], v[156:159]\n" \
    "ds_read_b64_tr_b16 v[60:61], v5 offset:9216\n" \
    "ds_read_b64_tr_b16 v[62:63], v5 offset:10240\n" \
    "v_mfma_f32_16x16x32_bf16 v[160:163], v[36:39], v[28:31], v[160:163]\n" \
    "ds_read_b64_tr_b16 v[64:65], v6 offset:9216\n" \
    "ds_read_b64_tr_b16 v[66:67], v6 offset:10240\n" \
    "v_mfma_f32_16x16x32_bf16 v[164:167], v[40:43], v[28:31], v[164:167]\n" \
    "v_mfma_f32_16x16x32_bf16 v[168:171], v[44:47], v[28:31], v[168:171]\n" \
    "v_mfma_f32_16x16x32_bf16 v[172:175], v[48:51], v[28:31], v[172:175]\n" \
    "s_waitcnt lgkmcnt(0)\n" \
    "v_mfma_f32_16x16x32_bf16 v[96:99], v[68:71], v[52:55], v[96:99]\n" \
    "v_mfma_f32_16x16x32_bf16 v[100:103], v[72:75], v[52:55], v[100:103]\n" \
    "v_mfma_f32_16x16x32_bf16 v[104:107], v[76:79], v[52:55], v[104:107]\n" \
    "v_mfma_f32_16x16x32_bf16 v[108:111], v[80:83], v[52:55], v[108:111]\n" \
    "v_mfma_f32_16x16x32_bf16 v[112:115], v[84:87], v[52:55], v[112:115]\n" \
    "v_mfma_f32_16x16x32_bf16 v[116:119], v[68:71], v[56:59], v[116:119]\n" \
    "v_mfma_f32_16x16x32_bf16 v[120:123], v[72:75], v[56:59], v[120:123]\n" \
    "v_mfma_f32_16x16x32_bf16 v[124:127], v[76:79], v[56:59], v[124:127]\n" \
    "v_mfma_f32_16x16x32_bf16 v[128:131], v[80:83], v[56:59], v[128:131]\n" \
    "v_mfma_f32_16x16x32_bf16 v[132:135], v[84:87], v[56:59], v[132:135]\n" \
    "v_mfma_f32_16x16x32_bf16 v[136:139], v[68:71], v[60:63], v[136:139]\n" \
    "v_mfma_f32_16x16x32_bf16 v[140:143], v[72:75], v[60:63], v[140:143]\n" \
    "v_mfma_f32_16x16x32_bf16 v[144:147], v[76:79], v[60:63], v[144:147]\n" \
    "v_mfma_f32_16x16x32_bf16 v[148:151], v[80:83], v[60:63], v[148:151]\n" \
    "v_mfma_f32_16x16x32_bf16 v[152:155], v[84:87], v[60:63], v[152:155]\n" \
    "v_mfma_f32_16x16x32_bf16 v[156:159], v[68:71], v[64:67], v[156:159]\n" \
    "v_mfma_f32_16x16x32_bf16 v[160:163], v[72:75], v[64:67], v[160:163]\n" \
    "v_mfma_f32_16x16x32_bf16 v[164:167], v[76:79], v[64:67], v[164:167]\n" \
    "v_mfma_f32_16x16x32_bf16 v[168:171], v[80:83], v[64:67], v[168:171]\n" \
    "v_mfma_f32_16x16x32_bf16 v[172:175], v[84:87], v[64:67], v[172:175]\n" \
    "s_branch GEND%=\n" \
    "GEND%=:\n" \
    "s_barrier\n" \
    "s_mul_i32 s50, s40, 20480\n" \
    "v_add_u32 v14, s50, v2\n" \
    "s_nop 4\n" \
    "ds_write_b128 v14, v[96:99] offset:0\n" \
    "ds_write_b128 v14, v[100:103] offset:1024\n" \
    "ds_write_b128 v14, v[104:107] offset:2048\n" \
    "ds_write_b128 v14, v[108:111] offset:3072\n" \
    "ds_write_b128 v14, v[112:115] offset:4096\n" \
    "ds_write_b128 v14, v[116:119] offset:5120\n" \
    "ds_write_b128 v14, v[120:123] offset:6144\n" \
    "ds_write_b128 v14, v[124:127] offset:7168\n" \
    "s_waitcnt lgkmcnt(0)\n" \
    "ds_write_b128 v14, v[128:131] offset:8192\n" \
    "ds_write_b128 v14, v[132:135] offset:9216\n" \
    "ds_write_b128 v14, v[136:139] offset:10240\n" \
    "ds_write_b128 v14, v[140:143] offset:11264\n" \
    "ds_write_b128 v14, v[144:147] offset:12288\n" \
    "ds_write_b128 v14, v[148:151] offset:13312\n" \
    "ds_write_b128 v14, v[152:155] offset:14336\n" \
    "ds_write_b128 v14, v[156:159] offset:15360\n" \
    "s_waitcnt lgkmcnt(0)\n" \
    "ds_write_b128 v14, v[160:163] offset:16384\n" \
    "ds_write_b128 v14, v[164:167] offset:17408\n" \
    "ds_write_b128 v14, v[168:171] offset:18432\n" \
    "ds_write_b128 v14, v[172:175] offset:19456\n" \
    "s_waitcnt lgkmcnt(0)\n" \
    "s_barrier\n" \
    ""
#define CLOB_V192 "v0", "v1", "v2", "v3", "v4", "v5", "v6", "v7", "v8", "v9", "v10", "v11", "v12", "v13", "v14", "v15", "v16", "v17", "v18", "v19", "v20", "v21", "v22", "v23", "v24", "v25", "v26", "v27", "v28", "v29", "v30", "v31", "v32", "v33", "v34", "v35", "v36", "v37", "v38", "v39", "v40", "v41", "v42", "v43", "v44", "v45", "v46", "v47", "v48", "v49", "v50", "v51", "v52", "v53", "v54", "v55", "v56", "v57", "v58", "v59", "v60", "v61", "v62", "v63", "v64", "v65", "v66", "v67", "v68", "v69", "v70", "v71", "v72", "v73", "v74", "v75", "v76", "v77", "v78", "v79", "v80", "v81", "v82", "v83", "v84", "v85", "v86", "v87", "v88", "v89", "v90", "v91", "v92", "v93", "v94", "v95", "v96", "v97", "v98", "v99", "v100", "v101", "v102", "v103", "v104", "v105", "v106", "v107", "v108", "v109", "v110", "v111", "v112", "v113", "v114", "v115", "v116", "v117", "v118", "v119", "v120", "v121", "v122", "v123", "v124", "v125", "v126", "v127", "v128", "v129", "v130", "v131", "v132", "v133", "v134", "v135", "v136", "v137", "v138", "v139", "v140", "v141", "v142", "v143", "v144", "v145", "v146", "v147", "v148", "v149", "v150", "v151", "v152", "v153", "v154", "v155", "v156", "v157", "v158", "v159", "v160", "v161", "v162", "v163", "v164", "v165", "v166", "v167", "v168", "v169", "v170", "v171", "v172", "v173", "v174", "v175", "v176", "v177", "v178", "v179", "v180", "v181", "v182", "v183", "v184", "v185", "v186", "v187", "v188", "v189", "v190", "v191"
#define CLOB_A160 "a0", "a1", "a2", "a3", "a4", "a5", "a6", "a7", "a8", "a9", "a10", "a11", "a12", "a13", "a14", "a15", "a16", "a17", "a18", "a19", "a20", "a21", "a22", "a23", "a24", "a25", "a26", "a27", "a28", "a29", "a30", "a31", "a32", "a33", "a34", "a35", "a36", "a37", "a38", "a39", "a40", "a41", "a42", "a43", "a44", "a45", "a46", "a47", "a48", "a49", "a50", "a51", "a52", "a53", "a54", "a55", "a56", "a57", "a58", "a59", "a60", "a61", "a62", "a63", "a64", "a65", "a66", "a67", "a68", "a69", "a70", "a71", "a72", "a73", "a74", "a75", "a76", "a77", "a78", "a79", "a80", "a81", "a82", "a83", "a84", "a85", "a86", "a87", "a88", "a89", "a90", "a91", "a92", "a93", "a94", "a95", "a96", "a97", "a98", "a99", "a100", "a101", "a102", "a103", "a104", "a105", "a106", "a107", "a108", "a109", "a110", "a111", "a112", "a113", "a114", "a115", "a116", "a117", "a118", "a119", "a120", "a121", "a122", "a123", "a124", "a125", "a126", "a127", "a128", "a129", "a130", "a131", "a132", "a133", "a134", "a135", "a136", "a137", "a138", "a139", "a140", "a141", "a142", "a143", "a144", "a145", "a146", "a147", "a148", "a149", "a150", "a151", "a152", "a153", "a154", "a155", "a156", "a157", "a158", "a159"
#define CLOB_SW "s40", "s41", "s42", "s43", "s44", "s45", "s46", "s47", "s48", "s49", "s50", "s51", "s52", "s53", "s54", "s55"
typedef unsigned short u16;
typedef unsigned v4u __attribute__((ext_vector_type(4)));
constexpr unsigned SENT = 0xFFBFFFBFu;
constexpr size_t MB = 1u << 20;
constexpr size_t OFF_RING = 0, OFF_QKV = 4 * MB, OFF_CTL = 68 * MB;
struct RecArgs { const u16 *Wq, *Wk, *Wv, *Wo, *x; char* ws; char* dbg; int layer, D12, Cinit, D1, workers, pad; };
__device__ void worker(const RecArgs& a, int xb, int i, int w, char* smem);

__global__ void __launch_bounds__(256, 1) rec_kernel(RecArgs a) {
    extern __shared__ __attribute__((aligned(16))) char smem[];
    const int b = blockIdx.x, x = b & 7, i = b >> 3;
    const int w = __builtin_amdgcn_readfirstlane(threadIdx.x >> 6);
    if (x >= 4) { if (a.workers) worker(a, x - 4, i, w, smem); return; }
    const int ai = (x & 1) * 64 + i * 2 + (x >> 1);
    const int rh = (ai - 32) | ((ai >= 64 ? ai - 64 : 255) << 8);
    const unsigned* ctl = (const unsigned*)(a.ws + OFF_CTL);
    const unsigned long long pb = __builtin_amdgcn_ballot_w64((ctl[(threadIdx.x & 63) < 34 ? (threadIdx.x & 63) : 0] & 1u) != 0);
    const unsigned pring = (unsigned)(pb >> a.layer) & 1u, pmask = (unsigned)(pb >> 2);
    const unsigned q14 = __builtin_amdgcn_readfirstlane((ctl[a.layer] & 2u) << 13);
    char* ring = a.ws + OFF_RING + (size_t)a.layer * 2 * MB + (pring << 13);
    if (ai < 32) {
        asm volatile(HEAD_ASM "s_endpgm\n" :: "s"(a.Wq), "s"(a.Wk), "s"(ring), "s"(a.ws + OFF_QKV), "s"(pmask), "s"(ai), "s"(w), "s"(a.D12), "s"(a.Cinit), "s"(a.dbg), "s"(q14), "s"(a.layer + 1)
                     : "memory", "vcc", "scc", CLOB_S, CLOB_V, CLOB_A);
        __builtin_unreachable();
    } else {
        asm volatile(RED_ASM "s_endpgm\n" :: "s"(a.Wo), "s"(a.Wv), "s"(ring), "s"(a.ws + OFF_QKV), "s"(pmask), "s"(rh), "s"(w), "s"(a.D1), "s"(a.D12 - a.D1), "s"(q14), "s"(a.dbg), "s"(a.layer + 1)
                     : "memory", "vcc", "scc", CLOB_S, CLOB_V, CLOB_A);
        __builtin_unreachable();
    }
}
__device__ __forceinline__ unsigned probe_lat(char* p) {
    unsigned long long t0, t1;
    asm volatile("s_waitcnt vmcnt(0) lgkmcnt(0)\n\ts_memtime %0\n\ts_waitcnt lgkmcnt(0)\n\tglobal_store_dword %2, %3, off sc1\n\ts_waitcnt vmcnt(0)\n\ts_memtime %1\n\ts_waitcnt lgkmcnt(0)"
                 : "=&s"(t0), "=&s"(t1) : "v"(p), "v"(0) : "memory");
    return (unsigned)(t1 - t0);
}
__device__ __forceinline__ void probe_body(char* ws, int b, int force = -1) {
    if (b & 7) return;
    const int pg = (b >> 3) * 4 + (threadIdx.x >> 6);
    if ((threadIdx.x & 63) != 0 || pg >= 34) return;
    char* p = ws + (size_t)pg * 2 * MB + 0x1f0000;
    unsigned l0 = ~0u, l1 = ~0u;
    probe_lat(p);
    for (int r = 0; r < 3; ++r) { l0 = min(l0, probe_lat(p)); l1 = min(l1, probe_lat(p + 8192)); }
    unsigned res = l1 < l0 ? 1u : 0u;
    if (pg < 2) {
        unsigned l2 = ~0u; l0 = ~0u; char* q = p + (res << 13);
        for (int r = 0; r < 4; ++r) { l0 = min(l0, probe_lat(q) + probe_lat(q + 4096)); l2 = min(l2, probe_lat(q + 16384) + probe_lat(q + 16384 + 4096)); }
        res |= (l2 < l0 ? 0u : 2u); l1 = l2;
        if (force >= 0) res = (res & 1u) | (force << 1);
    }
    ((unsigned*)(ws + OFF_CTL))[pg] = res;
    ((unsigned*)(ws + OFF_CTL))[64 + pg] = l0 | (l1 << 16);
}
__device__ __forceinline__ void st16_sc1(char* p, v4u v) { asm volatile("global_store_dwordx4 %0, %1, off sc1\n\ts_nop 1" :: "v"(p), "v"(v) : "memory"); }
__device__ __forceinline__ void ring_body(char* ws, const u16* mem0, int b) {
    const int i = b * 256 + threadIdx.x;
    const int layer = i >> 17, u = i & 131071, blk = u >> 9, in = u & 511;
    char* p = ws + OFF_RING + (size_t)layer * 2 * MB + (size_t)u * 16;
    v4u v = {SENT, SENT, SENT, SENT};
    if ((blk >= 96 && blk < 128) || blk >= 192) v = (v4u){0u, 0u, 0u, 0u};
    else if (blk >= 64 && blk < 72 && (blk < 68 || in < 128)) {
        const int j = ((blk - 64) >> 2) * 8 + (in >> 6), l = in & 63, g = l >> 4, r = l & 15, row = 2 * (r >> 2) + (r & 1), col = 64 * j + 16 * g + 8 * ((r >> 1) & 1);
        v = *(const v4u*)(mem0 + row * 640 + col);
    }
    st16_sc1(p, v);
}

typedef float f4 __attribute__((ext_vector_type(4)));
typedef float f16v __attribute__((ext_vector_type(16)));
typedef __bf16 bf8 __attribute__((ext_vector_type(8)));
typedef unsigned v2u __attribute__((ext_vector_type(2)));
constexpr size_t OFF_FLAG = OFF_CTL + 65536;
constexpr size_t OFF_DONE = OFF_CTL + 131072;
constexpr size_t OFF_FA = OFF_CTL + 163840;
constexpr size_t OFF_FO = OFF_CTL + 196608;
constexpr size_t CTL_ZERO0 = OFF_FLAG, CTL_ZERO1 = OFF_CTL + 262144;
#define GAS __attribute__((address_space(1)))
typedef __amdgpu_buffer_rsrc_t rsrc_t;
__device__ __forceinline__ rsrc_t ws_rsrc(char* ws) { return __builtin_amdgcn_make_buffer_rsrc(ws, 0, 254 << 20, 0x00020000); }
template <bool SC> __device__ __forceinline__ v4u ldw16(rsrc_t r, unsigned o) { return __builtin_amdgcn_raw_buffer_load_b128(r, o, 0, SC ? 16 : 0); }
template <bool SC> __device__ __forceinline__ unsigned ldw4(rsrc_t r, unsigned o) { return __builtin_amdgcn_raw_buffer_load_b32(r, o, 0, SC ? 16 : 0); }
template <bool SC> __device__ __forceinline__ void stw16(rsrc_t r, unsigned o, v4u v) { __builtin_amdgcn_raw_buffer_store_b128(v, r, o, 0, SC ? 16 : 0); }
__device__ __forceinline__ void wait_ge(const char* p, unsigned mask, unsigned want) {
    for (unsigned sp = 0; sp < (1u << 17); ++sp) {
        const unsigned v = __builtin_amdgcn_readfirstlane(__hip_atomic_load((const GAS unsigned*)p, __ATOMIC_RELAXED, __HIP_MEMORY_SCOPE_AGENT));
        if ((v & mask) >= want) break;
        __builtin_amdgcn_s_sleep(4);
    }
    asm volatile("" ::: "memory");
}
__device__ __forceinline__ void signal_add(char* p) {
    asm volatile("s_waitcnt vmcnt(0)" ::: "memory");
    if ((threadIdx.x & 63) == 0) __hip_atomic_fetch_add((GAS unsigned*)p, 1u, __ATOMIC_RELAXED, __HIP_MEMORY_SCOPE_AGENT);
}
constexpr size_t OFF_HF = 70 * MB, OFF_OT = 74 * MB, OFF_SLAB = 92 * MB, OFF_X1 = 136 * MB, OFF_X2 = 140 * MB, OFF_HB = 144 * MB, OFF_U = 148 * MB;
__device__ __forceinline__ float bflo(unsigned u) { return __uint_as_float(u << 16); }
__device__ __forceinline__ float bfhi(unsigned u) { return __uint_as_float(u & 0xffff0000u); }
__device__ __forceinline__ unsigned pk(float a, float b) { typedef __bf16 b2 __attribute__((ext_vector_type(2))); b2 r = {(__bf16)a, (__bf16)b}; return __builtin_bit_cast(unsigned, r); }
__device__ __forceinline__ float rbf(float a) { return (float)(__bf16)a; }
__device__ __forceinline__ unsigned qkv_par(const char* ws, int seg) { return ((const unsigned*)(ws + OFF_CTL))[2 + (seg >> 1)] & 1u; }

__device__ __forceinline__ float row_sum(float v) {
    v += __builtin_bit_cast(float, __builtin_amdgcn_update_dpp(0, __builtin_bit_cast(int, v), 0x121, 0xf, 0xf, true));
    v += __builtin_bit_cast(float, __builtin_amdgcn_update_dpp(0, __builtin_bit_cast(int, v), 0x122, 0xf, 0xf, true));
    v += __builtin_bit_cast(float, __builtin_amdgcn_update_dpp(0, __builtin_bit_cast(int, v), 0x124, 0xf, 0xf, true));
    v += __builtin_bit_cast(float, __builtin_amdgcn_update_dpp(0, __builtin_bit_cast(int, v), 0x128, 0xf, 0xf, true));
    return v;
}
__device__ __forceinline__ void xch32(float v, float& a, float& b) { a = v; b = v; asm volatile("s_nop 1\n\tv_permlane32_swap_b32 %0, %1" : "+v"(a), "+v"(b)); }
__device__ __forceinline__ void xch16(float v, float& a, float& b) { a = v; b = v; asm volatile("s_nop 1\n\tv_permlane16_swap_b32 %0, %1" : "+v"(a), "+v"(b)); }
__device__ __forceinline__ void ln_rows(const u16* x, char* hf, int tok0, int lane) {
    const int tok = tok0 + (lane >> 4), c = lane & 15;
    v4u r[5]; float f[40], s = 0.f;
#pragma unroll
    for (int i = 0; i < 5; ++i) r[i] = *(const v4u*)(x + (size_t)tok * 640 + (c + 16 * i) * 8);
#pragma unroll
    for (int i = 0; i < 5; ++i)
#pragma unroll
        for (int j = 0; j < 4; ++j) { f[8 * i + 2 * j] = bflo(r[i][j]); f[8 * i + 2 * j + 1] = bfhi(r[i][j]); s += f[8 * i + 2 * j] + f[8 * i + 2 * j + 1]; }
    s = row_sum(s);
    const float mean = s * (1.f / 640.f); float q = 0.f;
#pragma unroll
    for (int i = 0; i < 40; ++i) { f[i] -= mean; q += f[i] * f[i]; }
    q = row_sum(q);
    const float rs = rsqrtf(q * (1.f / 640.f) + 1e-5f);
#pragma unroll
    for (int i = 0; i < 5; ++i) { const int u = c + 16 * i; v4u o;
#pragma unroll
        for (int j = 0; j < 4; ++j) o[j] = pk(f[8 * i + 2 * j] * rs, f[8 * i + 2 * j + 1] * rs);
        *(v4u*)(hf + (size_t)(tok >> 5) * 40960 + (u >> 1) * 1024 + ((u & 1) * 32 + (tok & 31)) * 16) = o; }
}
__global__ void __launch_bounds__(256) ln_kernel(const u16* x, char* ws) { ln_rows(x, ws + OFF_HF, blockIdx.x * 16 + (threadIdx.x >> 6) * 4, threadIdx.x & 63); }

__device__ __forceinline__ void qkv_task(const u16* Wq, const u16* Wk, const u16* Wv, char* ws, int tile, int h, int w, unsigned tag, int m0, int mc) {
    const int seg = tile * 4 + w;
    const unsigned par = __builtin_amdgcn_readfirstlane(qkv_par(ws, seg));
    char* out = ws + OFF_QKV + (size_t)seg * MB + h * 32768 + (par << 13);
    asm volatile(QKV_ASM :: "s"(Wk + h * 128), "s"(Wv + h * 128), "s"(Wq + h * 128), "s"(ws + OFF_HF + (size_t)seg * 40960), "s"(out), "s"(w),
                 "s"(ws + OFF_FLAG + (seg * 32 + h) * 16), "s"(tag), "s"(m0), "s"(mc) : "memory", "vcc", "scc", CLOB_SW, CLOB_V192, CLOB_A160);
}
__global__ void __launch_bounds__(256, 1) qkv_kernel(const u16* Wq, const u16* Wk, const u16* Wv, char* ws) {
    const int b = blockIdx.x, x = b & 7, i = b >> 3, w = __builtin_amdgcn_readfirstlane(threadIdx.x >> 6);
    for (int r = 0; r < 2; ++r) qkv_task(Wq, Wk, Wv, ws, (i >> 2) + 8 * r, x * 4 + (i & 3), w, 1u, 0, 3);
}

__device__ __forceinline__ void attn_task(char* ws, int seg, int h, int lane, const char* mflag, unsigned mmask, unsigned mwant) {
    const unsigned par = qkv_par(ws, seg);
    const rsrc_t rs = ws_rsrc(ws);
    const unsigned kb = OFF_QKV + seg * MB + h * 32768 + (par << 13), qb = kb ^ 8192u;
    const int hi = lane >> 5, c = lane & 31;
    v4u q[8], k[8], km[8], vv[8]; unsigned vm[4][2];
    const int ko = ((c >> 3) & 1) * 1024 + hi * 256 + (8 * (c >> 4) + (c & 7)) * 16, vo = (c >> 4) * 1024 + hi * 256 + (c & 15) * 16;
#pragma unroll
    for (int i = 0; i < 8; ++i) { q[i] = ldw16<true>(rs, qb + vo + (i >> 1) * 2048 + (i & 1) * 512); k[i] = ldw16<true>(rs, kb + ko + (i >> 1) * 2048 + (i & 1) * 512); }
#pragma unroll
    for (int i = 0; i < 8; ++i) vv[i] = ldw16<true>(rs, kb + 16384 + vo + (i >> 1) * 2048 + (i & 1) * 512);
    __builtin_amdgcn_sched_barrier(0);
    f16v sx = {}, sm = {};
#pragma unroll
    for (int i = 0; i < 8; ++i) sx = __builtin_amdgcn_mfma_f32_32x32x16_bf16(__builtin_bit_cast(bf8, k[i]), __builtin_bit_cast(bf8, q[i]), sx, 0, 0, 0);
    asm volatile("" : "+v"(sx));
    wait_ge(mflag, mmask, mwant);
#pragma unroll
    for (int i = 0; i < 8; ++i) km[i] = ldw16<true>(rs, qb + 16384 + ((i * 2 + hi) * 8 + (c & 7)) * 16);
#pragma unroll
    for (int t = 0; t < 4; ++t) { const unsigned p = qb + 16384 + 2048 + t * 512 + hi * 256 + (c & 15) * 8 + (c >> 4) * 4; vm[t][0] = ldw4<true>(rs, p); vm[t][1] = ldw4<true>(rs, p + 128); }
    __builtin_amdgcn_sched_barrier(0);
#pragma unroll
    for (int i = 0; i < 8; ++i) {
        sm = __builtin_amdgcn_mfma_f32_32x32x16_bf16(__builtin_bit_cast(bf8, km[i]), __builtin_bit_cast(bf8, q[i]), sm, 0, 0, 0);
    }
    float m = fmaxf(fmaxf(sm[0], sm[1]), fmaxf(sm[2], sm[3]));
#pragma unroll
    for (int r = 0; r < 16; ++r) { const int key = (r & 3) + 8 * (r >> 2) + 4 * hi; sx[r] = key <= c ? sx[r] : -INFINITY; m = fmaxf(m, sx[r]); }
    { float a, b; xch32(m, a, b); m = fmaxf(a, b); }
    const float C = 0.12751743082459868f, mc = m * C; float l = 0.f; float p[20];
#pragma unroll
    for (int r = 0; r < 16; ++r) { p[r] = __builtin_amdgcn_exp2f(sx[r] * C - mc); l += p[r]; }
#pragma unroll
    for (int r = 0; r < 4; ++r) { p[16 + r] = __builtin_amdgcn_exp2f(sm[r] * C - mc); l += p[16 + r]; }
    { float a, b; xch32(l, a, b); l = a + b; }
    const float inv = 1.f / l;
    v4u p0, p1, pm;
#pragma unroll
    for (int j = 0; j < 4; ++j) { p0[j] = pk(p[2 * j] * inv, p[2 * j + 1] * inv); p1[j] = pk(p[8 + 2 * j] * inv, p[9 + 2 * j] * inv); }
    pm = (v4u){pk(p[16] * inv, p[17] * inv), pk(p[18] * inv, p[19] * inv), 0u, 0u};
    const unsigned ob = OFF_OT + (seg * 32 + h) * 8192 + lane * 16;
#pragma unroll
    for (int t = 0; t < 4; ++t) {
        f16v o = {};
        o = __builtin_amdgcn_mfma_f32_32x32x16_bf16(__builtin_bit_cast(bf8, vv[2 * t]), __builtin_bit_cast(bf8, p0), o, 0, 0, 0);
        o = __builtin_amdgcn_mfma_f32_32x32x16_bf16(__builtin_bit_cast(bf8, vv[2 * t + 1]), __builtin_bit_cast(bf8, p1), o, 0, 0, 0);
        const v4u va = {vm[t][0], vm[t][1], 0u, 0u};
        o = __builtin_amdgcn_mfma_f32_32x32x16_bf16(__builtin_bit_cast(bf8, va), __builtin_bit_cast(bf8, pm), o, 0, 0, 0);
#pragma unroll
        for (int s = 0; s < 2; ++s) { v4u r;
#pragma unroll
            for (int j = 0; j < 4; ++j) r[j] = pk(o[8 * s + 2 * j], o[8 * s + 2 * j + 1]);
            stw16<true>(rs, ob + (2 * t + s) * 1024, r); }
    }
}

__device__ __forceinline__ void outp_task(const u16* Wo, char* ws, int tile, int slice, int h0, int nh, int kq, int w, const char* cnt, unsigned want) {
    const int seg = tile * 4 + w;
    asm volatile(OUTP_ASM :: "s"(Wo + (size_t)h0 * 128 * 640 + slice * 128), "s"(ws + OFF_OT + ((size_t)seg * 32 + h0) * 8192),
                 "s"(ws + OFF_SLAB + ((size_t)kq * 2048 + seg * 32) * 2560 + slice * 512), "s"(w), "s"(nh), "s"(cnt), "s"(want) : "memory", "vcc", "scc", CLOB_SW, CLOB_V192);
}
__device__ __forceinline__ float half_sum(float v) { float a, b; xch16(row_sum(v), a, b); return a + b; }
template <int NKQ>
__device__ __forceinline__ void ln2_rows(const u16* x, char* ws, int tok0, int lane) {
    const int tok = tok0 + (lane >> 5), c = lane & 31;
    const rsrc_t rs = ws_rsrc(ws);
    v2u r[5]; v4u p[NKQ][5]; float f[20];
#pragma unroll
    for (int i = 0; i < 5; ++i) r[i] = *(const v2u*)(x + (size_t)tok * 640 + (c + 32 * i) * 4);
#pragma unroll
    for (int k = 0; k < NKQ; ++k)
#pragma unroll
        for (int i = 0; i < 5; ++i) p[k][i] = ldw16<true>(rs, OFF_SLAB + (k * 2048 + tok) * 2560 + (c + 32 * i) * 16);
    __builtin_amdgcn_sched_barrier(0);
    unsigned off[5];
#pragma unroll
    for (int i = 0; i < 5; ++i) { f4 a = __builtin_bit_cast(f4, p[0][i]);
#pragma unroll
        for (int k = 1; k < NKQ; ++k) a += __builtin_bit_cast(f4, p[k][i]);
        const v2u y = {pk(bflo(r[i][0]) + a[0], bfhi(r[i][0]) + a[1]), pk(bflo(r[i][1]) + a[2], bfhi(r[i][1]) + a[3])};
        const int u = c + 32 * i; off[i] = ((u / 20) * 2048 + tok) * 160 + (u % 20) * 8;
        *(v2u*)(ws + OFF_X1 + off[i]) = y;
        f[4 * i] = bflo(y[0]); f[4 * i + 1] = bfhi(y[0]); f[4 * i + 2] = bflo(y[1]); f[4 * i + 3] = bfhi(y[1]); }
#pragma unroll
    for (int pass = 0; pass < 2; ++pass) {
        float s = 0.f;
#pragma unroll
        for (int i = 0; i < 20; ++i) s += f[i];
        const float mean = half_sum(s) * (1.f / 640.f); float q = 0.f;
#pragma unroll
        for (int i = 0; i < 20; ++i) { f[i] -= mean; q += f[i] * f[i]; }
        const float rsd = rsqrtf(half_sum(q) * (1.f / 640.f) + 1e-5f);
#pragma unroll
        for (int i = 0; i < 20; ++i) f[i] *= rsd;
    }
#pragma unroll
    for (int i = 0; i < 5; ++i) *(v2u*)(ws + OFF_HB + off[i]) = (v2u){pk(f[4 * i], f[4 * i + 1]), pk(f[4 * i + 2], f[4 * i + 3])};
}

template <int MODE>
__global__ void __launch_bounds__(256, 1) gemmb_kernel(const u16* R, const u16* bias, char* ws, u16* out) {
    extern __shared__ __attribute__((aligned(16))) char smem[];
    const int b = blockIdx.x, w = __builtin_amdgcn_readfirstlane(threadIdx.x >> 6), lane = threadIdx.x & 63;
    const int x = b & 7, i = b >> 3, pan = (x & 1) * 4 + (i & 3), rt = (i >> 2) * 4 + (x >> 1);
    if (MODE == 0 && rt >= 27) return;
    constexpr int RS = MODE == 0 ? 1728 : 2048, K = MODE == 0 ? 2048 : 1728;
    const char* P = ws + (MODE == 0 ? OFF_HB : OFF_U) + (size_t)pan * K * 160;
    const int nk = MODE == 0 ? 16 : (w < 2 ? 14 : 13);
    asm volatile(GEMMB_ASM :: "s"(R + (size_t)w * 32 * RS + rt * 64), "s"(P + w * 5120), "s"(RS * 2), "s"(w), "s"(nk) : "memory", "vcc", "scc", CLOB_SW, CLOB_V192);
    const int g = lane >> 4, row = w * 16 + (lane & 15);
    const float bs = bflo(bias[rt * 64 + row]);
    char* img = smem + 81920;
#pragma unroll
    for (int nt = 0; nt < 5; ++nt) {
        const char* p = smem + (w * 5 + nt) * 1024 + lane * 16;
        f4 a = *(const f4*)p + *(const f4*)(p + 20480) + (*(const f4*)(p + 40960) + *(const f4*)(p + 61440));
#pragma unroll
        for (int j = 0; j < 4; ++j) { float t = rbf(rbf(a[j]) + bs);
            if (MODE == 0) { const float z = 0.7978845608028654f * (t + 0.044715f * t * t * t); const float ex = __builtin_amdgcn_exp2f(z * 2.885390081777927f); t = t * (1.f - 1.f / (ex + 1.f)); }
            a[j] = t; }
        *(v2u*)(img + row * 160 + nt * 32 + g * 8) = (v2u){pk(a[0], a[1]), pk(a[2], a[3])};
    }
    __syncthreads();
    for (int u = threadIdx.x; u < 640; u += 256) {
        v4u y = *(const v4u*)(img + u * 16);
        if (MODE == 0) *(v4u*)(ws + OFF_U + ((size_t)pan * 1728 + rt * 64) * 160 + u * 16) = y;
        else { const v4u xv = *(const v4u*)(ws + OFF_X1 + ((size_t)pan * 2048 + rt * 64) * 160 + u * 16); v4u o;
#pragma unroll
            for (int j = 0; j < 4; ++j) o[j] = pk(bflo(xv[j]) + bflo(y[j]), bfhi(xv[j]) + bfhi(y[j]));
            *(v4u*)(out + (size_t)(rt * 64 + u / 10) * 640 + pan * 80 + (u % 10) * 8) = o; }
    }
}

#define WST(n, k)
__device__ void worker(const RecArgs& a, int xb, int i, int w, char* smem) {
    char* ws = a.ws; const unsigned tag = a.layer + 1; const int h = xb * 8 + (i & 7), lane = threadIdx.x & 63;
    for (int r = 0; r < 5; ++r) {
        int tile, m0 = 0, mc = 3;
        if (r == 0) { tile = i >> 4; m0 = (i >> 3) & 1; mc = 1; }
        else if (r < 4) tile = 4 * r - 2 + (i >> 3);
        else if (i < 16) tile = 14 + (i >> 3);
        else { tile = (i >> 3) - 2; m0 = 2; mc = 1; }
        WST(r, 0); WST(r, 1);
        qkv_task(a.Wq, a.Wk, a.Wv, ws, tile, h, w, tag, m0, mc);
        WST(r, 2);
    }
    const unsigned pring = ((const unsigned*)(ws + OFF_CTL))[a.layer] & 1u;
    const char* plan = ws + OFF_RING + (size_t)a.layer * 2 * MB + (pring << 13) + 786432 + 4;
    const int wi = i * 4 + xb;
    constexpr int NA = 52, NO = 60, NL = 16;
    if (wi < NA) {
        for (int id = wi; id < 512; id += NA) {
            const int tile = id >> 5, k = id & 31, seg = tile * 4 + w;
            WST(5 + id / NA, 0);
            const char* f = ws + OFF_FLAG + (seg * 32 + k) * 16;
            wait_ge(f, ~0u, tag); wait_ge(f + 4, ~0u, tag); wait_ge(f + 8, ~0u, tag);
            WST(5 + id / NA, 1);
            attn_task(ws, seg, k, lane, seg < 62 ? plan + (seg + 2) * 128 : ws + OFF_DONE + (tag * 32 + k) * 128, seg < 62 ? 255u : ~0u, seg < 62 ? seg + 3 : 8u);
            signal_add(ws + OFF_FA + ((a.layer * 16 + tile) * 8 + (k >> 2)) * 128);
            WST(5 + id / NA, 2);
        }
    } else if (wi < NA + NO) {
        for (int id = wi - NA; id < 340; id += NO) {
            WST(5 + id / NO, 0); WST(5 + id / NO, 1);
            if (id < 300) {
                const int tile = id / 20, kq = id & 3, slice = (id % 20) >> 2;
                const char* c = ws + OFF_FA + ((a.layer * 16 + tile) * 8 + 2 * kq) * 128;
                wait_ge(c, ~0u, 16u);
                outp_task(a.Wo, ws, tile, slice, kq * 8, 8, kq, w, c + 128, 16u);
                signal_add(ws + OFF_FO + (a.layer * 64 + tile * 4 + w) * 128);
            } else {
                const int kq = (id - 300) & 7, slice = (id - 300) >> 3;
                outp_task(a.Wo, ws, 15, slice, kq * 4, 4, kq, w, ws + OFF_FA + ((a.layer * 16 + 15) * 8 + kq) * 128, 16u);
                signal_add(ws + OFF_FO + (a.layer * 64 + 60 + w) * 128);
            }
            WST(5 + id / NO, 2);
        }
    } else {
        for (int id = wi - NA - NO; id < 256; id += NL) {
            WST(id < 160 ? 5 : id / NL - 5, 0);
            wait_ge(ws + OFF_FO + (a.layer * 64 + (id >> 2)) * 128, ~0u, id < 240 ? 20u : 40u);
            WST(id < 160 ? 5 : id / NL - 5, 1);
            if (id < 240) ln2_rows<4>(a.x, ws, id * 8 + w * 2, lane); else ln2_rows<8>(a.x, ws, id * 8 + w * 2, lane);
            WST(id < 160 ? 5 : id / NL - 5, 2);
        }
    }
}
__global__ void __launch_bounds__(256) prologue_kernel(char* ws, const u16* x, const u16* mem0) {
    const int b = blockIdx.x;
    if (b < 256) { probe_body(ws, b); if (b < 128) ln_rows(x, ws + OFF_HF, b * 16 + (threadIdx.x >> 6) * 4, threadIdx.x & 63); }
    else if (b < 256 + 1024) ring_body(ws, mem0, b - 256);
    else st16_sc1(ws + CTL_ZERO0 + ((b - 1280) * 256 + threadIdx.x) * 16, (v4u){0u, 0u, 0u, 0u});
}
extern "C" void kernel_launch(void* const* d_in, const int* in_sizes, int n_in, void* d_out, int out_size, void* d_ws, size_t ws_size, hipStream_t stream) {
    const u16 *x = (const u16*)d_in[0], *mem0 = (const u16*)d_in[1], *Wq = (const u16*)d_in[2], *Wk = (const u16*)d_in[3], *Wv = (const u16*)d_in[4], *Wo = (const u16*)d_in[5];
    const u16 *w1 = (const u16*)d_in[8], *b1 = (const u16*)d_in[9], *w2 = (const u16*)d_in[10], *b2 = (const u16*)d_in[11];
    char* ws = (char*)(((uintptr_t)d_ws + 2 * MB - 1) & ~(uintptr_t)(2 * MB - 1));
    constexpr int LDS = 160 * 1024;
    (void)hipFuncSetAttribute((const void*)rec_kernel, hipFuncAttributeMaxDynamicSharedMemorySize, LDS);
    (void)hipFuncSetAttribute((const void*)gemmb_kernel<0>, hipFuncAttributeMaxDynamicSharedMemorySize, LDS);
    (void)hipFuncSetAttribute((const void*)gemmb_kernel<1>, hipFuncAttributeMaxDynamicSharedMemorySize, LDS);
    prologue_kernel<<<256 + 1024 + (CTL_ZERO1 - CTL_ZERO0) / 4096, 256, 0, stream>>>(ws, x, mem0);
    for (int layer = 0; layer < 2; ++layer) {
        const u16* xin = layer ? (const u16*)(ws + OFF_X2) : x;
        if (layer) ln_kernel<<<128, 256, 0, stream>>>(xin, ws);
        RecArgs a{Wq, Wk, Wv, Wo, xin, ws, ws + 200 * MB, layer, P_D12, P_LINIT, P_D1, 1, 0};
        rec_kernel<<<256, 256, LDS, stream>>>(a);
        gemmb_kernel<0><<<224, 256, LDS, stream>>>(w1, b1, ws, nullptr);
        gemmb_kernel<1><<<256, 256, LDS, stream>>>(w2, b2, ws, layer ? (u16*)d_out : (u16*)(ws + OFF_X2));
    }
}
```
